```python
import torch

def _softmax(x, axis=-1):
    return torch.softmax(x, dim=axis)
def _gelu(x):
    return torch.nn.functional.gelu(x, approximate="tanh")
def _silu(x):
    return torch.nn.functional.silu(x)
def _sigmoid(x):
    return torch.sigmoid(x)
def _layernorm(x, axis=-1, eps=1e-5):
    m = x.mean(dim=axis, keepdim=True)
    v = x.var(dim=axis, keepdim=True, unbiased=False)
    return (x - m) / torch.sqrt(v + eps)
def _scatter_add(tgt, idx, src, axis=0):
    return tgt.clone().index_add_(axis, idx, src)
def _segment_sum(src, seg, n):
    out = torch.zeros((n,) + src.shape[1:], dtype=src.dtype)
    return out.index_add_(0, seg, src)
def _pad(x, pw):
    flat = tuple(v for pair in reversed(pw) for v in pair)
    return torch.nn.functional.pad(x, flat)

kernel_name = 'recurrent_scan__ssm_conv_s4d__mixer_channel_mlp__prompt'

def setup_inputs():

    _w = (lambda a: (lambda t: t.to(torch.bfloat16) if t.is_floating_point() else t)(torch.as_tensor(a)))
    def _A_setup():
        Bsz, L, D, N = 1, 2048, 640, 128
        return {
            "u": _w((lambda *s: torch.randn(*s, dtype=torch.bfloat16) * 0.05)(Bsz, L, D)),
            "log_A_real": _w((lambda *s: torch.randn(*s, dtype=torch.bfloat16) * 3.2)(D, N) * 0.5),
            "A_imag": _w((lambda *s: torch.randn(*s, dtype=torch.bfloat16) * 0.2)(D, N) * 3.14),
            "C_real": _w((lambda *s: torch.randn(*s, dtype=torch.bfloat16) * 0.8)(D, N) / N ** 0.5),
            "C_imag": _w((lambda *s: torch.randn(*s, dtype=torch.bfloat16) * 0.8)(D, N) / N ** 0.5),
            "log_dt": _w((lambda *s: torch.randn(*s, dtype=torch.bfloat16) * 0.2)(D) * 0.5 - 4.0),
            "D_skip": _w((lambda *s: torch.randn(*s, dtype=torch.bfloat16) * 0.2)(D)),
        }
    _A_in = _A_setup()

    def _B_setup():
        B, S, D, F = 1, 2048, 640, 1728
        return {
            "x": _w((lambda *s: torch.randn(*s, dtype=torch.bfloat16) * 0.05)(B, S, D)),
            "w1": _w((lambda *s: torch.randn(*s, dtype=torch.bfloat16) * 1.6)(D, F) * (D ** -0.5)),
            "b1": _w((lambda *s: torch.randn(*s, dtype=torch.bfloat16) * 0.8)(F)),
            "w2": _w((lambda *s: torch.randn(*s, dtype=torch.bfloat16) * 0.1)(F, D) * (F ** -0.5)),
            "b2": _w((lambda *s: torch.randn(*s, dtype=torch.bfloat16) * 0.1)(D)),
        }
    _B_in = _B_setup()

    return {
        "A_u": _A_in["u"],
        "A_log_A_real": _A_in["log_A_real"],
        "A_A_imag": _A_in["A_imag"],
        "A_C_real": _A_in["C_real"],
        "A_C_imag": _A_in["C_imag"],
        "A_log_dt": _A_in["log_dt"],
        "A_D_skip": _A_in["D_skip"],
        "B_x": _B_in["x"],
        "B_w1": _B_in["w1"],
        "B_b1": _B_in["b1"],
        "B_w2": _B_in["w2"],
        "B_b2": _B_in["b2"],
    }

def reference(A_u, A_log_A_real, A_A_imag, A_C_real, A_C_imag, A_log_dt, A_D_skip, B_x, B_w1, B_b1, B_w2, B_b2):
    def _A_ref(u, log_A_real, A_imag, C_real, C_imag, log_dt, D_skip):
        L = u.shape[1]
        dt = torch.exp(log_dt)
        A = -torch.exp(log_A_real) + 1j * A_imag
        C = (C_real + 1j * C_imag) * dt[:, None]
        pos = (lambda x, dt: x.to(dt))(torch.arange(L), log_dt.dtype)
        P = torch.exp((A * dt[:, None])[:, :, None] * pos[None, None, :])
        K = 2.0 * torch.einsum("dn,dnl->dl", C, P).real
        L2 = 2 * L
        u_t = (lambda x, *d: x.permute(*d))(u, 0, 2, 1)
        Kf = (lambda x, *a, **k: torch.fft.rfft(x.float(), *a, **k))(K, L2)
        Uf = (lambda x, *a, **k: torch.fft.rfft(x.float(), *a, **k))(u_t, L2)
        y = (lambda x, *a, **k: torch.fft.irfft(x, *a, **k).to(torch.bfloat16))(Uf * Kf[None, :, :], L2)[:, :, :L]
        y = (lambda x, *d: x.permute(*d))(y, 0, 2, 1)
        return y + u * D_skip[None, None, :]

    def _B_ref(x, w1, b1, w2, b2):
        y = _layernorm(x, axis=-1)
        h = _gelu(torch.einsum("bsd,df->bsf", y, w1) + b1)
        return torch.einsum("bsf,fd->bsd", h, w2) + b2

    x = A_u
    xn = _layernorm(x, axis=-1)
    s = _A_ref(xn, A_log_A_real, A_A_imag, A_C_real, A_C_imag, A_log_dt, A_D_skip)
    g = _silu(_B_ref(xn, B_w1, B_b1, B_w2, B_b2))
    return x + s * g
```

```cpp
#include <hip/hip_runtime.h>
#include <hip/hip_bf16.h>

namespace {

typedef unsigned v4u __attribute__((ext_vector_type(4)));
typedef unsigned v2u __attribute__((ext_vector_type(2)));
typedef short s4v __attribute__((ext_vector_type(4)));
typedef float f2v __attribute__((ext_vector_type(2)));
typedef float f4v __attribute__((ext_vector_type(4)));
typedef float f16v __attribute__((ext_vector_type(16)));
typedef _Float16 h2v __attribute__((ext_vector_type(2)));
typedef _Float16 h8v __attribute__((ext_vector_type(8)));
typedef __bf16 b2v __attribute__((ext_vector_type(2)));
typedef __bf16 b8v __attribute__((ext_vector_type(8)));
#define GAS __attribute__((address_space(1)))
#define LAS __attribute__((address_space(3)))
#define DEV __device__ __forceinline__
typedef __amdgpu_buffer_rsrc_t rsrc_t;

constexpr int L_ = 2048, D_ = 640, F_ = 1728;
constexpr unsigned X_BYTES = L_ * D_ * 2, W_BYTES = D_ * F_ * 2;
constexpr int SC1 = 16;
constexpr unsigned SPIN_CAP = 1u << 16;

constexpr unsigned WS_EPOCH = 0;
constexpr unsigned WS_F1 = 4096;
constexpr unsigned WS_FW = 8192;
constexpr unsigned WS_F2 = 12288;
constexpr unsigned WS_F3 = 16384;
constexpr unsigned WS_STAT = 32768;
constexpr unsigned WS_XNT = 131072;
constexpr unsigned WS_W2T = 2752512;
constexpr unsigned WS_H = 5242880;
constexpr unsigned WS_ST = 12582912;
constexpr unsigned WS_STAMP = 15204352;
constexpr unsigned WS_BYTES = WS_STAMP + 256 * 8 * 16 * 8;

constexpr unsigned LDS_BYTES = 163840;
constexpr unsigned RING1 = 32768;
constexpr unsigned PERS = 131072;
constexpr unsigned KSCR = 8704;
constexpr unsigned LNST = PERS + 2 * KSCR;
constexpr unsigned CNT = PERS + 28160;
constexpr unsigned SEXC = PERS + 28672;
constexpr unsigned UCP = 4160;
constexpr unsigned UARR = 4608;
constexpr unsigned RING2 = 30720;

struct Params {
  const unsigned short *x, *lA, *Ai, *Cr, *Ci, *ldt, *Dsk, *w1, *b1, *w2, *b2;
  unsigned short* out;
  unsigned char* ws;
};

struct ConvLds { unsigned u0, u1, ua, red; };

#define STAMP(i) do { } while (0)

#define BAR() asm volatile("s_waitcnt lgkmcnt(0)\n\ts_barrier" ::: "memory")
#define WAITV(n) asm volatile("s_waitcnt vmcnt(" #n ")" ::: "memory")
#define SCHED() __builtin_amdgcn_sched_barrier(0)

DEV float u2f(unsigned u) { return __builtin_bit_cast(float, u); }
DEV unsigned f2u(float f) { return __builtin_bit_cast(unsigned, f); }
DEV h2v u2h(unsigned u) { return __builtin_bit_cast(h2v, u); }
DEV float blo(unsigned u) { return u2f(u << 16); }
DEV float bhi(unsigned u) { return u2f(u & 0xffff0000u); }
DEV unsigned pkbf(float a, float b) { return __builtin_bit_cast(unsigned, __builtin_convertvector((f2v){a, b}, b2v)); }
DEV unsigned pkh(float a, float b) { return __builtin_bit_cast(unsigned, __builtin_convertvector((f2v){a, b}, h2v)); }
DEV float bfr(float a) { return blo(pkbf(a, a)); }
DEV float dot2bf(unsigned a, unsigned b, float c) {
  return __builtin_amdgcn_fdot2_f32_bf16(__builtin_bit_cast(b2v, a), __builtin_bit_cast(b2v, b), c, false);
}
DEV f16v mfma32b(v4u a, v4u b, f16v c) { return __builtin_amdgcn_mfma_f32_32x32x16_bf16(__builtin_bit_cast(b8v, a), __builtin_bit_cast(b8v, b), c, 0, 0, 0); }
DEV f16v mfma32h(v4u a, v4u b, f16v c) { return __builtin_amdgcn_mfma_f32_32x32x16_f16(__builtin_bit_cast(h8v, a), __builtin_bit_cast(h8v, b), c, 0, 0, 0); }
DEV f4v mfma16b(v4u a, v4u b, f4v c) { return __builtin_amdgcn_mfma_f32_16x16x32_bf16(__builtin_bit_cast(b8v, a), __builtin_bit_cast(b8v, b), c, 0, 0, 0); }
DEV f4v mfma16h(v4u a, v4u b, f4v c) { return __builtin_amdgcn_mfma_f32_16x16x32_f16(__builtin_bit_cast(h8v, a), __builtin_bit_cast(h8v, b), c, 0, 0, 0); }

DEV LAS unsigned char* lp(unsigned a) { return (LAS unsigned char*)(size_t)a; }
DEV v4u lds128(unsigned a) { return *(LAS v4u*)lp(a); }
DEV v2u lds64(unsigned a) { return *(LAS v2u*)lp(a); }
DEV unsigned lds32(unsigned a) { return *(LAS unsigned*)lp(a); }
DEV void sts128(unsigned a, v4u v) { *(LAS v4u*)lp(a) = v; }
DEV void sts64(unsigned a, v2u v) { *(LAS v2u*)lp(a) = v; }
DEV void sts32(unsigned a, unsigned v) { *(LAS unsigned*)lp(a) = v; }
DEV v2u ldstr(unsigned a) { return __builtin_bit_cast(v2u, __builtin_amdgcn_ds_read_tr16_b64_v4i16((LAS s4v*)lp(a))); }
DEV v4u ldstr2(unsigned a0, unsigned a1) { const v2u x = ldstr(a0), y = ldstr(a1); return (v4u){x.x, x.y, y.x, y.y}; }

DEV v4u make_srd(const void* ptr, unsigned bytes) {
  return (v4u){(unsigned)(size_t)ptr, (unsigned)((size_t)ptr >> 32) & 0xffffu, bytes, 0x00020000u};
}
DEV rsrc_t make_rsrc(const void* ptr, unsigned bytes) { return __builtin_amdgcn_make_buffer_rsrc((void*)ptr, 0, bytes, 0x00020000); }
DEV void dma(v4u srd, unsigned voff, unsigned soff, unsigned lds) {
  asm volatile("s_mov_b32 m0, %0\n\ts_nop 0\n\tbuffer_load_dwordx4 %1, %2, %3 offen lds" ::"s"(lds), "v"(voff), "s"(srd), "s"(soff) : "memory");
}
DEV void dma_sc1(v4u srd, unsigned voff, unsigned soff, unsigned lds) {
  asm volatile("s_mov_b32 m0, %0\n\ts_nop 0\n\tbuffer_load_dwordx4 %1, %2, %3 offen sc1 lds" ::"s"(lds), "v"(voff), "s"(srd), "s"(soff) : "memory");
}

DEV void poll_words(rsrc_t ws, unsigned off, bool on, unsigned tag) {
  for (unsigned s = 0; s < SPIN_CAP; ++s) {
    const unsigned v = __builtin_amdgcn_raw_buffer_load_b32(ws, off, 0, SC1);
    if (__all(!on || v == tag)) break;
    __builtin_amdgcn_s_sleep(1);
  }
  asm volatile("" ::: "memory");
}
DEV void poll_words4(rsrc_t ws, unsigned off, unsigned tag) {
  for (unsigned s = 0; s < SPIN_CAP; ++s) {
    const v4u v = __builtin_amdgcn_raw_buffer_load_b128(ws, off, 0, SC1);
    if (__all(v.x == tag && v.y == tag && v.z == tag && v.w == tag)) break;
    __builtin_amdgcn_s_sleep(1);
  }
  asm volatile("" ::: "memory");
}
DEV unsigned lds_arrive(unsigned cnt, int lane) {
  asm volatile("s_waitcnt lgkmcnt(0)" ::: "memory");
  unsigned old = 0;
  if (lane == 0) old = __hip_atomic_fetch_add((LAS unsigned*)lp(cnt), 1u, __ATOMIC_RELAXED, __HIP_MEMORY_SCOPE_WORKGROUP);
  return __builtin_amdgcn_readfirstlane(old);
}
DEV void group_sync(unsigned cnt, unsigned target, int lane) {
  if (lds_arrive(cnt, lane) + 1 < target) {
    for (unsigned s = 0; s < (SPIN_CAP << 4); ++s) {
      const unsigned v = __hip_atomic_load((LAS unsigned*)lp(cnt), __ATOMIC_RELAXED, __HIP_MEMORY_SCOPE_WORKGROUP);
      if (__builtin_amdgcn_readfirstlane(v) >= target) break;
    }
  }
  asm volatile("" ::: "memory");
}
DEV void publish(rsrc_t ws, unsigned cnt, unsigned nwaves, unsigned flag_off, unsigned tag, int lane) {
  WAITV(0);
  if (lds_arrive(cnt, lane) + 1 == nwaves && lane == 0) __builtin_amdgcn_raw_buffer_store_b32(tag, ws, flag_off, 0, SC1);
}

DEV unsigned epoch_tag(rsrc_t ws, bool share, int lane) {
  const unsigned e = __builtin_amdgcn_readfirstlane(__builtin_amdgcn_raw_buffer_load_b32(ws, WS_EPOCH, 0, SC1));
  const unsigned tag = e * 0x9E3779B1u + 0x7F4A7C15u;
  if (share && lane == 0) sts64(CNT + 120, (v2u){tag, e});
  return tag;
}
DEV unsigned lds_tag() { return __builtin_amdgcn_readfirstlane(lds32(CNT + 120)); }

template <int CTRL> DEV float dpp_add(float v) {
  return v + u2f(__builtin_amdgcn_update_dpp(0u, f2u(v), CTRL, 0xf, 0xf, false));
}
template <int CTRL> DEV float dpp_max(float v) {
  return fmaxf(v, u2f(__builtin_amdgcn_update_dpp(0u, f2u(v), CTRL, 0xf, 0xf, false)));
}
DEV float half_sum(float v) {
  v = dpp_add<0x128>(v); v = dpp_add<0x124>(v); v = dpp_add<0x122>(v); v = dpp_add<0x121>(v);
  const auto r = __builtin_amdgcn_permlane16_swap(f2u(v), f2u(v), false, false);
  return u2f(r[0]) + u2f(r[1]);
}
DEV float wave_max(float v) {
  v = dpp_max<0x128>(v); v = dpp_max<0x124>(v); v = dpp_max<0x122>(v); v = dpp_max<0x121>(v);
  const auto r = __builtin_amdgcn_permlane16_swap(f2u(v), f2u(v), false, false);
  v = fmaxf(u2f(r[0]), u2f(r[1]));
  const auto q = __builtin_amdgcn_permlane32_swap(f2u(v), f2u(v), false, false);
  return fmaxf(u2f(q[0]), u2f(q[1]));
}

struct LnIn { v2u v[5]; };
DEV LnIn ln_load(rsrc_t xr, int token, int lane) {
  LnIn r;
#pragma unroll
  for (int m = 0; m < 5; ++m) r.v[m] = __builtin_amdgcn_raw_buffer_load_b64(xr, token * 1280 + (lane & 31) * 8 + m * 256, 0, 0);
  return r;
}
DEV void ln_compute(const LnIn& in, rsrc_t ws, int token, int row, unsigned tag, int lane) {
  float s = 0.f, q = 0.f;
#pragma unroll
  for (int m = 0; m < 5; ++m) {
    s = dot2bf(in.v[m].x, 0x3f803f80u, s); q = dot2bf(in.v[m].x, in.v[m].x, q);
    s = dot2bf(in.v[m].y, 0x3f803f80u, s); q = dot2bf(in.v[m].y, in.v[m].y, q);
  }
  s = half_sum(s); q = half_sum(q);
  const float mean = s * (1.f / 640.f);
  const float var = fmaxf(fmaf(-mean, mean, q * (1.f / 640.f)), 0.f);
  const float m1 = bfr(mean), s1 = bfr(__builtin_amdgcn_sqrtf(bfr(bfr(var) + 1e-5f)));
  const float r1 = __builtin_amdgcn_rcpf(s1);
  const float m2 = bfr((mean - m1) * r1), s2 = bfr(__builtin_amdgcn_sqrtf(bfr(bfr(var * r1 * r1) + 1e-5f)));
  if ((lane & 31) == 0)
    __builtin_amdgcn_raw_buffer_store_b128((v4u){f2u(fmaf(m2, s1, m1)), tag, f2u(s1 * s2), tag}, ws, WS_STAT + token * 16, 0, SC1);
  const float c = -m1 * r1;
#pragma unroll
  for (int m = 0; m < 5; ++m) {
    const v2u o = {pkh(fmaf(blo(in.v[m].x), r1, c), fmaf(bhi(in.v[m].x), r1, c)), pkh(fmaf(blo(in.v[m].y), r1, c), fmaf(bhi(in.v[m].y), r1, c))};
    sts64(LNST + row * 1344 + (lane & 31) * 8 + m * 256, o);
  }
}
DEV void ln_publish(rsrc_t ws, int tb, int aw, int lane) {
  const unsigned a = LNST + ((lane & 15) >> 2) * 1344 + (lane >> 4) * 32 + (lane & 3) * 8 + aw * 640;
#pragma unroll
  for (int i = 0; i < 5; ++i) {
    const v4u v = ldstr2(a + i * 128, a + i * 128 + 4 * 1344);
    __builtin_amdgcn_raw_buffer_store_b128(v, ws, WS_XNT + tb * 10240 + (aw * 320 + i * 64 + lane) * 16, 0, SC1);
  }
}

struct Cx { float r, i; };
DEV Cx cmul(Cx a, Cx b) { return {fmaf(a.r, b.r, -a.i * b.i), fmaf(a.r, b.i, a.i * b.r)}; }
DEV Cx csqr(Cx a) { return {fmaf(a.r, a.r, -a.i * a.i), (a.r + a.r) * a.i}; }
DEV unsigned ch(Cx a) { return pkh(a.r, a.i); }
DEV unsigned chc(Cx a) { return pkh(a.r, -a.i); }
DEV unsigned hmul(unsigned a, unsigned b) {
  unsigned t, r;
  asm("v_pk_mul_f16 %0, %1, %2 op_sel:[0,0] op_sel_hi:[0,1]" : "=v"(t) : "v"(a), "v"(b));
  asm("v_pk_fma_f16 %0, %1, %2, %3 op_sel:[1,1,0] op_sel_hi:[1,0,1] neg_lo:[0,1,0]" : "=v"(r) : "v"(a), "v"(b), "v"(t));
  return r;
}
DEV float exp_bf(float x) {
  const float t = x * 1.44268798828125f, lo = x * 7.0526077138765e-06f;
  const float n = __builtin_rintf(t);
  return bfr(__builtin_amdgcn_ldexpf(__builtin_amdgcn_exp2f((t - n) + lo), (int)fminf(fmaxf(n, -200.f), 200.f)));
}
DEV Cx cexp_at(float lg, float rh, float rl, float P) {
  const float m = __builtin_amdgcn_exp2f(lg * P), ph = fmaf(rl, P, __builtin_amdgcn_fractf(rh * P));
  return {m * __builtin_amdgcn_cosf(ph), m * __builtin_amdgcn_sinf(ph)};
}
struct KgIn { unsigned lA, Ai, Cr, Ci; float dt, dsk; };
DEV KgIn kg_load(const Params& p, int d, int lane) {
  KgIn k;
  k.lA = ((const GAS unsigned*)p.lA)[d * 64 + lane]; k.Ai = ((const GAS unsigned*)p.Ai)[d * 64 + lane];
  k.Cr = ((const GAS unsigned*)p.Cr)[d * 64 + lane]; k.Ci = ((const GAS unsigned*)p.Ci)[d * 64 + lane];
  k.dt = u2f((unsigned)((const GAS unsigned short*)p.ldt)[d] << 16);
  k.dsk = u2f((unsigned)((const GAS unsigned short*)p.Dsk)[d] << 16);
  return k;
}
template <class Sync>
DEV void kg_group(float lA, float Ai, float cr, float ci, float dt, unsigned scr, f16v& acc, int lane, Sync sync) {
  const float lg = -(exp_bf(lA) * dt) * 1.44269504088896f, ai = Ai * dt;
  const float rh = ai * 0.15915494f, rl = fmaf(ai, 0.15915494f, -rh) + ai * 6.4206382e-09f;
  const Cx v1 = cexp_at(lg, rh, rl, 1.f), w1 = cexp_at(lg, rh, rl, 8.f), G = cexp_at(lg, rh, rl, 256.f);
  const unsigned wa = scr + lane * 4, ra = scr + (lane & 31) * 272 + (lane >> 5) * 16;
  {
    const Cx w2 = csqr(w1), w3 = cmul(w2, w1), w4 = csqr(w2), w8 = csqr(w4), w12 = cmul(w8, w4), w16 = csqr(w8);
    const Cx w20 = cmul(w16, w4), w24 = csqr(w12), w28 = cmul(w24, w4);
    const unsigned o[3] = {ch(w1), ch(w2), ch(w3)};
    const unsigned bs[7] = {ch(w4), ch(w8), ch(w12), ch(w16), ch(w20), ch(w24), ch(w28)};
    sync(0);
    sts32(wa, 0x00003c00u);
#pragma unroll
    for (int j = 0; j < 3; ++j) sts32(wa + (1 + j) * 272, o[j]);
#pragma unroll
    for (int b = 0; b < 7; ++b) {
      sts32(wa + (4 + 4 * b) * 272, bs[b]);
#pragma unroll
      for (int j = 0; j < 3; ++j) sts32(wa + (5 + 4 * b + j) * 272, hmul(bs[b], o[j]));
    }
  }
  asm volatile("" ::: "memory");
  v4u y[8];
#pragma unroll
  for (int s = 0; s < 8; ++s) y[s] = lds128(ra + s * 32);
  sync(1);
  {
    const Cx c0 = {cr, ci};
    const Cx v2 = csqr(v1), v3 = cmul(v2, v1), v4 = csqr(v2), v6 = csqr(v3), x4 = cmul(c0, v4);
    const unsigned h1 = chc(v1), h2 = chc(v2), h3 = chc(v3), h4 = chc(v4), h6 = chc(v6), hx0 = chc(c0), hx4 = chc(x4);
    asm volatile("s_waitcnt lgkmcnt(0)" ::: "memory");
    sts32(wa + 0 * 272, hx0); sts32(wa + 1 * 272, hmul(hx0, h1)); sts32(wa + 2 * 272, hmul(hx0, h2)); sts32(wa + 3 * 272, hmul(hx0, h3));
    sts32(wa + 4 * 272, hx4); sts32(wa + 5 * 272, hmul(hx4, h1)); sts32(wa + 6 * 272, hmul(hx4, h2)); sts32(wa + 7 * 272, hmul(hx4, h3));
    sync(2);
    const Cx d1 = cmul(c0, G), d2 = cmul(d1, G), d3 = cmul(d2, G), d4 = cmul(d3, G);
    const Cx G4 = csqr(csqr(G));
    const Cx d5 = cmul(d1, G4), d6 = cmul(d2, G4), d7 = cmul(d3, G4), d8 = cmul(d4, G4);
    const unsigned e1 = chc(d1), e2 = chc(d2), e3 = chc(d3);
    sts32(wa + 8 * 272, e1); sts32(wa + 9 * 272, hmul(e1, h2)); sts32(wa + 10 * 272, hmul(e1, h4)); sts32(wa + 11 * 272, hmul(e1, h6));
    sts32(wa + 12 * 272, e2); sts32(wa + 13 * 272, hmul(e2, h4)); sts32(wa + 14 * 272, e3); sts32(wa + 15 * 272, hmul(e3, h4));
    sts32(wa + 16 * 272, chc(d4)); sts32(wa + 17 * 272, chc(d5)); sts32(wa + 18 * 272, chc(d6)); sts32(wa + 19 * 272, chc(d7));
    sts32(wa + 20 * 272, chc(d8));
  }
  asm volatile("" ::: "memory");
#pragma unroll
  for (int s = 0; s < 8; ++s) acc = mfma32h(lds128(ra + s * 32), y[s], acc);
  asm volatile("s_waitcnt lgkmcnt(0)" ::: "memory");
  sync(3);
}
template <class Sync>
DEV float kg_channel(const KgIn& k, unsigned scr, int lane, Sync sync) {
  const float dt = exp_bf(k.dt), c2 = dt + dt;
  const float cr0 = blo(k.Cr) * c2, cr1 = bhi(k.Cr) * c2, ci0 = blo(k.Ci) * c2, ci1 = bhi(k.Ci) * c2;
  const float mx = wave_max(fmaxf(fmaxf(fabsf(cr0), fabsf(cr1)), fmaxf(fabsf(ci0), fabsf(ci1))));
  int ex = __builtin_amdgcn_frexp_expf(mx);
  ex = ex < -120 ? -120 : (ex > 120 ? 120 : ex);
  const float sc = __builtin_amdgcn_ldexpf(1.f, -ex), isc = __builtin_amdgcn_ldexpf(1.f, ex);
  f16v acc = {};
  kg_group(blo(k.lA), blo(k.Ai), cr0 * sc, ci0 * sc, dt, scr, acc, lane, [&](int i) { sync(i); });
  kg_group(bhi(k.lA), bhi(k.Ai), cr1 * sc, ci1 * sc, dt, scr, acc, lane, [&](int i) { sync(4 + i); });
  const unsigned fp = scr + UCP;
  const int j = lane & 31, h = lane >> 5;
  sts128(fp + (8 * j + 4 * h) * 4, (v4u){f2u(acc[0]), f2u(acc[1]), f2u(acc[2]), f2u(acc[3])});
  sts64(fp + (h ? 384 + 2 * j : 256 + 4 * j) * 4, (v2u){f2u(acc[4]), f2u(acc[5])});
  sts64(fp + (h ? 448 + 2 * j : 258 + 4 * j) * 4, (v2u){f2u(acc[6]), f2u(acc[7])});
#pragma unroll
  for (int i = 0; i < 4; ++i) sts32(fp + (512 + 128 * h + j + 32 * i) * 4, f2u(acc[8 + i]));
  asm volatile("s_waitcnt lgkmcnt(0)" ::: "memory");
  {
    const unsigned a = fp + (h ? 256 + 4 * j : 8 * j) * 4;
    const v4u lo = lds128(a), hi = lds128(a + 16);
    const float e0 = u2f(lo.x), e1 = u2f(lo.y), e2 = u2f(lo.z), e3 = u2f(lo.w), e4 = u2f(hi.x);
    const v4u o0 = {pkh(e0, e1), pkh(e2, e3), pkh(e4, u2f(hi.y)), pkh(u2f(hi.z), u2f(hi.w))};
    const v4u o1 = {pkh(e0, e0), pkh(e1, e2), pkh(e2, e2), pkh(e3, e4)};
    sts128(scr + 32 + lane * 16, h ? o1 : o0);
  }
  {
    const v2u ab = lds64(fp + (384 + 2 * lane) * 4);
    const float a = u2f(ab.x), b = u2f(ab.y), c = u2f(lds32(fp + (386 + 2 * lane) * 4));
    sts128(scr + 32 + (64 + lane) * 16, (v4u){pkh(a, a), pkh(a, b), pkh(b, b), pkh(c, c)});
  }
#pragma unroll
  for (int i = 0; i < 2; ++i) {
    const float a = u2f(lds32(fp + (512 + 64 * i + lane) * 4)), b = u2f(lds32(fp + (513 + 64 * i + lane) * 4));
    const unsigned aa = pkh(a, a);
    sts128(scr + 32 + (128 + 64 * i + lane) * 16, (v4u){aa, aa, pkh((lane & 1) ? b : a, b), pkh(b, b)});
  }
  if (lane < 8) sts32(scr + lane * 4, 0u);
  asm volatile("s_waitcnt lgkmcnt(0)" ::: "memory");
  return isc;
}
DEV void kg_copies(const ConvLds& c, int lane) {
#pragma unroll
  for (int i = 0; i < 4; ++i) {
    const unsigned o = 32 + (64 * i + lane) * 16;
    const v2u pv = lds64(c.u0 + o - 8);
    const v4u e = lds128(c.u0 + o);
    const unsigned a0 = __builtin_amdgcn_alignbit(pv.y, pv.x, 16), a1 = __builtin_amdgcn_alignbit(e.x, pv.y, 16), a2 = __builtin_amdgcn_alignbit(e.y, e.x, 16),
                   a3 = __builtin_amdgcn_alignbit(e.z, e.y, 16), a4 = __builtin_amdgcn_alignbit(e.w, e.z, 16);
    sts128(c.u1 + o, (v4u){a1, a2, a3, a4});
    sts128(c.u1 + UCP + o, (v4u){pv.y, e.x, e.y, e.z});
    sts128(c.u1 + 2 * UCP + o, (v4u){a0, a1, a2, a3});
  }
  if (lane < 24) sts32(c.u1 + (lane >> 3) * UCP + (lane & 7) * 4, 0u);
}

DEV void conv_channel(rsrc_t ws, const ConvLds& c, unsigned red_other, int d, int eh, float isc, float dsk, unsigned cnt, unsigned target, int lane) {
  const int q = (lane & 15) >> 2, g = lane >> 4;
  const unsigned ga = (q == 0 ? c.u0 : c.u1 + (q - 1) * UCP) + 64 - 16 * g + 8 * (lane & 3) + eh * 256;
  const unsigned ba = c.ua + 480 + 32 * (lane & 15) + 16 * g - eh * 256;
  f4v acc[8] = {};
  v4u G[2][8], B[2][8];
#pragma unroll
  for (int i = 0; i < 8; ++i) { G[0][i] = ldstr2(ga + 512 * i, ga + 512 * i - 8); B[0][i] = lds128(ba + 512 * i); }
#pragma unroll
  for (int e = 0; e < 4; ++e) {
    if (e < 3) {
#pragma unroll
      for (int i = 0; i < 8; ++i) {
        G[(e + 1) & 1][i] = ldstr2(ga + 512 * i + 64 * (e + 1), ga + 512 * i + 64 * (e + 1) - 8);
        B[(e + 1) & 1][i] = lds128(ba + 512 * i - 64 * (e + 1));
      }
    }
#pragma unroll
    for (int cc = 0; cc < 8; ++cc)
#pragma unroll
      for (int j = cc; j < 8; ++j) acc[j] = mfma16h(G[e & 1][cc], B[e & 1][j - cc], acc[j]);
    SCHED();
  }
  f4v mine[4];
#pragma unroll
  for (int j = 0; j < 4; ++j) {
    sts128(c.red + j * 1024 + lane * 16, __builtin_bit_cast(v4u, eh ? acc[j] : acc[4 + j]));
    mine[j] = eh ? acc[4 + j] : acc[j];
  }
  group_sync(cnt, target, lane);
  const unsigned to = 2 * (1024 * eh + 16 * (lane & 15) + 4 * g);
#pragma unroll
  for (int j = 0; j < 4; ++j) {
    const f4v o = __builtin_bit_cast(f4v, lds128(red_other + j * 1024 + lane * 16));
    const v2u uu = lds64(c.ua + 512 + to + 512 * j);
    const h2v u0 = u2h(uu.x), u1 = u2h(uu.y);
    const float s0 = fmaf(mine[j][0] + o[0], isc, dsk * (float)u0.x), s1 = fmaf(mine[j][1] + o[1], isc, dsk * (float)u0.y);
    const float s2 = fmaf(mine[j][2] + o[2], isc, dsk * (float)u1.x), s3 = fmaf(mine[j][3] + o[3], isc, dsk * (float)u1.y);
    __builtin_amdgcn_raw_buffer_store_b64((v2u){pkbf(s0, s1), pkbf(s2, s3)}, ws, WS_ST + d * 4096 + to + 512 * j, 0, SC1);
  }
}
DEV void conv_load_u(rsrc_t ws, unsigned ua0, unsigned ua1, int d0, int aw, int lane) {
  v4u v[4];
#pragma unroll
  for (int i = 0; i < 4; ++i)
    v[i] = __builtin_amdgcn_raw_buffer_load_b128(ws, WS_XNT + (128 * aw + 32 * i + (lane >> 1)) * 10240 + (d0 + (lane & 1)) * 16, 0, SC1);
  const unsigned ua = (lane & 1) ? ua1 : ua0;
  if (lane < 32) sts128((aw ? ua1 : ua0) + lane * 16, (v4u){0u, 0u, 0u, 0u});
#pragma unroll
  for (int i = 0; i < 4; ++i) sts128(ua + 512 + (128 * aw + 32 * i + (lane >> 1)) * 16, v[i]);
}

DEV ConvLds conv_lds_main(int ci, int eh) {
  return {PERS + ci * KSCR, 92224u + ci * 12544u, 117248u + ci * UARR, eh ? LNST + ci * 4096u : PERS + ci * KSCR + UCP};
}
DEV ConvLds conv_lds_extra(int ci, int eh) {
  return {ci * KSCR, 34880u + ci * 12544u, 85056u + ci * UARR, eh ? 103488u + ci * 4096u : ci * KSCR + UCP};
}

DEV void w2_chunk(v4u wss, int ct, int lw, int c, int lane) {
#pragma unroll
  for (int i = 0; i < 15; ++i)
    dma_sc1(wss, lane * 16, WS_W2T + ((ct * 54 + 27 * lw + 3 * c) * 5 + i) * 1024, (c % 5) * RING2 + lw * 15360 + i * 1024);
}

enum { C_H = 0, C_S = 4, C_G0 = 8, C_G1 = 12, C_G2 = 16, C_LN = 20, C_W = 24, C_X0 = 28, C_X1 = 32, C_Y0 = 36, C_Y1 = 40, C_F1 = 44 };

__global__ __launch_bounds__(512, 2) void fused_kernel(Params p) {
  extern __shared__ __attribute__((aligned(16))) unsigned char lds_base[];
  const int lane = threadIdx.x & 63, wave = __builtin_amdgcn_readfirstlane(threadIdx.x >> 6);
  const int xcd = blockIdx.x & 7, slot = blockIdx.x >> 3, tb = 32 * xcd + slot;
  const bool gemm_wg = slot < 28;
  const rsrc_t ws = make_rsrc(p.ws, WS_BYTES), xr = make_rsrc(p.x, X_BYTES);
  const v4u wss = make_srd(p.ws, WS_BYTES);
  const int d0 = 80 * xcd + (gemm_wg ? 2 * slot : 56 + 6 * (slot - 28) + 4);
  const int tt = 4 * xcd + (slot >> 3), ct = slot & 7;
  STAMP(0);
  asm volatile("" ::"v"((LAS unsigned char*)lds_base));
  if (threadIdx.x < 16) sts32(CNT + threadIdx.x * 4, 0u);
  float isc = 0.f, dsk = 0.f;

  if (gemm_wg) {
    const int rp = 2 * xcd + slot / 14, cp = slot % 14, t0 = 128 * rp, f0 = 128 * cp;
    if (wave < 4) {
      const int wf = wave & 1, wt = wave >> 1, h = lane >> 5;
      const LnIn ln = ln_load(xr, 8 * tb + 2 * wave + h, lane);
      const unsigned tag = epoch_tag(ws, wave == 0, lane);
      ln_compute(ln, ws, 8 * tb + 2 * wave + h, 2 * wave + h, tag, lane);
      STAMP(1);
      unsigned aa[2], ba[4];
      {
        const int q = (lane & 15) >> 2, pp = lane & 3, fb = (lane >> 4) & 1;
#pragma unroll
        for (int mi = 0; mi < 2; ++mi)
          aa[mi] = 16384 + (8 * h + q) * 256 + 16 * ((8 * wf + 4 * (mi ^ wt) + 2 * fb + (pp >> 1)) ^ (q << 2)) + 8 * (pp & 1);
#pragma unroll
        for (int s = 0; s < 4; ++s) ba[s] = (64 * wt + (lane & 31)) * 128 + 16 * ((2 * s + h) ^ ((lane >> 1) & 7));
      }
      f16v acc[2][2] = {};
      v4u A[2][2], B[2][2], st[2];
      unsigned b1v[2];
      float cs = 0.f;
      auto rd = [&](int buf, int n) {
        const unsigned so = ((n >> 2) & 3) * RING1, s = n & 3;
#pragma unroll
        for (int mi = 0; mi < 2; ++mi) A[buf][mi] = ldstr2(aa[mi] + so + s * 4096, aa[mi] + so + s * 4096 + 1024);
#pragma unroll
        for (int ni = 0; ni < 2; ++ni) B[buf][ni] = lds128(ba[s] + so + ni * 4096);
      };
      BAR();
      STAMP(2);
      rd(0, 0);
#pragma unroll
      for (int n = 0; n < 40; ++n) {
        if (n == 28) {
#pragma unroll
          for (int i = 0; i < 2; ++i) {
            st[i] = __builtin_amdgcn_raw_buffer_load_b128(ws, WS_STAT + (t0 + 64 * wt + 32 * i + (lane & 31)) * 16, 0, SC1);
            const int f = f0 + 64 * wf + 32 * (i ^ wt) + (lane & 31);
            b1v[i] = ((const GAS unsigned short*)p.b1)[f < F_ ? f : 0];
          }
        }
        if (n < 39) {
          if (((n + 1) & 3) == 0) BAR();
          rd((n + 1) & 1, n + 1);
        }
        const v4u a0 = A[n & 1][0];
        cs = dot2bf(a0.x, 0x3f803f80u, cs); cs = dot2bf(a0.y, 0x3f803f80u, cs);
        cs = dot2bf(a0.z, 0x3f803f80u, cs); cs = dot2bf(a0.w, 0x3f803f80u, cs);
        if (n == 39) {
          const auto r = __builtin_amdgcn_permlane32_swap(f2u(cs), f2u(cs), false, false);
          cs = u2f(r[0]) + u2f(r[1]);
          if (h == 0) sts32(SEXC + wave * 128 + lane * 4, f2u(cs));
          BAR();
        }
#pragma unroll
        for (int mi = 0; mi < 2; ++mi)
#pragma unroll
          for (int ni = 0; ni < 2; ++ni) acc[mi][ni] = mfma32b(A[n & 1][mi], B[n & 1][ni], acc[mi][ni]);
        SCHED();
      }
      STAMP(3);
      for (unsigned s = 0; s < SPIN_CAP; ++s) {
        if (__all(st[0].y == tag && st[0].w == tag && st[1].y == tag && st[1].w == tag)) break;
#pragma unroll
        for (int i = 0; i < 2; ++i) st[i] = __builtin_amdgcn_raw_buffer_load_b128(ws, WS_STAT + (t0 + 64 * wt + 32 * i + (lane & 31)) * 16, 0, SC1);
      }
      {
        const float S[2] = {cs, u2f(lds32(SEXC + (wave ^ 2) * 128 + (lane & 31) * 4))};
        v4u ea[2], eb[2];
#pragma unroll
        for (int i = 0; i < 2; ++i) {
          const float sh = bfr(S[i]);
          const unsigned bb = b1v[i] | (b1v[i] << 16);
          ea[i] = h ? (v4u){0u, 0u, 0u, 0u} : (v4u){pkbf(sh, sh), pkbf(S[i] - sh, 0.f), bb, 0u};
          const float mu = -u2f(st[i].x), ir = u2f(st[i].z), mh = bfr(mu), ih = bfr(ir);
          eb[i] = h ? (v4u){0u, 0u, 0u, 0u} : (v4u){pkbf(mh, mu - mh), pkbf(mh, 0.f), pkbf(ih, ir - ih), 0u};
        }
#pragma unroll
        for (int mi = 0; mi < 2; ++mi)
#pragma unroll
          for (int ni = 0; ni < 2; ++ni) acc[mi][ni] = mfma32b(ea[mi], eb[ni], acc[mi][ni]);
      }
      if (f0 + 64 * wf < F_) {
#pragma unroll
        for (int ni = 0; ni < 2; ++ni) {
          const float ir = u2f(st[ni].z), rho = __builtin_amdgcn_rcpf(ir);
          const float k1 = -2.302208198f * rho, k2 = k1 * 0.044715f * rho * rho;
          const unsigned ho = WS_H + ((t0 + 64 * wt + 32 * ni) / 16 + ((lane >> 4) & 1)) * 55296 + h * 256 + (lane & 15) * 16;
#pragma unroll
          for (int mi = 0; mi < 2; ++mi) {
            const int ks = (f0 + 64 * wf) / 32 + (mi ^ wt);
#pragma unroll
            for (int hf = 0; hf < 2; ++hf) {
              unsigned o[4];
#pragma unroll
              for (int r = 0; r < 4; ++r) {
                const f2v a = {acc[mi][ni][8 * hf + 2 * r], acc[mi][ni][8 * hf + 2 * r + 1]};
                const f2v w = a * (a * a * k2 + k1);
                const f2v e = {__builtin_amdgcn_exp2f(w.x), __builtin_amdgcn_exp2f(w.y)};
                const f2v dd = e * ir + ir;
                const f2v rr = {__builtin_amdgcn_rcpf(dd.x), __builtin_amdgcn_rcpf(dd.y)};
                const f2v gl = a * rr;
                o[r] = pkbf(gl.x, gl.y);
              }
              __builtin_amdgcn_raw_buffer_store_b128((v4u){o[0], o[1], o[2], o[3]}, ws, ho + ks * 1024 + hf * 512, 0, SC1);
            }
          }
        }
      }
      STAMP(4);
      publish(ws, CNT + C_H, 4, WS_F2 + (rp * 16 + cp) * 4, tag, lane);
      STAMP(5);
    } else if (wave < 6) {
      const int lw = wave - 4, rot = (slot * 3) % 10;
      const v4u xs = make_srd(p.x, X_BYTES), w1s = make_srd(p.w1, W_BYTES);
      const unsigned vx0 = (lane >> 3) * 1280 + 16 * ((lane & 7) ^ (lane >> 4)), vx1 = vx0 ^ 64;
      const unsigned vw = (lane >> 4) * 3456 + 16 * ((lane & 15) ^ ((lane >> 4) << 2));
      auto stage = [&](int i) {
        const int kt = (i + rot) % 10;
        const unsigned sx = (t0 + 64 * lw) * 1280 + 128 * kt, sw = (64 * kt + 32 * lw) * 3456 + 2 * f0, lb = (i & 3) * RING1;
#pragma unroll
        for (int j = 0; j < 8; ++j) {
          dma(xs, (j & 1) ? vx1 : vx0, sx + j * 10240, lb + lw * 8192 + j * 1024);
          dma(w1s, vw, sw + j * 13824, lb + 16384 + lw * 8192 + j * 1024);
        }
      };
      stage(0); stage(1); stage(2);
      WAITV(32);
      stage(3);
      STAMP(1);
      BAR();
#pragma unroll 1
      for (int i = 1; i < 7; ++i) { WAITV(32); BAR(); stage(i + 3); }
      WAITV(32); BAR();
      WAITV(16); BAR();
      WAITV(0); BAR();
      STAMP(2);
      poll_words(ws, WS_FW + (lane & 31) * 4, true, lds_tag());
      BAR();
    } else {
      const int aw = wave - 6;
      const KgIn kin = kg_load(p, d0 + aw, lane);
      dsk = kin.dsk;
      BAR();
      const unsigned tag = lds_tag();
      ln_publish(ws, tb, aw, lane);
      STAMP(1);
      isc = kg_channel(kin, PERS + aw * KSCR, lane, [&](int i) {
        SCHED();
        if (i == 2) publish(ws, CNT + C_F1, 2, WS_F1 + tb * 4, tag, lane);
        BAR();
        SCHED();
      });
      STAMP(2);
      if (lane == 0) { sts32(CNT + 64 + aw * 8, f2u(isc)); sts32(CNT + 68 + aw * 8, f2u(dsk)); }
      BAR();
      BAR();
    }
  } else {
    const int fi = 4 * xcd + (slot - 28), dA = d0 - 4;
    if (wave < 6) {
      const KgIn kin = kg_load(p, dA + wave, lane);
      dsk = kin.dsk;
      BAR();
      isc = kg_channel(kin, wave < 4 ? wave * KSCR : PERS + (wave - 4) * KSCR, lane, [&](int) {});
      if (lane == 0) { sts32(CNT + 64 + ((wave + 2) % 6) * 8, f2u(isc)); sts32(CNT + 68 + ((wave + 2) % 6) * 8, f2u(dsk)); }
      STAMP(1);
    } else {
      const int aw = wave - 6, h = lane >> 5;
      const LnIn la = ln_load(xr, 8 * tb + 4 * aw + h, lane), lb = ln_load(xr, 8 * tb + 4 * aw + 2 + h, lane);
      if (aw == 0) epoch_tag(ws, true, lane);
      BAR();
      const unsigned tag = lds_tag();
      ln_compute(la, ws, 8 * tb + 4 * aw + h, 4 * aw + h, tag, lane);
      ln_compute(lb, ws, 8 * tb + 4 * aw + 2 + h, 4 * aw + 2 + h, tag, lane);
      group_sync(CNT + C_LN, 2, lane);
      ln_publish(ws, tb, aw, lane);
      publish(ws, CNT + C_F1, 2, WS_F1 + tb * 4, tag, lane);
      STAMP(1);
      const v4u w2s = make_srd(p.w2, W_BYTES);
      const unsigned stg = 49152 + aw * 36864;
      const unsigned vw = (lane >> 3) * 1280 + 16 * ((lane & 7) ^ (((lane >> 4) & 3) << 1));
      const int b0 = fi * 17 + aw * 9, nb = min(aw ? 8 : 9, 540 - b0);
#pragma unroll 1
      for (int i = 0; i < nb; ++i) {
        const int blk = b0 + i, ks = blk / 10, cb = blk % 10;
#pragma unroll
        for (int j = 0; j < 4; ++j) dma(w2s, vw, (32 * ks + 8 * j) * 1280 + cb * 128, stg + i * 4096 + j * 1024);
      }
      WAITV(0);
      const int g = lane >> 4, q = (lane & 15) >> 2, pp = lane & 3, row = 16 * (g >> 1) + 4 * (g & 1) + q;
#pragma unroll 1
      for (int i = 0; i < nb; ++i) {
        const int blk = b0 + i, ks = blk / 10, cb = blk % 10;
#pragma unroll
        for (int j = 0; j < 4; ++j) {
          const unsigned a = stg + i * 4096 + row * 128 + 16 * ((2 * j + (pp >> 1)) ^ (((row >> 1) & 3) << 1)) + 8 * (pp & 1);
          const int ng = 4 * cb + j;
          __builtin_amdgcn_raw_buffer_store_b128(ldstr2(a, a + 1024), ws, WS_W2T + (((ng / 5) * 54 + ks) * 5 + ng % 5) * 1024 + lane * 16, 0, SC1);
        }
      }
      publish(ws, CNT + C_W, 2, WS_FW + fi * 4, tag, lane);
      STAMP(2);
    }
    BAR();
    const unsigned tag = lds_tag();
    {
      const int grp = wave >> 2, cw = wave & 3, ci = 2 * grp + (cw >> 1), eh = cw & 1;
      if (cw < 2) {
        poll_words4(ws, WS_F1 + lane * 16, tag);
        conv_load_u(ws, conv_lds_extra(2 * grp, 0).ua, conv_lds_extra(2 * grp + 1, 0).ua, dA + 2 * grp, cw, lane);
      } else {
        kg_copies(conv_lds_extra(2 * grp + cw - 2, 0), lane);
      }
      BAR();
      STAMP(3);
      conv_channel(ws, conv_lds_extra(ci, eh), conv_lds_extra(ci, eh ^ 1).red, dA + ci, eh, u2f(lds32(CNT + 80 + ci * 8)), u2f(lds32(CNT + 84 + ci * 8)),
                   CNT + (grp ? C_X1 : C_X0), 4, lane);
      WAITV(0);
      STAMP(4);
    }
    if (wave == 4 || wave == 5) poll_words(ws, WS_FW + (lane & 31) * 4, true, tag);
    BAR();
  }

  const unsigned tag = lds_tag();
  if (wave < 4) {
    const int mh = wave & 1, kh = wave >> 1, g = lane >> 4;
    const int tok = 64 * tt + 32 * mh + 16 * kh + (lane & 15);
    v2u xv[5], bv[5];
#pragma unroll
    for (int n = 0; n < 5; ++n) {
      xv[n] = __builtin_amdgcn_raw_buffer_load_b64(xr, tok * 1280 + (80 * ct + 16 * n + 4 * g) * 2, 0, 0);
      bv[n] = *(const GAS v2u*)(p.b2 + 80 * ct + 16 * n + 4 * g);
    }
    poll_words(ws, WS_F2 + ((tt >> 1) * 16 + (lane & 15)) * 4, (lane & 15) < 14, tag);
    STAMP(6);
    constexpr int W = 12;
    v4u H[W][2], A[2][5];
    f4v acc[2][5] = {};
    const unsigned hb = WS_H + (4 * tt + 2 * mh) * 55296 + 27 * kh * 1024 + lane * 16;
#pragma unroll
    for (int k = 0; k < W; ++k)
#pragma unroll
      for (int ti = 0; ti < 2; ++ti) H[k][ti] = __builtin_amdgcn_raw_buffer_load_b128(ws, hb + (ti ^ kh) * 55296 + k * 1024, 0, SC1);
    SCHED();
    const unsigned ab = kh * 15360 + lane * 16;
    auto rd = [&](int buf, int k) {
#pragma unroll
      for (int n = 0; n < 5; ++n) A[buf][n] = lds128(ab + ((k / 3) % 5) * RING2 + (k % 3) * 5120 + n * 1024);
    };
    BAR();
    STAMP(7);
    rd(0, 0);
#pragma unroll
    for (int k = 0; k < 27; ++k) {
      if (k < 26) {
        if ((k + 1) % 3 == 0) BAR();
        rd((k + 1) & 1, k + 1);
      }
#pragma unroll
      for (int ti = 0; ti < 2; ++ti)
#pragma unroll
        for (int n = 0; n < 5; ++n) acc[ti][n] = mfma16b(A[k & 1][n], H[k % W][ti], acc[ti][n]);
      if (k + W < 27) {
#pragma unroll
        for (int ti = 0; ti < 2; ++ti) H[k % W][ti] = __builtin_amdgcn_raw_buffer_load_b128(ws, hb + (ti ^ kh) * 55296 + (k + W) * 1024, 0, SC1);
      }
      SCHED();
    }
    STAMP(8);
#pragma unroll
    for (int n = 0; n < 5; ++n) sts128(wave * 5120 + n * 1024 + lane * 16, __builtin_bit_cast(v4u, acc[1][n]));
    BAR();
    STAMP(9);
    const unsigned sa = 4 * RING2 + (4 * g + ((lane & 15) >> 2)) * 160 + (32 * mh + 16 * kh + 4 * (lane & 3)) * 2;
#pragma unroll
    for (int n = 0; n < 5; ++n) {
      const f4v o = __builtin_bit_cast(f4v, lds128((wave ^ 2) * 5120 + n * 1024 + lane * 16));
      const v2u sv = ldstr(sa + n * 2560);
      const float bz[4] = {blo(bv[n].x), bhi(bv[n].x), blo(bv[n].y), bhi(bv[n].y)};
      const float sz[4] = {blo(sv.x), bhi(sv.x), blo(sv.y), bhi(sv.y)};
      const float xz[4] = {blo(xv[n].x), bhi(xv[n].x), blo(xv[n].y), bhi(xv[n].y)};
      float r[4];
#pragma unroll
      for (int i = 0; i < 4; ++i) {
        const float z = acc[0][n][i] + o[i] + bz[i];
        const float gt = z * __builtin_amdgcn_rcpf(1.f + __builtin_amdgcn_exp2f(z * -1.44269504088896f));
        r[i] = fmaf(sz[i], gt, xz[i]);
      }
      *(GAS v2u*)(p.out + tok * 640 + 80 * ct + 16 * n + 4 * g) = (v2u){pkbf(r[0], r[1]), pkbf(r[2], r[3])};
    }
    STAMP(10);
  } else {
    const int cw = wave - 4, ci = cw >> 1, eh = cw & 1;
    if (cw < 2) {
      w2_chunk(wss, ct, cw, 0, lane); w2_chunk(wss, ct, cw, 1, lane); w2_chunk(wss, ct, cw, 2, lane);
    } else {
      poll_words4(ws, WS_F1 + lane * 16, tag);
      if (blockIdx.x == 0 && wave == 6 && lane == 0) __builtin_amdgcn_raw_buffer_store_b32(lds32(CNT + 124) + 1u, ws, WS_EPOCH, 0, SC1);
      conv_load_u(ws, conv_lds_main(0, 0).ua, conv_lds_main(1, 0).ua, d0, cw - 2, lane);
      kg_copies(conv_lds_main(cw - 2, 0), lane);
    }
    STAMP(6);
    group_sync(CNT + C_G0, 4, lane);
    STAMP(7);
    conv_channel(ws, conv_lds_main(ci, eh), conv_lds_main(ci, eh ^ 1).red, d0 + ci, eh, u2f(lds32(CNT + 64 + ci * 8)), u2f(lds32(CNT + 68 + ci * 8)),
                 CNT + C_G1, 4, lane);
    STAMP(8);
    publish(ws, CNT + C_S, 4, WS_F3 + tb * 4, tag, lane);
    group_sync(CNT + C_G2, 4, lane);
    STAMP(9);
    if (cw < 2) {
      w2_chunk(wss, ct, cw, 3, lane); w2_chunk(wss, ct, cw, 4, lane);
      BAR();
      BAR(); w2_chunk(wss, ct, cw, 5, lane);
      BAR(); w2_chunk(wss, ct, cw, 6, lane);
      WAITV(45); BAR(); w2_chunk(wss, ct, cw, 7, lane);
      WAITV(45); BAR(); w2_chunk(wss, ct, cw, 8, lane);
      WAITV(45); BAR();
      WAITV(30); BAR();
      WAITV(15); BAR();
      WAITV(0); BAR();
      BAR();
    } else {
      const int aw = cw - 2;
      BAR(); BAR(); BAR(); BAR(); BAR(); BAR(); BAR();
      poll_words(ws, WS_F3 + (ct * 32 + (lane & 31)) * 4, true, tag);
      v4u sv[5];
#pragma unroll
      for (int i = 0; i < 5; ++i)
        sv[i] = __builtin_amdgcn_raw_buffer_load_b128(ws, WS_ST + (80 * ct + 40 * aw + 8 * i + (lane >> 3)) * 4096 + tt * 128 + (lane & 7) * 16, 0, SC1);
      BAR(); BAR();
#pragma unroll
      for (int i = 0; i < 5; ++i) sts128(4 * RING2 + (40 * aw + 8 * i + (lane >> 3)) * 160 + (lane & 7) * 16, sv[i]);
      STAMP(10);
      BAR();
    }
  }
}

}

extern "C" void kernel_launch(void* const* d_in, const int* in_sizes, int n_in, void* d_out, int out_size, void* d_ws, size_t ws_size,
                              hipStream_t stream) {
  Params p;
  p.x = (const unsigned short*)d_in[0];
  p.lA = (const unsigned short*)d_in[1];
  p.Ai = (const unsigned short*)d_in[2];
  p.Cr = (const unsigned short*)d_in[3];
  p.Ci = (const unsigned short*)d_in[4];
  p.ldt = (const unsigned short*)d_in[5];
  p.Dsk = (const unsigned short*)d_in[6];
  p.w1 = (const unsigned short*)d_in[8];
  p.b1 = (const unsigned short*)d_in[9];
  p.w2 = (const unsigned short*)d_in[10];
  p.b2 = (const unsigned short*)d_in[11];
  p.out = (unsigned short*)d_out;
  p.ws = (unsigned char*)d_ws;
  (void)hipFuncSetAttribute((const void*)fused_kernel, hipFuncAttributeMaxDynamicSharedMemorySize, LDS_BYTES);
  fused_kernel<<<256, 512, LDS_BYTES, stream>>>(p);
}
```

```cpp
#include <hip/hip_runtime.h>
#include <hip/hip_bf16.h>

namespace {

typedef unsigned v4u __attribute__((ext_vector_type(4)));
typedef unsigned v2u __attribute__((ext_vector_type(2)));
typedef short s4v __attribute__((ext_vector_type(4)));
typedef float f2v __attribute__((ext_vector_type(2)));
typedef float f4v __attribute__((ext_vector_type(4)));
typedef float f16v __attribute__((ext_vector_type(16)));
typedef _Float16 h2v __attribute__((ext_vector_type(2)));
typedef _Float16 h8v __attribute__((ext_vector_type(8)));
typedef __bf16 b2v __attribute__((ext_vector_type(2)));
typedef __bf16 b8v __attribute__((ext_vector_type(8)));
#define GAS __attribute__((address_space(1)))
#define LAS __attribute__((address_space(3)))
#define DEV __device__ __forceinline__
typedef __amdgpu_buffer_rsrc_t rsrc_t;

constexpr int L_ = 2048, D_ = 640, F_ = 1728;
constexpr unsigned X_BYTES = L_ * D_ * 2, W_BYTES = D_ * F_ * 2;
constexpr int SC1 = 16;
constexpr unsigned SPIN_CAP = 1u << 16;

constexpr unsigned WS_EPOCH = 0;
constexpr unsigned WS_F1 = 4096;
constexpr unsigned WS_F2 = 65536;
constexpr unsigned WS_F3 = 16384;
constexpr unsigned WS_STAT = 32768;
constexpr unsigned WS_XNT = 131072;
constexpr unsigned WS_W2T = 2752512;
constexpr unsigned WS_H = 5242880;
constexpr unsigned WS_ST = 12582912;
constexpr unsigned WS_STAMP = 15204352;
constexpr unsigned WS_BYTES = WS_STAMP + 256 * 12 * 64 * 8;

constexpr unsigned LDS_BYTES = 163840;
constexpr unsigned UNIT = 16384;
constexpr unsigned KSCR = 8704;
constexpr unsigned FPOS = 2576;
constexpr unsigned GRP = 7 * UNIT;
constexpr unsigned LNST = GRP + 4 * KSCR;
constexpr unsigned CNT = LNST + 10752;
constexpr unsigned SEXC = CNT + 512;
constexpr unsigned JUNK = CNT + 1024;
constexpr unsigned RSTG = 6 * UNIT;
constexpr unsigned UCP = 2368;
constexpr unsigned CH_R3 = 4 * UCP, CH_U = 9856, CH_BYTES = 2 * KSCR;
constexpr unsigned RING2 = 30720;
constexpr unsigned STILE = 4 * RING2, RED2 = STILE + 12800;
constexpr unsigned E_FULL = CNT, E_DONE = CNT + 80;
constexpr unsigned E_GELU = CNT + 160, E_LN = CNT + 164, E_LNPUB = CNT + 168, E_SRDY = CNT + 172, E_RED = CNT + 176, E_SX = CNT + 192;
constexpr unsigned C_H = CNT + 208, C_F1 = CNT + 212, E_F1OK = CNT + 216, E_CONV = CNT + 220, E_VMQ = CNT + 76;
constexpr unsigned C_GRP = CNT + 224;
constexpr unsigned W_TAG = CNT + 368;
constexpr int N_ZERO = 92;
constexpr unsigned CODE_KIB = 42;

struct Params {
  const unsigned short *x, *lA, *Ai, *Cr, *Ci, *ldt, *Dsk, *w1, *b1, *w2, *b2;
  unsigned short* out;
  unsigned char* ws;
};

#define STAMP(i) do { } while (0)
#define STAMPC(i) do { } while (0)
#define STAMPV(i, v) do { } while (0)
#define STAMP_INIT() do { } while (0)
#define STAMP_FLUSH() do { } while (0)

#define PRIO(x) __builtin_amdgcn_s_setprio(x)
#define PACE0 4
#define APRIO 0
#define LT0 t0
#define LF0 f0
#undef TACC
#define TACC(acc, stmt) do { stmt; } while (0)
#define LPRIO 1
#define BAR() asm volatile("s_waitcnt lgkmcnt(0)\n\ts_barrier" ::: "memory")
#define WAITV(n) asm volatile("s_waitcnt vmcnt(" #n ")" ::: "memory")
#define SCHED() __builtin_amdgcn_sched_barrier(0)

template <int I> struct IC { constexpr operator int() const { return I; } };
template <int N, int I = 0, class F> DEV void static_for(F&& f) {
  if constexpr (I < N) { f(IC<I>{}); static_for<N, I + 1>(f); }
}
DEV float u2f(unsigned u) { return __builtin_bit_cast(float, u); }
DEV unsigned f2u(float f) { return __builtin_bit_cast(unsigned, f); }
DEV h2v u2h(unsigned u) { return __builtin_bit_cast(h2v, u); }
DEV float blo(unsigned u) { return u2f(u << 16); }
DEV float bhi(unsigned u) { return u2f(u & 0xffff0000u); }
DEV unsigned pkbf(float a, float b) { return __builtin_bit_cast(unsigned, __builtin_convertvector((f2v){a, b}, b2v)); }
DEV unsigned pkh(float a, float b) { return __builtin_bit_cast(unsigned, __builtin_convertvector((f2v){a, b}, h2v)); }
DEV float opq(float x) { asm("" : "+v"(x)); return x; }
DEV float bfr(float a) { return blo(pkbf(a, a)); }
DEV f16v mfma32b(v4u a, v4u b, f16v c) { return __builtin_amdgcn_mfma_f32_32x32x16_bf16(__builtin_bit_cast(b8v, a), __builtin_bit_cast(b8v, b), c, 0, 0, 0); }
DEV f16v mfma32h(v4u a, v4u b, f16v c) { return __builtin_amdgcn_mfma_f32_32x32x16_f16(__builtin_bit_cast(h8v, a), __builtin_bit_cast(h8v, b), c, 0, 0, 0); }
DEV f4v mfma16b(v4u a, v4u b, f4v c) { return __builtin_amdgcn_mfma_f32_16x16x32_bf16(__builtin_bit_cast(b8v, a), __builtin_bit_cast(b8v, b), c, 0, 0, 0); }
DEV f4v mfma16h(v4u a, v4u b, f4v c) { return __builtin_amdgcn_mfma_f32_16x16x32_f16(__builtin_bit_cast(h8v, a), __builtin_bit_cast(h8v, b), c, 0, 0, 0); }

DEV LAS unsigned char* lp(unsigned a) { return (LAS unsigned char*)(size_t)a; }
DEV v4u lds128(unsigned a) { return *(LAS v4u*)lp(a); }
DEV v2u lds64(unsigned a) { return *(LAS v2u*)lp(a); }
DEV unsigned lds32(unsigned a) { return *(LAS unsigned*)lp(a); }
DEV void sts128(unsigned a, v4u v) { *(LAS v4u*)lp(a) = v; }
DEV void sts64(unsigned a, v2u v) { *(LAS v2u*)lp(a) = v; }
DEV void sts32(unsigned a, unsigned v) { *(LAS unsigned*)lp(a) = v; }
DEV v2u ldstr(unsigned a) { return __builtin_bit_cast(v2u, __builtin_amdgcn_ds_read_tr16_b64_v4i16((LAS s4v*)lp(a))); }
DEV v4u ldstr2(unsigned a0, unsigned a1) { const v2u x = ldstr(a0), y = ldstr(a1); return (v4u){x.x, x.y, y.x, y.y}; }

DEV v4u make_srd(const void* ptr, unsigned bytes) {
  return (v4u){(unsigned)(size_t)ptr, (unsigned)((size_t)ptr >> 32) & 0xffffu, bytes, 0x00020000u};
}
DEV rsrc_t make_rsrc(const void* ptr, unsigned bytes) { return __builtin_amdgcn_make_buffer_rsrc((void*)ptr, 0, bytes, 0x00020000); }
#define DMA_HEAD "s_mov_b32 m0, %0\n\ts_nop 0\n\t"
DEV void dma1(v4u srd, unsigned v, unsigned soff, unsigned lds) {
  asm volatile(DMA_HEAD "buffer_load_dwordx4 %1, %2, %3 offen lds" ::"s"(lds), "v"(v), "s"(srd), "s"(soff) : "memory");
}
template <int OFF> DEV void dma_next(v4u srd, unsigned v, unsigned soff) {
  asm volatile("buffer_load_dwordx4 %0, %1, %2 offen offset:%3 lds" ::"v"(v), "s"(srd), "s"(soff), "i"(OFF) : "memory");
}
DEV void dma4(v4u srd, const unsigned (&v)[4], unsigned soff, unsigned lds) {
  asm volatile(DMA_HEAD "buffer_load_dwordx4 %1, %5, %6 offen lds\n\tbuffer_load_dwordx4 %2, %5, %6 offen offset:1024 lds\n\t"
               "buffer_load_dwordx4 %3, %5, %6 offen offset:2048 lds\n\tbuffer_load_dwordx4 %4, %5, %6 offen offset:3072 lds"
               ::"s"(lds), "v"(v[0]), "v"(v[1]), "v"(v[2]), "v"(v[3]), "s"(srd), "s"(soff) : "memory");
}
DEV void dma4_lin_sc1(v4u srd, unsigned v, unsigned soff, unsigned lds) {
  asm volatile(DMA_HEAD "buffer_load_dwordx4 %1, %2, %3 offen sc1 lds\n\tbuffer_load_dwordx4 %1, %2, %3 offen offset:1024 sc1 lds\n\t"
               "buffer_load_dwordx4 %1, %2, %3 offen offset:2048 sc1 lds\n\tbuffer_load_dwordx4 %1, %2, %3 offen offset:3072 sc1 lds"
               ::"s"(lds), "v"(v), "s"(srd), "s"(soff) : "memory");
}

DEV void poll_words(rsrc_t ws, unsigned off, unsigned tag) {
#pragma unroll 1
  for (unsigned s = 0; s < SPIN_CAP; ++s) {
    asm volatile("" ::: "memory");
    if (__all(__builtin_amdgcn_raw_buffer_load_b32(ws, off, 0, SC1) == tag)) break;
  }
  asm volatile("" ::: "memory");
}
DEV void poll_words4(rsrc_t ws, unsigned off, unsigned tag) {
#pragma unroll 1
  for (unsigned s = 0; s < SPIN_CAP; ++s) {
    asm volatile("" ::: "memory");
    const v4u v = __builtin_amdgcn_raw_buffer_load_b128(ws, off, 0, SC1);
    if (__all(v.x == tag && v.y == tag && v.z == tag && v.w == tag)) break;
    __builtin_amdgcn_s_sleep(8);
  }
  asm volatile("" ::: "memory");
}
DEV void signal(unsigned a) {
  asm volatile("s_mov_b64 exec, 1\n\tds_add_u32 %0, %1\n\ts_mov_b64 exec, -1" ::"v"(a), "v"(1u) : "memory");
}
template <int OFF> DEV void signal_at(unsigned base, unsigned one) {
  asm volatile("s_mov_b64 exec, 1\n\tds_add_u32 %0, %1 offset:%2\n\ts_mov_b64 exec, -1" ::"v"(base), "v"(one), "i"(OFF) : "memory");
}
DEV unsigned peek(unsigned a) { return *(volatile LAS unsigned*)lp(a); }
template <int SLEEP = 1> DEV void await(unsigned a, unsigned need) {
#pragma unroll 1
  for (unsigned s = 0; s < (SPIN_CAP << 2); ++s) {
    asm volatile("" : "+s"(s));
    if (__builtin_amdgcn_readfirstlane(peek(a)) >= need) break;
    __builtin_amdgcn_s_sleep(SLEEP);
  }
  asm volatile("" ::: "memory");
}
DEV void check(unsigned early, unsigned a, unsigned need) {
  if (__builtin_expect(__builtin_amdgcn_readfirstlane(early) < need, 0)) await<1>(a, need);
  asm volatile("" ::: "memory");
}
DEV unsigned lds_arrive(unsigned cnt) {
  unsigned old;
  asm volatile("s_mov_b64 exec, 1\n\tds_add_rtn_u32 %0, %1, %2\n\ts_mov_b64 exec, -1\n\ts_waitcnt lgkmcnt(0)" : "=&v"(old) : "v"(cnt), "v"(1u) : "memory");
  return __builtin_amdgcn_readfirstlane(old);
}
template <int SLEEP = 1> DEV void group_sync(unsigned cnt, unsigned target) {
  if (lds_arrive(cnt) + 1 < target) await<SLEEP>(cnt, target);
}
DEV void publish(rsrc_t ws, unsigned cnt, unsigned nwaves, unsigned flag_off, unsigned tag, int lane) {
  WAITV(0);
  if (lds_arrive(cnt) + 1 == nwaves && lane == 0) __builtin_amdgcn_raw_buffer_store_b32(tag, ws, flag_off, 0, SC1);
}
DEV unsigned epoch_load(rsrc_t ws) { return __builtin_amdgcn_raw_buffer_load_b32(ws, WS_EPOCH, 0, SC1); }
DEV unsigned epoch_tag(unsigned ev, bool share, int lane) {
  const unsigned e = __builtin_amdgcn_readfirstlane(ev);
  const unsigned tag = e * 0x9E3779B1u + 0x7F4A7C15u;
  if (share && lane == 0) sts64(W_TAG, (v2u){tag, e});
  return tag;
}
DEV unsigned lds_tag() { return __builtin_amdgcn_readfirstlane(lds32(W_TAG)); }

template <int CTRL> DEV float dpp_add(float v) {
  return v + u2f(__builtin_amdgcn_update_dpp(0u, f2u(v), CTRL, 0xf, 0xf, false));
}
template <int CTRL> DEV float dpp_max(float v) {
  return fmaxf(v, u2f(__builtin_amdgcn_update_dpp(0u, f2u(v), CTRL, 0xf, 0xf, false)));
}
DEV float half_sum(float v) {
  v = dpp_add<0x128>(v); v = dpp_add<0x124>(v); v = dpp_add<0x122>(v); v = dpp_add<0x121>(v);
  const auto r = __builtin_amdgcn_permlane16_swap(f2u(v), f2u(v), false, false);
  return u2f(r[0]) + u2f(r[1]);
}
DEV float wave_max(float v) {
  v = dpp_max<0x128>(v); v = dpp_max<0x124>(v); v = dpp_max<0x122>(v); v = dpp_max<0x121>(v);
  const auto r = __builtin_amdgcn_permlane16_swap(f2u(v), f2u(v), false, false);
  v = fmaxf(u2f(r[0]), u2f(r[1]));
  const auto q = __builtin_amdgcn_permlane32_swap(f2u(v), f2u(v), false, false);
  return fmaxf(u2f(q[0]), u2f(q[1]));
}

struct LnIn { v2u v[5]; };
DEV LnIn ln_load(rsrc_t xr, int token, int lane) {
  LnIn r;
#pragma unroll
  for (int m = 0; m < 5; ++m) r.v[m] = __builtin_amdgcn_raw_buffer_load_b64(xr, token * 1280 + (lane & 31) * 8 + m * 256, 0, 0);
  return r;
}
DEV void ln_compute(const LnIn& in, rsrc_t ws, int token, int row, unsigned tag, int lane) {
  float xv[20], s = 0.f, q = 0.f;
#pragma unroll
  for (int m = 0; m < 5; ++m) { xv[4 * m] = blo(in.v[m].x); xv[4 * m + 1] = bhi(in.v[m].x); xv[4 * m + 2] = blo(in.v[m].y); xv[4 * m + 3] = bhi(in.v[m].y); }
#pragma unroll
  for (int m = 0; m < 20; ++m) { s = opq(s + xv[m]); q = opq(fmaf(xv[m], xv[m], q)); }
  s = half_sum(s); q = half_sum(q);
  const float mean = s * (1.f / 640.f);
  const float var = fmaxf(fmaf(-mean, mean, q * (1.f / 640.f)), 0.f);
  const float m1 = bfr(mean), s1 = bfr(__builtin_amdgcn_sqrtf(bfr(bfr(var) + 1e-5f)));
  const float r1 = __builtin_amdgcn_rcpf(s1);
  const float m2 = bfr((mean - m1) * r1), s2 = bfr(__builtin_amdgcn_sqrtf(bfr(bfr(var * r1 * r1) + 1e-5f)));
  if ((lane & 31) == 0)
    __builtin_amdgcn_raw_buffer_store_b128((v4u){f2u(fmaf(m2, s1, m1)), tag, f2u(s1 * s2), tag}, ws, WS_STAT + token * 16, 0, SC1);
  const float c = -m1 * r1;
#pragma unroll
  for (int m = 0; m < 5; ++m) {
    const v2u o = {pkh(opq(fmaf(xv[4 * m], r1, c)), opq(fmaf(xv[4 * m + 1], r1, c))), pkh(opq(fmaf(xv[4 * m + 2], r1, c)), opq(fmaf(xv[4 * m + 3], r1, c)))};
    sts64(LNST + row * 1344 + (lane & 31) * 8 + m * 256, o);
  }
}
DEV void ln_publish(rsrc_t ws, int tb, int aw, int lane) {
  const unsigned a = LNST + ((lane & 15) >> 2) * 1344 + (lane >> 4) * 32 + (lane & 3) * 8 + aw * 640;
#pragma unroll
  for (int i = 0; i < 5; ++i) {
    const v4u v = ldstr2(a + i * 128, a + i * 128 + 4 * 1344);
    __builtin_amdgcn_raw_buffer_store_b128(v, ws, WS_XNT + tb * 10240 + (aw * 320 + i * 64 + lane) * 16, 0, SC1);
  }
}

struct Cx { float r, i; };
DEV Cx cmul(Cx a, Cx b) { return {opq(fmaf(a.r, b.r, -a.i * b.i)), opq(fmaf(a.r, b.i, a.i * b.r))}; }
DEV Cx csqr(Cx a) { return {opq(fmaf(a.r, a.r, -a.i * a.i)), opq((a.r + a.r) * a.i)}; }
DEV unsigned ch(Cx a) { return pkh(a.r, a.i); }
DEV unsigned hmul(unsigned a, unsigned b) {
  unsigned t, r;
  asm("v_pk_mul_f16 %0, %1, %2 op_sel:[0,0] op_sel_hi:[0,1]" : "=v"(t) : "v"(a), "v"(b));
  asm("v_pk_fma_f16 %0, %1, %2, %3 op_sel:[1,1,0] op_sel_hi:[1,0,1] neg_lo:[0,1,0]" : "=v"(r) : "v"(a), "v"(b), "v"(t));
  return r;
}
DEV float exp_bf(float x) {
  const float t = x * 1.44268798828125f, lo = x * 7.0526077138765e-06f;
  const float n = __builtin_rintf(t);
  return bfr(__builtin_amdgcn_ldexpf(__builtin_amdgcn_exp2f((t - n) + lo), (int)fminf(fmaxf(n, -200.f), 200.f)));
}
DEV Cx cexp_at(float lg, float rh, float rl, float P) {
  const float m = __builtin_amdgcn_exp2f(lg * P), ph = fmaf(rl, P, __builtin_amdgcn_fractf(rh * P));
  return {opq(m * __builtin_amdgcn_cosf(ph)), opq(m * __builtin_amdgcn_sinf(ph))};
}
struct KgIn { unsigned lA, Ai, Cr, Ci; float dt, dsk; };
DEV KgIn kg_load(const Params& p, int d, int lane) {
  KgIn k;
  k.lA = ((const GAS unsigned*)p.lA)[d * 64 + lane]; k.Ai = ((const GAS unsigned*)p.Ai)[d * 64 + lane];
  k.Cr = ((const GAS unsigned*)p.Cr)[d * 64 + lane]; k.Ci = ((const GAS unsigned*)p.Ci)[d * 64 + lane];
  k.dt = u2f((unsigned)((const GAS unsigned short*)p.ldt)[d] << 16);
  k.dsk = u2f((unsigned)((const GAS unsigned short*)p.Dsk)[d] << 16);
  return k;
}
DEV float kg_wave(const KgIn& k, int g, unsigned scr, unsigned fp, unsigned fp_free, int lane) {
  const float dt = exp_bf(k.dt), c2 = dt + dt;
  const float mx = wave_max(fmaxf(fmaxf(fabsf(blo(k.Cr)), fabsf(bhi(k.Cr))), fmaxf(fabsf(blo(k.Ci)), fabsf(bhi(k.Ci))))) * c2;
  int ex = __builtin_amdgcn_frexp_expf(mx);
  ex = ex < -120 ? -120 : (ex > 120 ? 120 : ex);
  const float cs = c2 * __builtin_amdgcn_ldexpf(1.f, -ex);
  const int sh = g ? 0 : 16;
  const float lg = -(exp_bf(bhi(k.lA << sh)) * dt) * 1.44269504088896f, ai = bhi(k.Ai << sh) * dt;
  const Cx c0 = {opq(bhi(k.Cr << sh) * cs), opq(-bhi(k.Ci << sh) * cs)};
  const float rh = ai * 0.15915494f, rl = fmaf(ai, 0.15915494f, -rh) + ai * 6.4206382e-09f;
  const Cx v1 = cexp_at(lg, -rh, -rl, 1.f), w1 = cexp_at(lg, rh, rl, 8.f), G = cexp_at(lg, -rh, -rl, 256.f);
  const unsigned wa = scr + lane * 4, ra = scr + (lane & 31) * 272 + (lane >> 5) * 16;
  {
    const Cx w2 = csqr(w1), w3 = cmul(w2, w1), w4 = csqr(w2), w8 = csqr(w4), w12 = cmul(w8, w4), w16 = csqr(w8);
    const Cx w20 = cmul(w16, w4), w24 = csqr(w12), w28 = cmul(w24, w4);
    const unsigned o[3] = {ch(w1), ch(w2), ch(w3)};
    const unsigned bs[7] = {ch(w4), ch(w8), ch(w12), ch(w16), ch(w20), ch(w24), ch(w28)};
    sts32(wa, 0x00003c00u);
#pragma unroll
    for (int j = 0; j < 3; ++j) sts32(wa + (1 + j) * 272, o[j]);
#pragma unroll
    for (int b = 0; b < 7; ++b) {
      sts32(wa + (4 + 4 * b) * 272, bs[b]);
#pragma unroll
      for (int j = 0; j < 3; ++j) sts32(wa + (5 + 4 * b + j) * 272, hmul(bs[b], o[j]));
    }
  }
  asm volatile("" ::: "memory");
  v4u y[8];
#pragma unroll
  for (int s = 0; s < 8; ++s) y[s] = lds128(ra + s * 32);
  asm volatile("" ::: "memory");
  {
    const Cx v2 = csqr(v1), v3 = cmul(v2, v1), v4 = csqr(v2), v6 = csqr(v3), x4 = cmul(c0, v4);
    const unsigned h1 = ch(v1), h2 = ch(v2), h3 = ch(v3), h4 = ch(v4), h6 = ch(v6), hx0 = ch(c0), hx4 = ch(x4);
    sts32(wa + 0 * 272, hx0); sts32(wa + 1 * 272, hmul(hx0, h1)); sts32(wa + 2 * 272, hmul(hx0, h2)); sts32(wa + 3 * 272, hmul(hx0, h3));
    sts32(wa + 4 * 272, hx4); sts32(wa + 5 * 272, hmul(hx4, h1)); sts32(wa + 6 * 272, hmul(hx4, h2)); sts32(wa + 7 * 272, hmul(hx4, h3));
    const Cx d1 = cmul(c0, G), d2 = cmul(d1, G), d3 = cmul(d2, G), d4 = cmul(d3, G);
    const Cx G4 = csqr(csqr(G));
    const Cx d5 = cmul(d1, G4), d6 = cmul(d2, G4), d7 = cmul(d3, G4), d8 = cmul(d4, G4);
    const unsigned e1 = ch(d1), e2 = ch(d2), e3 = ch(d3);
    sts32(wa + 8 * 272, e1); sts32(wa + 9 * 272, hmul(e1, h2)); sts32(wa + 10 * 272, hmul(e1, h4)); sts32(wa + 11 * 272, hmul(e1, h6));
    sts32(wa + 12 * 272, e2); sts32(wa + 13 * 272, hmul(e2, h4)); sts32(wa + 14 * 272, e3); sts32(wa + 15 * 272, hmul(e3, h4));
    sts32(wa + 16 * 272, ch(d4)); sts32(wa + 17 * 272, ch(d5)); sts32(wa + 18 * 272, ch(d6)); sts32(wa + 19 * 272, ch(d7));
    sts32(wa + 20 * 272, ch(d8));
  }
  asm volatile("" ::: "memory");
  v4u xx[8];
#pragma unroll
  for (int s = 0; s < 8; ++s) xx[s] = lds128(ra + s * 32);
  SCHED();
  f16v acc = {};
#pragma unroll
  for (int s = 0; s < 8; ++s) acc = mfma32h(xx[s], y[s], acc);
  const int j = lane & 31, h = lane >> 5;
  await(E_LNPUB, fp_free);
  sts128(fp + (8 * j + 4 * h) * 4, (v4u){f2u(acc[0]), f2u(acc[1]), f2u(acc[2]), f2u(acc[3])});
  sts64(fp + (h ? 384 + 2 * j : 256 + 4 * j) * 4, (v2u){f2u(acc[4]), f2u(acc[5])});
  sts64(fp + (h ? 448 + 2 * j : 258 + 4 * j) * 4, (v2u){f2u(acc[6]), f2u(acc[7])});
  if (lane <= 32) sts32(fp + (512 + 128 * h + j) * 4, f2u(acc[8]));
  if (h == 0) {
#pragma unroll
    for (int i = 1; i < 4; ++i) sts32(fp + (512 + j + 32 * i) * 4, f2u(acc[8 + i]));
  }
  return __builtin_amdgcn_ldexpf(1.f, ex);
}
DEV void kg_table0(unsigned cb, unsigned fp, int g, int lane) {
  auto f1 = [&](int i) { return opq(u2f(lds32(fp + i * 4)) + u2f(lds32(fp + FPOS + i * 4))); };
  if (g == 0) {
    const int j = lane & 31, h = lane >> 5;
    const unsigned a = fp + (h ? 256 + 4 * j : 8 * j) * 4;
    const v4u l0 = lds128(a), h0 = lds128(a + 16), l1 = lds128(a + FPOS), h1 = lds128(a + FPOS + 16);
    const float e0 = opq(u2f(l0.x) + u2f(l1.x)), e1 = opq(u2f(l0.y) + u2f(l1.y)), e2 = opq(u2f(l0.z) + u2f(l1.z)), e3 = opq(u2f(l0.w) + u2f(l1.w));
    const float e4 = opq(u2f(h0.x) + u2f(h1.x)), e5 = opq(u2f(h0.y) + u2f(h1.y)), e6 = opq(u2f(h0.z) + u2f(h1.z)), e7 = opq(u2f(h0.w) + u2f(h1.w));
    const v4u o0 = {pkh(e0, e1), pkh(e2, e3), pkh(e4, e5), pkh(e6, e7)};
    const v4u o1 = {pkh(e0, e0), pkh(e1, e2), pkh(e2, e2), pkh(e3, e4)};
    sts128(cb + 32 + lane * 16, h ? o1 : o0);
    const float fa = f1(512), fb = f1(513);
    const unsigned aa = pkh(fa, fa), bb = pkh(fb, fb);
    if (lane < 4) sts128(cb + 32 + (128 + lane) * 16, lane == 0 ? (v4u){aa, aa, pkh(fa, fb), bb} : lane == 1 ? (v4u){bb, bb, 0u, 0u} : (v4u){0u, 0u, 0u, 0u});
  } else {
    const float a = f1(384 + 2 * lane), b = f1(385 + 2 * lane), c = f1(386 + 2 * lane);
    sts128(cb + 32 + (64 + lane) * 16, (v4u){pkh(a, a), pkh(a, b), pkh(b, b), pkh(c, c)});
    const float lo = f1(640 - 2 * lane), hi = f1(lane ? 641 - 2 * lane : 640);
    sts32(cb + CH_R3 + lane * 4, pkh(lane ? hi : 0.f, lo));
    if (lane < 16) sts32(cb + CH_R3 + 256 + lane * 4, 0u);
    if (lane < 32) sts32(cb + (lane >> 3) * UCP + (lane & 7) * 4, 0u);
  }
}
DEV void kg_copies(unsigned cb, int g, int lane) {
  auto one = [&](int chunk) {
    const unsigned o = cb + 32 + chunk * 16;
    const v2u pv = lds64(o - 8);
    const v4u e = lds128(o);
    const unsigned a0 = __builtin_amdgcn_alignbit(pv.y, pv.x, 16), a1 = __builtin_amdgcn_alignbit(e.x, pv.y, 16), a2 = __builtin_amdgcn_alignbit(e.y, e.x, 16),
                   a3 = __builtin_amdgcn_alignbit(e.z, e.y, 16), a4 = __builtin_amdgcn_alignbit(e.w, e.z, 16);
    sts128(o + UCP, (v4u){a1, a2, a3, a4});
    sts128(o + 2 * UCP, (v4u){pv.y, e.x, e.y, e.z});
    sts128(o + 3 * UCP, (v4u){a0, a1, a2, a3});
  };
  one(64 * g + lane);
  if (lane < 2) one(128 + 2 * g + lane);
}
DEV void conv_load_u(rsrc_t ws, unsigned grp, int d0, int w, int lane) {
  v4u v[2];
#pragma unroll
  for (int i = 0; i < 2; ++i)
    v[i] = __builtin_amdgcn_raw_buffer_load_b128(ws, (lane >> 1) * 10240 + (lane & 1) * 16, WS_XNT + (64 * w + 32 * i) * 10240 + d0 * 16, SC1);
  if (lane < 16) sts128(grp + (w >> 1) * CH_BYTES + CH_U + (w & 1) * 256 + lane * 16, (v4u){0u, 0u, 0u, 0u});
#pragma unroll
  for (int i = 0; i < 2; ++i) sts128(grp + (lane & 1) * CH_BYTES + CH_U + 512 + (64 * w + 32 * i + (lane >> 1)) * 16, v[i]);
}
template <class F1>
DEV float ssm_prepare(rsrc_t ws, const KgIn& k, unsigned grp, unsigned fp, unsigned fp_free, unsigned cnt, int d0, int w, int lane, F1 f1) {
  const int ci = w >> 1, g = w & 1;
  const float isc = kg_wave(k, g, grp + w * KSCR, fp + w * FPOS, fp_free, lane);
  group_sync(cnt, 4);
  kg_table0(grp + ci * CH_BYTES, fp + 2 * ci * FPOS, g, lane);
  group_sync(cnt + 4 + ci * 4, 2);
  kg_copies(grp + ci * CH_BYTES, g, lane);
  f1();
  conv_load_u(ws, grp, d0, w, lane);
  group_sync(cnt + 12, 4);
  return isc;
}

template <int B> DEV void conv_tail(f4v (&acc)[8], unsigned ua, unsigned r3, v4u w7, v4u w9) {
  const f4v b7 = mfma16h(lds128(ua + 512 * B), w7, (f4v){}), b9 = mfma16h(lds128(ua + 512 * B + 16), w9, (f4v){});
  const v4u bx = {pkh(b7[0], b9[0]), pkh(b7[1], b9[1]), pkh(b7[2], b9[2]), pkh(b7[3], b9[3])};
#pragma unroll
  for (int dl = 4; dl + B < 8; ++dl) {
    const unsigned a = r3 - 64 * dl;
    acc[dl + B] = mfma16h(bx, (v4u){lds32(a), lds32(a + 4), lds32(a + 8), lds32(a + 12)}, acc[dl + B]);
  }
}
DEV void conv_channel(rsrc_t ws, unsigned cb, unsigned cnt, int d, int eh, float isc, float dsk, int lane) {
  const int g = lane >> 4, c16 = lane & 15;
  const unsigned ga = cb + (c16 >> 2) * UCP + 64 - 16 * g + 8 * (lane & 3) + eh * 256;
  const unsigned ba = cb + CH_U + 480 + 32 * c16 + 16 * g - eh * 256;
  f4v acc[8] = {};
  v4u G[2][4], B[2][8];
#pragma unroll
  for (int i = 0; i < 8; ++i) { B[0][i] = lds128(ba + 512 * i); if (i < 4) G[0][i] = ldstr2(ga + 512 * i, ga + 512 * i - 8); }
  {
    unsigned w7[4], w9[4];
#pragma unroll
    for (int i = 0; i < 4; ++i) {
      const int k0 = 8 * g + 2 * i - c16;
      w7[i] = ((k0 >= 5 && k0 <= 11) ? 0x3c00u : 0u) | ((k0 >= 4 && k0 <= 10) ? 0x3c000000u : 0u);
      w9[i] = ((k0 >= 4 && k0 <= 12) ? 0x3c00u : 0u) | ((k0 >= 3 && k0 <= 11) ? 0x3c000000u : 0u);
    }
    const v4u W7 = {w7[0], w7[1], w7[2], w7[3]}, W9 = {w9[0], w9[1], w9[2], w9[3]};
    const unsigned ua = cb + CH_U + 448 + 32 * c16 + 16 * g, r3 = cb + CH_R3 + (127 - c16 + 4 * g) * 4;
    if (eh) {
      conv_tail<0>(acc, ua, r3, W7, W9);
      conv_tail<3>(acc, ua, r3, W7, W9);
    } else {
      conv_tail<1>(acc, ua, r3, W7, W9);
      conv_tail<2>(acc, ua, r3, W7, W9);
      const v4u g4 = ldstr2(ga + 2048, ga + 2048 - 8);
#pragma unroll
      for (int j = 4; j < 8; ++j) acc[j] = mfma16h(g4, B[0][j - 4], acc[j]);
    }
  }
  SCHED();
#pragma unroll
  for (int e = 0; e < 4; ++e) {
    int m = 0;
#pragma unroll
    for (int cc = 0; cc < 4; ++cc)
#pragma unroll
      for (int j = cc; j < 8; ++j) {
        acc[j] = mfma16h(G[e & 1][cc], B[e & 1][j - cc], acc[j]);
        if (e < 3 && (m & 1) == 0 && m < 24) {
          const int i = m >> 1;
          if (i == 1 || i > 8) { const int c = i == 1 ? 0 : i - 8; G[(e + 1) & 1][c] = ldstr2(ga + 512 * c + 64 * (e + 1), ga + 512 * c + 64 * (e + 1) - 8); }
          else { const int c = i == 0 ? 0 : i - 1; B[(e + 1) & 1][c] = lds128(ba + 512 * c - 64 * (e + 1)); }
        }
        ++m;
        SCHED();
      }
  }
  const unsigned to = 2 * (1024 * eh + 16 * c16 + 4 * g);
  v2u uu[4];
#pragma unroll
  for (int j = 0; j < 4; ++j) uu[j] = lds64(cb + CH_U + 512 + to + 512 * j);
  group_sync<1>(cnt, 2);
  f4v mine[4];
  if (eh) {
#pragma unroll
    for (int j = 0; j < 4; ++j) { sts128(cb + 4096 + j * 1024 + lane * 16, __builtin_bit_cast(v4u, acc[j])); mine[j] = acc[4 + j]; }
  } else {
#pragma unroll
    for (int j = 0; j < 4; ++j) { sts128(cb + j * 1024 + lane * 16, __builtin_bit_cast(v4u, acc[4 + j])); mine[j] = acc[j]; }
  }
  group_sync<1>(cnt + 8, 2);
#pragma unroll
  for (int j = 0; j < 4; ++j) {
    const f4v o = __builtin_bit_cast(f4v, lds128(cb + (eh ^ 1) * 4096 + j * 1024 + lane * 16));
    const h2v u0 = u2h(uu[j].x), u1 = u2h(uu[j].y);
    const float s0 = opq(fmaf(mine[j][0] + o[0], isc, dsk * (float)u0.x)), s1 = opq(fmaf(mine[j][1] + o[1], isc, dsk * (float)u0.y));
    const float s2 = opq(fmaf(mine[j][2] + o[2], isc, dsk * (float)u1.x)), s3 = opq(fmaf(mine[j][3] + o[3], isc, dsk * (float)u1.y));
    __builtin_amdgcn_raw_buffer_store_b64((v2u){pkbf(s0, s1), pkbf(s2, s3)}, ws, to + 512 * j, WS_ST + d * 4096, SC1);
  }
}

template <int PACE> DEV void w2_chunk(v4u wss, int ct, int lw, int c, int lane) {
  const unsigned po = (lw & 1) * 7168;
  const unsigned so = WS_W2T + (ct * 54 + 27 * (lw >> 1) + 3 * c) * 5120 + po, lb = (c % 5) * RING2 + (lw >> 1) * 15360 + po;
  dma4_lin_sc1(wss, lane * 16, so, lb);
  if (PACE) __builtin_amdgcn_s_sleep(PACE);
  dma4_lin_sc1(wss, lane * 16, so + 4096, lb + 4096);
  if (PACE) __builtin_amdgcn_s_sleep(PACE);
}
DEV void retile_load(const Params& p, int blk, unsigned stg, int lane) {
  const unsigned v0 = (lane >> 3) * 1280 + 16 * ((lane & 7) ^ (((lane >> 4) & 3) << 1));
  const unsigned v[4] = {v0, v0 + 9216, v0 + 2 * 9216, v0 + 3 * 9216};
  dma4(make_srd(p.w2, W_BYTES), v, (blk / 10) * 40960 + (blk % 10) * 128, stg);
}
struct Frag4 { v4u f[4]; };
DEV Frag4 retile_read(unsigned stg, int lane) {
  const int g = lane >> 4, pp = lane & 3, row = 16 * (g >> 1) + 4 * (g & 1) + ((lane & 15) >> 2);
  Frag4 r;
#pragma unroll
  for (int j = 0; j < 4; ++j) {
    const unsigned a = stg + row * 128 + 16 * ((2 * j + (pp >> 1)) ^ (((row >> 1) & 3) << 1)) + 8 * (pp & 1);
    r.f[j] = ldstr2(a, a + 1024);
  }
  asm volatile("s_waitcnt lgkmcnt(0)" ::: "memory");
  return r;
}
DEV void retile_write(rsrc_t ws, int blk, const Frag4& r, int lane) {
#pragma unroll
  for (int j = 0; j < 4; ++j) {
    const int ng = 4 * (blk % 10) + j;
    __builtin_amdgcn_raw_buffer_store_b128(r.f[j], ws, lane * 16 + WS_W2T + (((ng / 5) * 54 + blk / 10) * 5 + ng % 5) * 1024, 0, SC1);
  }
}

__global__ __launch_bounds__(768, 3) void fused_kernel(Params p) {
  extern __shared__ __attribute__((aligned(16))) unsigned char lds_base[];
  unsigned long long pc;
  asm volatile("s_getpc_b64 %0" : "=s"(pc));
  const int lane = threadIdx.x & 63, wave = __builtin_amdgcn_readfirstlane(threadIdx.x >> 6), role = wave >> 2, w = wave & 3;
  const int xcd = blockIdx.x & 7, slot = blockIdx.x >> 3, tb = 32 * xcd + slot;
  const bool gemm_wg = slot < 28;
  const int rp = 4 * (xcd >> 1) + slot / 7, cp = 7 * (xcd & 1) + slot % 7, t0 = 128 * rp, f0 = 128 * cp;
  STAMP_INIT();
  STAMP(0);
  asm volatile("" ::"v"((LAS unsigned char*)lds_base));

  const unsigned vx0 = (lane >> 3) * 1280 + 16 * ((lane & 7) ^ (lane >> 4));
  const unsigned vw0 = (lane >> 4) * 3456 + 16 * ((lane & 15) ^ ((lane >> 4) << 2));
  const unsigned vx[4] = {vx0, (vx0 ^ 64) + 9216, vx0 + 2 * 9216, (vx0 ^ 64) + 3 * 9216};
  const unsigned vw[4] = {vw0, vw0 + 12800, vw0 + 2 * 12800, vw0 + 3 * 12800};
  auto stage_x = [&](int i) { dma4(make_srd(p.x, X_BYTES), vx, (LT0 + 32 * w) * 1280 + 128 * i, ((2 * i) % 7) * UNIT + w * 4096); };
  auto stage_w = [&](int i) { dma4(make_srd(p.w1, W_BYTES), vw, (64 * i + 16 * w) * 3456 + 2 * LF0, ((2 * i + 1) % 7) * UNIT + w * 4096); };
  const rsrc_t ws = make_rsrc(p.ws, WS_BYTES), xr = make_rsrc(p.x, X_BYTES);
  unsigned ev = 0;
  LnIn ln = {};
  if (role == 1) {
    PRIO(3);
    if (gemm_wg) { stage_x(0); stage_w(0); }
    ev = epoch_load(ws);
    ln = ln_load(xr, 8 * tb + 2 * w + (lane >> 5), lane);
    if (w < 2) retile_load(p, 2 * tb + w, gemm_wg ? RSTG + w * 4096 : 98304 + w * 8192, lane);
  }
  if (wave < 2 && 2 * slot + wave < (int)CODE_KIB) dma1(make_srd((const void*)(pc & ~255ull), CODE_KIB * 1024), lane * 16, (2 * slot + wave) * 1024, JUNK);

  const v4u wss = make_srd(p.ws, WS_BYTES);
  const int dg = 80 * xcd + (gemm_wg ? 2 * slot : 56 + 6 * (slot - 28) + 2 * role);
  const int tt = 4 * xcd + (slot >> 3), ct = slot & 7;
  const unsigned fslot = (tb >> 2) * 128 + (tb & 3) * 4;
  if (threadIdx.x < N_ZERO) sts32(CNT + threadIdx.x * 4, 0u);

  if (role == 2) {
    const KgIn kin = kg_load(p, dg + (w >> 1), lane);
    BAR();
    const float isc = ssm_prepare(ws, kin, GRP, LNST, 2, C_GRP + 96, dg, w, lane, [&] { STAMP(2); await(E_F1OK, 1); STAMP(3); });
    STAMP(6);
    await(E_GELU, 4);
    STAMP(7);
    conv_channel(ws, GRP + (w >> 1) * CH_BYTES, C_GRP + 96 + 16 + (w >> 1) * 4, dg + (w >> 1), w & 1, isc, kin.dsk, lane);
    STAMP(8);
    signal(E_CONV);
    const unsigned tag = lds_tag();
    publish(ws, C_GRP + 96 + 36, 4, WS_F3 + fslot, tag, lane);
    STAMP(9);
    if (w < 2) {
      await<8>(E_DONE + 14 * 4, 4);
      poll_words4(ws, WS_F3 + (ct * 8 + (lane & 7)) * 128, tag);
      v4u sv[5];
#pragma unroll
      for (int i = 0; i < 5; ++i)
        sv[i] = __builtin_amdgcn_raw_buffer_load_b128(ws, (lane >> 3) * 4096 + (lane & 7) * 16, WS_ST + (80 * ct + 40 * w + 8 * i) * 4096 + tt * 128, SC1);
#pragma unroll
      for (int i = 0; i < 5; ++i) sts128(STILE + (40 * w + 8 * i + (lane >> 3)) * 160 + (lane & 7) * 16, sv[i]);
      signal(E_SRDY);
    }
    STAMP(10);
    STAMP_FLUSH();
    return;
  }

  auto poll_f1 = [&](unsigned tag) {
    poll_words4(ws, WS_F1 + lane * 128, tag);
    signal(E_F1OK);
    if (blockIdx.x == 0 && lane == 0) __builtin_amdgcn_raw_buffer_store_b32(lds32(W_TAG + 4) + 1u, ws, WS_EPOCH, 0, SC1);
  };
  if (role == 1) {
    const int h = lane >> 5, fi = 4 * xcd + slot - 28;
    const bool extra = !gemm_wg && w == 0 && fi < 28;
    const unsigned stg = gemm_wg ? RSTG + w * 4096 : 98304 + w * 8192;
    Frag4 rt = {};
    BAR();
    if (gemm_wg) {
      stage_x(1); stage_w(1);
      WAITV(8); signal(E_FULL);
      STAMP(1);
      stage_x(2); stage_w(2);
      if (w < 2) rt = retile_read(stg, lane);
      stage_x(3);
      WAITV(12); signal(E_FULL + 4);
      WAITV(4); signal(E_FULL + 8);
      WAITV(0); signal(E_FULL + 12);
    }
    STAMP(2);
    PRIO(LPRIO);
    if (extra) retile_load(p, 512 + fi, stg + 4096, lane);
    if (!gemm_wg && w < 2) { WAITV(0); rt = retile_read(stg, lane); }
    const unsigned tag = epoch_tag(ev, w == 0, lane);
    ln_compute(ln, ws, 8 * tb + 2 * w + h, 2 * w + h, tag, lane);
    signal(E_LN);
    if (w < 2) {
      retile_write(ws, 2 * tb + w, rt, lane);
      if (extra) retile_write(ws, 512 + fi, retile_read(stg + 4096, lane), lane);
    } else {
      await(E_LN, 4);
      ln_publish(ws, tb, w - 2, lane);
      signal(E_LNPUB);
    }
    STAMP(3);
    publish(ws, C_F1, 4, WS_F1 + fslot, tag, lane);
    STAMP(4);
    PRIO(0);
    if (gemm_wg) {
      if (w == 0) { await<4>(E_VMQ, 4); poll_f1(tag); }
      await<4>(E_F1OK, 1);
      await<4>(E_GELU, 4);
      STAMP(5);
    }
  }

  if (!gemm_wg) {
    const unsigned grp = role * (4 * KSCR + 4 * FPOS), cnt = C_GRP + role * 48;
    const KgIn kin = kg_load(p, dg + (w >> 1), lane);
    if (role == 0) BAR();
    const float isc = ssm_prepare(ws, kin, grp, grp + 4 * KSCR, 0, cnt, dg, w, lane, [&] {
      if (wave == 0) { await(E_LN, 4); poll_f1(lds_tag()); } else { await<4>(E_F1OK, 1); }
    });
    STAMP(16);
    conv_channel(ws, grp + (w >> 1) * CH_BYTES, cnt + 16 + (w >> 1) * 4, dg + (w >> 1), w & 1, isc, kin.dsk, lane);
    WAITV(0);
    group_sync(C_H, 8);
    if (role == 0) signal(E_GELU);
    STAMP(5);
  } else if (role == 0) {
    const int wf = w & 1, wt = w >> 1, h = lane >> 5;
    BAR();
    PRIO(2);
    unsigned aa[2][2], ba[2][4];
    {
      const int q = (lane & 15) >> 2, pp = lane & 3, fb = (lane >> 4) & 1;
#pragma unroll
      for (int mi = 0; mi < 2; ++mi) {
        aa[0][mi] = (8 * h + q) * 256 + 16 * ((8 * wf + 4 * (mi ^ wt) + 2 * fb + (pp >> 1)) ^ (q << 2)) + 8 * (pp & 1);
        aa[1][mi] = aa[0][mi] + 4 * UNIT;
        asm volatile("" : "+v"(aa[1][mi]));
      }
#pragma unroll
      for (int s = 0; s < 4; ++s) {
        ba[0][s] = (64 * wt + (lane & 31)) * 128 + 16 * ((2 * s + h) ^ ((lane >> 1) & 7));
        ba[1][s] = ba[0][s] + 4 * UNIT;
        asm volatile("" : "+v"(ba[1][s]));
      }
    }
    f16v acc[2][2] = {};
    v4u A[3][2], B[3][2], st[2];
    unsigned b1v[2], fl = 0, dn = 0, cbase = CNT, one = 1u, vxo = vx0 ^ 64;
    asm volatile("" : "+v"(cbase), "+v"(one), "+v"(vxo));
    f4v cs4 = {};
    const unsigned sel1 = (((lane >> 2) ^ (lane >> 4)) & 1) ? 0u : 0x3f803f80u;
    const v4u sel = {sel1, sel1, sel1, sel1};
    auto rdA = [&](int n, int mi) {
      const int u = (2 * (n >> 2) + 1) % 7;
      const unsigned a = aa[u >> 2][mi] + (u & 3) * UNIT + (n & 3) * 4096;
      A[n % 3][mi] = ldstr2(a, a + 1024);
    };
    auto rdB = [&](int n, int ni) {
      const int u = (2 * (n >> 2)) % 7;
      B[n % 3][ni] = lds128(ba[u >> 2][n & 3] + (u & 3) * UNIT + ni * 4096);
    };
    const v4u xs = make_srd(p.x, X_BYTES), w1s = make_srd(p.w1, W_BYTES);
    unsigned sx[4], sw[4], mb = w * 4096;
#pragma unroll
    for (int j = 0; j < 4; ++j) {
      sx[j] = (LT0 + 32 * w) * 1280 + 4 * 128 + j * 9216;
      sw[j] = (3 * 64 + 16 * w) * 3456 + 2 * LF0 + j * 12800;
      asm volatile("" : "+s"(sx[j]), "+s"(sw[j]));
    }
    auto piece = [&](auto qc) {
      constexpr int q = qc, i = q / 8 + 3 + ((q >> 2) & 1), j = q & 3;
      if constexpr (q >= 0 && q < 52) {
        if constexpr (q & 4) {
          if constexpr (j == 0) dma1(xs, vx0, sx[0], mb + ((2 * i) % 7) * UNIT);
          else dma_next<j * 1024>(xs, (j & 1) ? vxo : vx0, sx[j]);
          sx[j] += 128;
          asm volatile("" : "+s"(sx[j]));
        } else {
          if constexpr (j == 0) dma1(w1s, vw0, sw[0], mb + ((2 * i + 1) % 7) * UNIT);
          else dma_next<j * 1024>(w1s, vw0, sw[j]);
          sw[j] += 64 * 3456;
          asm volatile("" : "+s"(sw[j]));
        }
      }
    };
    auto load_stats = [&] {
#pragma unroll
      for (int i = 0; i < 2; ++i) st[i] = __builtin_amdgcn_raw_buffer_load_b128(ws, (lane & 31) * 16, WS_STAT + (t0 + 64 * wt + 32 * i) * 16, SC1);
    };
    await(E_FULL, 4);
    STAMP(2); STAMPC(11);
    rdA(0, 0); rdB(0, 0); rdB(0, 1); rdA(0, 1); rdA(1, 0); rdB(1, 0); rdB(1, 1); rdA(1, 1);
    SCHED();
    unsigned tag = 0;
    v4u eb[2];
    static_for<40>([&](auto nc) {
      constexpr int n = nc, sg = n >> 2, r = n & 3;
      acc[0][0] = mfma32b(A[n % 3][0], B[n % 3][0], acc[0][0]);
      SCHED();
      if (n < 38) { rdA(n + 2, 0); rdB(n + 2, 0); }
      SCHED();
      acc[0][1] = mfma32b(A[n % 3][0], B[n % 3][1], acc[0][1]);
      SCHED();
      piece(IC<2 * (n - 3)>{});
      if (r == 1 && sg < 9) fl = *(volatile LAS unsigned*)lp(cbase + (E_FULL - CNT) + (sg + 1) * 4);
      if (r == 2 && sg < 7) dn = *(volatile LAS unsigned*)lp(cbase + (E_DONE - CNT) + sg * 4);
      if (n == 34) { dn = *(volatile LAS unsigned*)lp(cbase + (E_LN - CNT)); tag = *(volatile LAS unsigned*)lp(cbase + (W_TAG - CNT)); }
      SCHED();
      cs4 = mfma16b(sel, A[n % 3][0], cs4);
      SCHED();
      acc[1][0] = mfma32b(A[n % 3][1], B[n % 3][0], acc[1][0]);
      SCHED();
      if (n < 38) { rdB(n + 2, 1); rdA(n + 2, 1); }
      if (r == 1) signal_at<E_DONE - CNT + sg * 4>(cbase, one);
      if (n == 39) { sts32(SEXC + w * 128 + (lane & 31) * 4, f2u(cs4[0])); signal(E_SX + w * 4); }
      SCHED();
      acc[1][1] = mfma32b(A[n % 3][1], B[n % 3][1], acc[1][1]);
      SCHED();
      piece(IC<2 * (n - 3) + 1>{});
      if (r == 1 && sg < 9) { check(fl, E_FULL + (sg + 1) * 4, sg == 2 ? 8 : 4); if (sg & 1) STAMP(16 + (sg >> 1)); }
      if (r == 2 && sg < 7) check(dn, E_DONE + sg * 4, 4);
      if (r == 0 && sg >= 2 && sg < 9) {
        if (sg < 8) WAITV(8); else WAITV(4);
        signal_at<E_FULL - CNT + (sg + 1) * 4>(cbase, one);
      }
      if (n == 29) load_stats();
      if (n == 30) {
#pragma unroll
        for (int i = 0; i < 2; ++i)
          b1v[i] = __builtin_amdgcn_raw_buffer_load_b16(make_rsrc(p.b1, F_ * 2), (lane & 31) * 2, (f0 + 64 * wf + 32 * (i ^ wt)) * 2, 0);
        signal_at<E_VMQ - CNT>(cbase, one);
      }
      if (n == 35) {
        if (__builtin_expect(__builtin_amdgcn_readfirstlane(dn) < 4, 0)) { await(E_LN, 4); tag = peek(W_TAG); }
        tag = __builtin_amdgcn_readfirstlane(tag);
      }
      if (n == 37) {
        if (__builtin_expect(!__all(st[0].y == tag && st[0].w == tag && st[1].y == tag && st[1].w == tag), 0)) {
#pragma unroll 1
          for (unsigned s = 0; s < SPIN_CAP; ++s) {
            asm volatile("" ::: "memory");
            load_stats();
            if (__all(st[0].y == tag && st[0].w == tag && st[1].y == tag && st[1].w == tag)) break;
          }
        }
      }
      if (n == 37 || n == 38) {
        constexpr int i = n == 38;
        const float mu = -u2f(st[i].x), ir = u2f(st[i].z), mh = bfr(mu), ih = bfr(ir);
        eb[i] = h ? (v4u){0u, 0u, 0u, 0u} : (v4u){pkbf(mh, opq(mu - mh)), pkbf(mh, 0.f), pkbf(ih, opq(ir - ih)), 0u};
        asm volatile("" : "+v"(eb[i]));
      }
      SCHED();
    });
    STAMP(3); STAMPC(12);
    auto ext = [&](int mi, float S) {
      const float sh = bfr(S);
      const v4u ea = h ? (v4u){0u, 0u, 0u, 0u} : (v4u){pkbf(sh, sh), pkbf(S - sh, 0.f), b1v[mi] | (b1v[mi] << 16), 0u};
      acc[mi][0] = mfma32b(ea, eb[0], acc[mi][0]);
      acc[mi][1] = mfma32b(ea, eb[1], acc[mi][1]);
    };
    fl = peek(E_SX + (w ^ 2) * 4);
    float so = u2f(peek(SEXC + (w ^ 2) * 128 + (lane & 31) * 4));
    ext(0, cs4[0]);
    SCHED();
    if (__builtin_expect(__builtin_amdgcn_readfirstlane(fl) < 1, 0)) {
      await(E_SX + (w ^ 2) * 4, 1);
      so = u2f(peek(SEXC + (w ^ 2) * 128 + (lane & 31) * 4));
    }
    ext(1, so);
    STAMP(1);
    if (f0 + 64 * wf < F_) {
      const unsigned hv = ((lane >> 4) & 1) * 55296 + h * 256 + (lane & 15) * 16;
#pragma unroll
      for (int mi = 0; mi < 2; ++mi) {
#pragma unroll
        for (int ni = 0; ni < 2; ++ni) {
          const float ir = u2f(st[ni].z), rho = __builtin_amdgcn_rcpf(ir);
          const float k1 = -2.302208198f * rho, k2 = k1 * 0.044715f * rho * rho;
          const unsigned hs = WS_H + ((t0 + 64 * wt + 32 * ni) / 16) * 55296 + ((f0 + 64 * wf) / 32 + (mi ^ wt)) * 1024;
#pragma unroll
          for (int hf = 0; hf < 2; ++hf) {
            unsigned o[4];
#pragma unroll
            for (int r = 0; r < 4; ++r) {
              const f2v a = {acc[mi][ni][8 * hf + 2 * r], acc[mi][ni][8 * hf + 2 * r + 1]};
              const f2v x = a * (a * a * k2 + k1);
              const f2v e = {__builtin_amdgcn_exp2f(x.x), __builtin_amdgcn_exp2f(x.y)};
              const f2v dd = e * ir + ir;
              const f2v rr = {__builtin_amdgcn_rcpf(dd.x), __builtin_amdgcn_rcpf(dd.y)};
              const f2v gl = a * rr;
              o[r] = pkbf(gl.x, gl.y);
            }
            __builtin_amdgcn_raw_buffer_store_b128((v4u){o[0], o[1], o[2], o[3]}, ws, hv + hs + hf * 512, 0, SC1);
            SCHED();
          }
        }
      }
    }
    signal(E_GELU);
    STAMP(4); STAMPC(13);
    publish(ws, C_H, 4, WS_F2 + (rp * 16 + cp) * 128, tag, lane);
    STAMP(5);
  }

  if (role == 0) {
    const unsigned tag = lds_tag();
    const int mh = w & 1, kh = w >> 1, g = lane >> 4;
    const int tok = 64 * tt + 32 * mh + 16 * kh + (lane & 15);
    poll_words(ws, WS_F2 + ((tt >> 1) * 16 + min(lane, 13)) * 128, tag);
    STAMP(6);
    PRIO(2);
    constexpr int W = 6;
    v4u H[W][2], A[2][5];
    f4v acc[2][5] = {};
    const unsigned hs = WS_H + (4 * tt + 2 * mh) * 55296 + 27 * kh * 1024;
    const unsigned hv[2] = {lane * 16 + kh * 55296u, lane * 16 + (kh ^ 1) * 55296u};
#pragma unroll
    for (int k = 0; k < W; ++k)
#pragma unroll
      for (int ti = 0; ti < 2; ++ti) H[k][ti] = __builtin_amdgcn_raw_buffer_load_b128(ws, hv[ti], hs + k * 1024, SC1);
    SCHED();
    v2u xv[5], bv[5];
    unsigned ab[3];
#pragma unroll
    for (int i = 0; i < 3; ++i) { ab[i] = kh * 15360 + lane * 16 + 2 * i * RING2; asm volatile("" : "+v"(ab[i])); }
    auto rd = [&](int k, int n) { A[k & 1][n] = lds128(ab[((k / 3) % 5) >> 1] + (((k / 3) % 5) & 1) * RING2 + (k % 3) * 5120 + n * 1024); };
    unsigned fl = 0;
    await<1>(E_FULL + 10 * 4, 4);
#pragma unroll
    for (int n = 0; n < 5; ++n) rd(0, n);
    STAMP(7); STAMPC(14);
    SCHED();
#pragma unroll
    for (int k = 0; k < 27; ++k) {
      if (k % 3 == 1 && k < 24) fl = peek(E_FULL + (11 + k / 3) * 4);
#pragma unroll
      for (int ti = 0; ti < 2; ++ti) {
#pragma unroll
        for (int n = 0; n < 5; ++n) {
          acc[ti][n] = mfma16b(A[k & 1][n], H[k % W][ti], acc[ti][n]);
          SCHED();
          if (ti == 0 && k < 26) {
            if (n == 0 && k % 3 == 2) { check(fl, E_FULL + (11 + k / 3) * 4, 4); STAMP(22 + k / 3); }
            rd(k + 1, n);
            if (n == 4 && k % 3 == 1) signal(E_DONE + (10 + k / 3) * 4);
            SCHED();
          }
        }
        if (k + W < 27) H[k % W][ti] = __builtin_amdgcn_raw_buffer_load_b128(ws, hv[ti], hs + (k + W) * 1024, SC1);
        if (k >= 21 && k < 26) {
          if (ti == 0) xv[k - 21] = __builtin_amdgcn_raw_buffer_load_b64(xr, tok * 1280 + 8 * g + 32 * (k - 21), 160 * ct, 0);
          else bv[k - 21] = __builtin_amdgcn_raw_buffer_load_b64(make_rsrc(p.b2, D_ * 2), 8 * g + 32 * (k - 21), 160 * ct, 0);
        }
        SCHED();
      }
    }
    STAMP(8); STAMPC(15);
    await<1>(E_DONE + 14 * 4, 4);
#pragma unroll
    for (int n = 0; n < 5; ++n) sts128(RED2 + w * 5120 + n * 1024 + lane * 16, __builtin_bit_cast(v4u, acc[1][n]));
    signal(E_RED + w * 4);
    await<1>(E_SRDY, 2);
    await<1>(E_RED + (w ^ 2) * 4, 1);
    STAMP(9);
    const unsigned sa = STILE + (4 * g + ((lane & 15) >> 2)) * 160 + (32 * mh + 16 * kh + 4 * (lane & 3)) * 2;
#pragma unroll
    for (int n = 0; n < 5; ++n) {
      const f4v o = __builtin_bit_cast(f4v, lds128(RED2 + (w ^ 2) * 5120 + n * 1024 + lane * 16));
      const v2u sv = ldstr(sa + n * 2560);
      const float bz[4] = {blo(bv[n].x), bhi(bv[n].x), blo(bv[n].y), bhi(bv[n].y)};
      const float sz[4] = {blo(sv.x), bhi(sv.x), blo(sv.y), bhi(sv.y)};
      const float xz[4] = {blo(xv[n].x), bhi(xv[n].x), blo(xv[n].y), bhi(xv[n].y)};
      float r[4];
#pragma unroll
      for (int i = 0; i < 4; ++i) {
        const float z = acc[0][n][i] + o[i] + bz[i];
        const float gt = z * __builtin_amdgcn_rcpf(1.f + __builtin_amdgcn_exp2f(z * -1.44269504088896f));
        r[i] = fmaf(sz[i], gt, xz[i]);
      }
      *(GAS v2u*)(p.out + tok * 640 + 80 * ct + 16 * n + 4 * g) = (v2u){pkbf(r[0], r[1]), pkbf(r[2], r[3])};
    }
    STAMP(10);
  } else {
    PRIO(3);
#pragma unroll 1
    for (int c = 0; c < 3; ++c) w2_chunk<PACE0>(wss, ct, w, c, lane);
    WAITV(16); signal(E_FULL + 10 * 4);
    WAITV(8); signal(E_FULL + 11 * 4);
    WAITV(0); signal(E_FULL + 12 * 4);
    STAMP(6);
    await<4>(E_CONV, 4);
    STAMP(7);
    w2_chunk<0>(wss, ct, w, 3, lane);
    w2_chunk<0>(wss, ct, w, 4, lane);
    WAITV(8);
    signal(E_FULL + 13 * 4);
#pragma unroll 1
    for (int c = 5; c < 9; ++c) {
      await(E_DONE + (5 + c) * 4, 4);
      w2_chunk<0>(wss, ct, w, c, lane);
      WAITV(8);
      signal(E_FULL + (9 + c) * 4);
      STAMP(19 + c);
    }
    WAITV(0);
    signal(E_FULL + 18 * 4);
    STAMP(10);
  }
  STAMP_FLUSH();
}

}

extern "C" void kernel_launch(void* const* d_in, const int* in_sizes, int n_in, void* d_out, int out_size, void* d_ws, size_t ws_size,
                              hipStream_t stream) {
  Params p;
  p.x = (const unsigned short*)d_in[0];
  p.lA = (const unsigned short*)d_in[1];
  p.Ai = (const unsigned short*)d_in[2];
  p.Cr = (const unsigned short*)d_in[3];
  p.Ci = (const unsigned short*)d_in[4];
  p.ldt = (const unsigned short*)d_in[5];
  p.Dsk = (const unsigned short*)d_in[6];
  p.w1 = (const unsigned short*)d_in[8];
  p.b1 = (const unsigned short*)d_in[9];
  p.w2 = (const unsigned short*)d_in[10];
  p.b2 = (const unsigned short*)d_in[11];
  p.out = (unsigned short*)d_out;
  p.ws = (unsigned char*)d_ws;
  (void)hipFuncSetAttribute((const void*)fused_kernel, hipFuncAttributeMaxDynamicSharedMemorySize, LDS_BYTES);
  fused_kernel<<<256, 768, LDS_BYTES, stream>>>(p);
}
```

```cpp
#include <hip/hip_runtime.h>
#include <hip/hip_bf16.h>

namespace {

typedef unsigned v4u __attribute__((ext_vector_type(4)));
typedef unsigned v2u __attribute__((ext_vector_type(2)));
typedef short s4v __attribute__((ext_vector_type(4)));
typedef float f2v __attribute__((ext_vector_type(2)));
typedef float f4v __attribute__((ext_vector_type(4)));
typedef float f16v __attribute__((ext_vector_type(16)));
typedef _Float16 h2v __attribute__((ext_vector_type(2)));
typedef _Float16 h8v __attribute__((ext_vector_type(8)));
typedef __bf16 b2v __attribute__((ext_vector_type(2)));
typedef __bf16 b8v __attribute__((ext_vector_type(8)));
#define GAS __attribute__((address_space(1)))
#define LAS __attribute__((address_space(3)))
#define DEV __device__ __forceinline__
typedef __amdgpu_buffer_rsrc_t rsrc_t;

constexpr int L_ = 2048, D_ = 640, F_ = 1728;
constexpr unsigned X_BYTES = L_ * D_ * 2, W_BYTES = D_ * F_ * 2;
constexpr int SC1 = 16;
constexpr unsigned SPIN_CAP = 1u << 16;

constexpr unsigned WS_EPOCH = 0;
constexpr unsigned WS_F1 = 4096;
constexpr unsigned WS_F2 = 65536;
constexpr unsigned WS_FW = 24576;
constexpr unsigned WS_F3 = 16384;
constexpr unsigned WS_STAT = 32768;
constexpr unsigned WS_XNT = 131072;
constexpr unsigned WS_W2T = 2752512;
constexpr unsigned WS_H = 5242880;
constexpr unsigned WS_ST = 12582912;
constexpr unsigned WS_STAMP = 15204352;
constexpr unsigned WS_BYTES = WS_STAMP + 256 * 12 * 64 * 8;

constexpr unsigned LDS_BYTES = 163840;
constexpr unsigned UNIT = 16384;
constexpr unsigned KSCR = 8704;
constexpr unsigned FPOS = 2576;
constexpr unsigned GRP = 7 * UNIT;
constexpr unsigned LNST = GRP + 4 * KSCR;
constexpr unsigned CNT = LNST + 10752;
constexpr unsigned SEXC = CNT + 512;
constexpr unsigned JUNK = CNT + 1024;
constexpr unsigned RSTG = 6 * UNIT;
constexpr unsigned UCP = 2368;
constexpr unsigned CH_R3 = 4 * UCP, CH_U = 9856, CH_BYTES = 2 * KSCR;
constexpr unsigned RING2 = 30720;
constexpr unsigned STILE = 4 * RING2, RED2 = STILE + 12800;
constexpr unsigned E_FULL = CNT, E_DONE = CNT + 80;
constexpr unsigned E_GELU = CNT + 160, E_LN = CNT + 164, E_LNPUB = CNT + 168, E_SRDY = CNT + 172, E_RED = CNT + 176, E_SX = CNT + 192;
constexpr unsigned C_H = CNT + 208, C_F1 = CNT + 212, E_F1OK = CNT + 216, E_CONV = CNT + 220, E_VMQ = CNT + 76, C_FW = CNT + 156, E_FWOK = CNT + 360;
constexpr unsigned C_GRP = CNT + 224;
constexpr unsigned W_TAG = CNT + 368;
constexpr int N_ZERO = 92;

struct Params {
  const unsigned short *x, *lA, *Ai, *Cr, *Ci, *ldt, *Dsk, *w1, *b1, *w2, *b2;
  unsigned short* out;
  unsigned char* ws;
};

#define STAMP(i) do { } while (0)
#define STAMPC(i) do { } while (0)
#define STAMPV(i, v) do { } while (0)
#define STAMP_INIT() do { } while (0)
#define STAMP_FLUSH() do { } while (0)

#define PRIO(x) __builtin_amdgcn_s_setprio(x)
#define PACE0 1
#define APRIO 0
#define LT0 t0
#define LF0 f0
#undef TACC
#define TACC(acc, stmt) do { stmt; } while (0)
#define LPRIO 1
#define FWPOLL_AT 4
#define STAMPL(i) do { } while (0)
#define CHK(e, a, n, t) check(e, a, n)
#define HADDR (hv + hs + hf * 512)
#define ROTV (((slot / 7 * 5 + 1) / 2 + slot % 7 * 3) % 10)
#define BAR() asm volatile("s_waitcnt lgkmcnt(0)\n\ts_barrier" ::: "memory")
#define WAITV(n) asm volatile("s_waitcnt vmcnt(" #n ")" ::: "memory")
#define SCHED() __builtin_amdgcn_sched_barrier(0)

template <int I> struct IC { constexpr operator int() const { return I; } };
template <int N, int I = 0, class F> DEV void static_for(F&& f) {
  if constexpr (I < N) { f(IC<I>{}); static_for<N, I + 1>(f); }
}
DEV float u2f(unsigned u) { return __builtin_bit_cast(float, u); }
DEV unsigned f2u(float f) { return __builtin_bit_cast(unsigned, f); }
DEV h2v u2h(unsigned u) { return __builtin_bit_cast(h2v, u); }
DEV float blo(unsigned u) { return u2f(u << 16); }
DEV float bhi(unsigned u) { return u2f(u & 0xffff0000u); }
DEV unsigned pkbf(float a, float b) { return __builtin_bit_cast(unsigned, __builtin_convertvector((f2v){a, b}, b2v)); }
DEV unsigned pkh(float a, float b) { return __builtin_bit_cast(unsigned, __builtin_convertvector((f2v){a, b}, h2v)); }
DEV float opq(float x) { asm("" : "+v"(x)); return x; }
DEV float bfr(float a) { return blo(pkbf(a, a)); }
DEV f16v mfma32b(v4u a, v4u b, f16v c) { return __builtin_amdgcn_mfma_f32_32x32x16_bf16(__builtin_bit_cast(b8v, a), __builtin_bit_cast(b8v, b), c, 0, 0, 0); }
DEV f16v mfma32h(v4u a, v4u b, f16v c) { return __builtin_amdgcn_mfma_f32_32x32x16_f16(__builtin_bit_cast(h8v, a), __builtin_bit_cast(h8v, b), c, 0, 0, 0); }
DEV f4v mfma16b(v4u a, v4u b, f4v c) { return __builtin_amdgcn_mfma_f32_16x16x32_bf16(__builtin_bit_cast(b8v, a), __builtin_bit_cast(b8v, b), c, 0, 0, 0); }
DEV f4v mfma16h(v4u a, v4u b, f4v c) { return __builtin_amdgcn_mfma_f32_16x16x32_f16(__builtin_bit_cast(h8v, a), __builtin_bit_cast(h8v, b), c, 0, 0, 0); }

DEV LAS unsigned char* lp(unsigned a) { return (LAS unsigned char*)(size_t)a; }
DEV v4u lds128(unsigned a) { return *(LAS v4u*)lp(a); }
DEV v2u lds64(unsigned a) { return *(LAS v2u*)lp(a); }
DEV unsigned lds32(unsigned a) { return *(LAS unsigned*)lp(a); }
DEV void sts128(unsigned a, v4u v) { *(LAS v4u*)lp(a) = v; }
DEV void sts64(unsigned a, v2u v) { *(LAS v2u*)lp(a) = v; }
DEV void sts32(unsigned a, unsigned v) { *(LAS unsigned*)lp(a) = v; }
DEV v2u ldstr(unsigned a) { return __builtin_bit_cast(v2u, __builtin_amdgcn_ds_read_tr16_b64_v4i16((LAS s4v*)lp(a))); }
DEV v4u ldstr2(unsigned a0, unsigned a1) { const v2u x = ldstr(a0), y = ldstr(a1); return (v4u){x.x, x.y, y.x, y.y}; }

DEV v4u make_srd(const void* ptr, unsigned bytes) {
  return (v4u){(unsigned)(size_t)ptr, (unsigned)((size_t)ptr >> 32) & 0xffffu, bytes, 0x00020000u};
}
DEV rsrc_t make_rsrc(const void* ptr, unsigned bytes) { return __builtin_amdgcn_make_buffer_rsrc((void*)ptr, 0, bytes, 0x00020000); }
#define DMA_HEAD "s_mov_b32 m0, %0\n\ts_nop 0\n\t"
DEV void dma1(v4u srd, unsigned v, unsigned soff, unsigned lds) {
  asm volatile(DMA_HEAD "buffer_load_dwordx4 %1, %2, %3 offen lds" ::"s"(lds), "v"(v), "s"(srd), "s"(soff) : "memory");
}
template <int OFF> DEV void dma_next(v4u srd, unsigned v, unsigned soff) {
  asm volatile("buffer_load_dwordx4 %0, %1, %2 offen offset:%3 lds" ::"v"(v), "s"(srd), "s"(soff), "i"(OFF) : "memory");
}
DEV void touch(v4u srd, unsigned v) {
}
DEV void dma4(v4u srd, const unsigned (&v)[4], unsigned soff, unsigned lds) {
  asm volatile(DMA_HEAD "buffer_load_dwordx4 %1, %5, %6 offen lds\n\tbuffer_load_dwordx4 %2, %5, %6 offen offset:1024 lds\n\t"
               "buffer_load_dwordx4 %3, %5, %6 offen offset:2048 lds\n\tbuffer_load_dwordx4 %4, %5, %6 offen offset:3072 lds"
               ::"s"(lds), "v"(v[0]), "v"(v[1]), "v"(v[2]), "v"(v[3]), "s"(srd), "s"(soff) : "memory");
}
DEV void dma4_lin_sc1(v4u srd, unsigned v, unsigned soff, unsigned lds) {
  asm volatile(DMA_HEAD "buffer_load_dwordx4 %1, %2, %3 offen sc1 lds\n\tbuffer_load_dwordx4 %1, %2, %3 offen offset:1024 sc1 lds\n\t"
               "buffer_load_dwordx4 %1, %2, %3 offen offset:2048 sc1 lds\n\tbuffer_load_dwordx4 %1, %2, %3 offen offset:3072 sc1 lds"
               ::"s"(lds), "v"(v), "s"(srd), "s"(soff) : "memory");
}

DEV void poll_words(rsrc_t ws, unsigned off, unsigned tag) {
#pragma unroll 1
  for (unsigned s = 0; s < SPIN_CAP; ++s) {
    asm volatile("" ::: "memory");
    if (__all(__builtin_amdgcn_raw_buffer_load_b32(ws, off, 0, SC1) == tag)) break;
  }
  asm volatile("" ::: "memory");
}
DEV void poll_words4(rsrc_t ws, unsigned off, unsigned tag) {
#pragma unroll 1
  for (unsigned s = 0; s < SPIN_CAP; ++s) {
    asm volatile("" ::: "memory");
    const v4u v = __builtin_amdgcn_raw_buffer_load_b128(ws, off, 0, SC1);
    if (__all(v.x == tag && v.y == tag && v.z == tag && v.w == tag)) break;
    __builtin_amdgcn_s_sleep(8);
  }
  asm volatile("" ::: "memory");
}
DEV unsigned sig_base(int lane) { return lane == 0 ? CNT : JUNK + lane * 4; }
template <int OFF> DEV void signal_at(unsigned base, unsigned one) {
  asm volatile("ds_add_u32 %0, %1 offset:%2" ::"v"(base), "v"(one), "i"(OFF) : "memory");
}
DEV void signal(unsigned a, int lane) {
  asm volatile("ds_add_u32 %0, %1" ::"v"(sig_base(lane) + (a - CNT)), "v"(1u) : "memory");
}
DEV unsigned peek(unsigned a) { return *(volatile LAS unsigned*)lp(a); }
template <int SLEEP = 1> DEV void await(unsigned a, unsigned need) {
#pragma unroll 1
  for (unsigned s = 0; s < (SPIN_CAP << 2); ++s) {
    asm volatile("" : "+s"(s));
    if (__builtin_amdgcn_readfirstlane(peek(a)) >= need) break;
    __builtin_amdgcn_s_sleep(SLEEP);
  }
  asm volatile("" ::: "memory");
}
DEV void check(unsigned early, unsigned a, unsigned need) {
  if (__builtin_expect(__builtin_amdgcn_readfirstlane(early) < need, 0)) await<1>(a, need);
  asm volatile("" ::: "memory");
}
DEV unsigned lds_arrive(unsigned cnt, int lane) {
  unsigned old;
  asm volatile("ds_add_rtn_u32 %0, %1, %2\n\ts_waitcnt lgkmcnt(0)" : "=&v"(old) : "v"(sig_base(lane) + (cnt - CNT)), "v"(1u) : "memory");
  return __builtin_amdgcn_readfirstlane(old);
}
template <int SLEEP = 1> DEV void group_sync(unsigned cnt, unsigned target, int lane) {
  if (lds_arrive(cnt, lane) + 1 < target) await<SLEEP>(cnt, target);
}
DEV void publish(rsrc_t ws, unsigned cnt, unsigned nwaves, unsigned flag_off, unsigned tag, int lane) {
  WAITV(0);
  if (lds_arrive(cnt, lane) + 1 == nwaves && lane == 0) __builtin_amdgcn_raw_buffer_store_b32(tag, ws, flag_off, 0, SC1);
}
DEV unsigned epoch_load(rsrc_t ws) { return __builtin_amdgcn_raw_buffer_load_b32(ws, WS_EPOCH, 0, SC1); }
DEV unsigned epoch_tag(unsigned ev, bool share, int lane) {
  const unsigned e = __builtin_amdgcn_readfirstlane(ev);
  const unsigned tag = e * 0x9E3779B1u + 0x7F4A7C15u;
  if (share && lane == 0) sts64(W_TAG, (v2u){tag, e});
  return tag;
}
DEV unsigned lds_tag() { return __builtin_amdgcn_readfirstlane(lds32(W_TAG)); }

template <int CTRL> DEV float dpp_add(float v) {
  return v + u2f(__builtin_amdgcn_update_dpp(0u, f2u(v), CTRL, 0xf, 0xf, false));
}
template <int CTRL> DEV float dpp_max(float v) {
  return fmaxf(v, u2f(__builtin_amdgcn_update_dpp(0u, f2u(v), CTRL, 0xf, 0xf, false)));
}
DEV float half_sum(float v) {
  v = dpp_add<0x128>(v); v = dpp_add<0x124>(v); v = dpp_add<0x122>(v); v = dpp_add<0x121>(v);
  const auto r = __builtin_amdgcn_permlane16_swap(f2u(v), f2u(v), false, false);
  return u2f(r[0]) + u2f(r[1]);
}
DEV float wave_max(float v) {
  v = dpp_max<0x128>(v); v = dpp_max<0x124>(v); v = dpp_max<0x122>(v); v = dpp_max<0x121>(v);
  const auto r = __builtin_amdgcn_permlane16_swap(f2u(v), f2u(v), false, false);
  v = fmaxf(u2f(r[0]), u2f(r[1]));
  const auto q = __builtin_amdgcn_permlane32_swap(f2u(v), f2u(v), false, false);
  return fmaxf(u2f(q[0]), u2f(q[1]));
}

struct LnIn { v2u v[5]; };
DEV LnIn ln_load(rsrc_t xr, int token, int lane) {
  LnIn r;
#pragma unroll
  for (int m = 0; m < 5; ++m) r.v[m] = __builtin_amdgcn_raw_buffer_load_b64(xr, token * 1280 + (lane & 31) * 8 + m * 256, 0, 0);
  return r;
}
DEV void ln_compute(const LnIn& in, rsrc_t ws, int token, int row, unsigned tag, int lane) {
  float xv[20], s = 0.f, q = 0.f;
#pragma unroll
  for (int m = 0; m < 5; ++m) { xv[4 * m] = blo(in.v[m].x); xv[4 * m + 1] = bhi(in.v[m].x); xv[4 * m + 2] = blo(in.v[m].y); xv[4 * m + 3] = bhi(in.v[m].y); }
#pragma unroll
  for (int m = 0; m < 20; ++m) { s = opq(s + xv[m]); q = opq(fmaf(xv[m], xv[m], q)); }
  s = half_sum(s); q = half_sum(q);
  const float mean = s * (1.f / 640.f);
  const float var = fmaxf(fmaf(-mean, mean, q * (1.f / 640.f)), 0.f);
  const float m1 = bfr(mean), s1 = bfr(__builtin_amdgcn_sqrtf(bfr(bfr(var) + 1e-5f)));
  const float r1 = __builtin_amdgcn_rcpf(s1);
  const float m2 = bfr((mean - m1) * r1), s2 = bfr(__builtin_amdgcn_sqrtf(bfr(bfr(var * r1 * r1) + 1e-5f)));
  if ((lane & 31) == 0)
    __builtin_amdgcn_raw_buffer_store_b128((v4u){f2u(fmaf(m2, s1, m1)), tag, f2u(s1 * s2), tag}, ws, WS_STAT + token * 16, 0, SC1);
  const float c = -m1 * r1;
#pragma unroll
  for (int m = 0; m < 5; ++m) {
    const v2u o = {pkh(opq(fmaf(xv[4 * m], r1, c)), opq(fmaf(xv[4 * m + 1], r1, c))), pkh(opq(fmaf(xv[4 * m + 2], r1, c)), opq(fmaf(xv[4 * m + 3], r1, c)))};
    sts64(LNST + row * 1344 + (lane & 31) * 8 + m * 256, o);
  }
}
DEV void ln_publish(rsrc_t ws, int tb, int aw, int lane) {
  const unsigned a = LNST + ((lane & 15) >> 2) * 1344 + (lane >> 4) * 32 + (lane & 3) * 8 + aw * 640;
#pragma unroll
  for (int i = 0; i < 5; ++i) {
    const v4u v = ldstr2(a + i * 128, a + i * 128 + 4 * 1344);
    __builtin_amdgcn_raw_buffer_store_b128(v, ws, WS_XNT + tb * 10240 + (aw * 320 + i * 64 + lane) * 16, 0, SC1);
  }
}

struct Cx { float r, i; };
DEV Cx cmul(Cx a, Cx b) { return {opq(fmaf(a.r, b.r, -a.i * b.i)), opq(fmaf(a.r, b.i, a.i * b.r))}; }
DEV Cx csqr(Cx a) { return {opq(fmaf(a.r, a.r, -a.i * a.i)), opq((a.r + a.r) * a.i)}; }
DEV unsigned ch(Cx a) { return pkh(a.r, a.i); }
DEV unsigned hmul(unsigned a, unsigned b) {
  unsigned t, r;
  asm("v_pk_mul_f16 %0, %1, %2 op_sel:[0,0] op_sel_hi:[0,1]" : "=v"(t) : "v"(a), "v"(b));
  asm("v_pk_fma_f16 %0, %1, %2, %3 op_sel:[1,1,0] op_sel_hi:[1,0,1] neg_lo:[0,1,0]" : "=v"(r) : "v"(a), "v"(b), "v"(t));
  return r;
}
DEV float exp_bf(float x) {
  const float t = x * 1.44268798828125f, lo = x * 7.0526077138765e-06f;
  const float n = __builtin_rintf(t);
  return bfr(__builtin_amdgcn_ldexpf(__builtin_amdgcn_exp2f((t - n) + lo), (int)fminf(fmaxf(n, -200.f), 200.f)));
}
DEV Cx cexp_at(float lg, float rh, float rl, float P) {
  const float m = __builtin_amdgcn_exp2f(lg * P), ph = fmaf(rl, P, __builtin_amdgcn_fractf(rh * P));
  return {opq(m * __builtin_amdgcn_cosf(ph)), opq(m * __builtin_amdgcn_sinf(ph))};
}
struct KgIn { unsigned lA, Ai, Cr, Ci; float dt, dsk; };
DEV KgIn kg_load(const Params& p, int d, int lane) {
  KgIn k;
  k.lA = ((const GAS unsigned*)p.lA)[d * 64 + lane]; k.Ai = ((const GAS unsigned*)p.Ai)[d * 64 + lane];
  k.Cr = ((const GAS unsigned*)p.Cr)[d * 64 + lane]; k.Ci = ((const GAS unsigned*)p.Ci)[d * 64 + lane];
  k.dt = u2f((unsigned)((const GAS unsigned short*)p.ldt)[d] << 16);
  k.dsk = u2f((unsigned)((const GAS unsigned short*)p.Dsk)[d] << 16);
  return k;
}
DEV float kg_wave(const KgIn& k, int g, unsigned scr, unsigned fp, unsigned fp_free, int lane) {
  const float dt = exp_bf(k.dt), c2 = dt + dt;
  const float mx = wave_max(fmaxf(fmaxf(fabsf(blo(k.Cr)), fabsf(bhi(k.Cr))), fmaxf(fabsf(blo(k.Ci)), fabsf(bhi(k.Ci))))) * c2;
  int ex = __builtin_amdgcn_frexp_expf(mx);
  ex = ex < -120 ? -120 : (ex > 120 ? 120 : ex);
  const float cs = c2 * __builtin_amdgcn_ldexpf(1.f, -ex);
  const int sh = g ? 0 : 16;
  const float lg = -(exp_bf(bhi(k.lA << sh)) * dt) * 1.44269504088896f, ai = bhi(k.Ai << sh) * dt;
  const Cx c0 = {opq(bhi(k.Cr << sh) * cs), opq(-bhi(k.Ci << sh) * cs)};
  const float rh = ai * 0.15915494f, rl = fmaf(ai, 0.15915494f, -rh) + ai * 6.4206382e-09f;
  const Cx v1 = cexp_at(lg, -rh, -rl, 1.f), w1 = cexp_at(lg, rh, rl, 8.f), G = cexp_at(lg, -rh, -rl, 256.f);
  const unsigned wa = scr + lane * 4, ra = scr + (lane & 31) * 272 + (lane >> 5) * 16;
  {
    const Cx w2 = csqr(w1), w3 = cmul(w2, w1), w4 = csqr(w2), w8 = csqr(w4), w12 = cmul(w8, w4), w16 = csqr(w8);
    const Cx w20 = cmul(w16, w4), w24 = csqr(w12), w28 = cmul(w24, w4);
    const unsigned o[3] = {ch(w1), ch(w2), ch(w3)};
    const unsigned bs[7] = {ch(w4), ch(w8), ch(w12), ch(w16), ch(w20), ch(w24), ch(w28)};
    sts32(wa, 0x00003c00u);
#pragma unroll
    for (int j = 0; j < 3; ++j) sts32(wa + (1 + j) * 272, o[j]);
#pragma unroll
    for (int b = 0; b < 7; ++b) {
      sts32(wa + (4 + 4 * b) * 272, bs[b]);
#pragma unroll
      for (int j = 0; j < 3; ++j) sts32(wa + (5 + 4 * b + j) * 272, hmul(bs[b], o[j]));
    }
  }
  asm volatile("" ::: "memory");
  v4u y[8];
#pragma unroll
  for (int s = 0; s < 8; ++s) y[s] = lds128(ra + s * 32);
  asm volatile("" ::: "memory");
  {
    const Cx v2 = csqr(v1), v3 = cmul(v2, v1), v4 = csqr(v2), v6 = csqr(v3), x4 = cmul(c0, v4);
    const unsigned h1 = ch(v1), h2 = ch(v2), h3 = ch(v3), h4 = ch(v4), h6 = ch(v6), hx0 = ch(c0), hx4 = ch(x4);
    sts32(wa + 0 * 272, hx0); sts32(wa + 1 * 272, hmul(hx0, h1)); sts32(wa + 2 * 272, hmul(hx0, h2)); sts32(wa + 3 * 272, hmul(hx0, h3));
    sts32(wa + 4 * 272, hx4); sts32(wa + 5 * 272, hmul(hx4, h1)); sts32(wa + 6 * 272, hmul(hx4, h2)); sts32(wa + 7 * 272, hmul(hx4, h3));
    const Cx d1 = cmul(c0, G), d2 = cmul(d1, G), d3 = cmul(d2, G), d4 = cmul(d3, G);
    const Cx G4 = csqr(csqr(G));
    const Cx d5 = cmul(d1, G4), d6 = cmul(d2, G4), d7 = cmul(d3, G4), d8 = cmul(d4, G4);
    const unsigned e1 = ch(d1), e2 = ch(d2), e3 = ch(d3);
    sts32(wa + 8 * 272, e1); sts32(wa + 9 * 272, hmul(e1, h2)); sts32(wa + 10 * 272, hmul(e1, h4)); sts32(wa + 11 * 272, hmul(e1, h6));
    sts32(wa + 12 * 272, e2); sts32(wa + 13 * 272, hmul(e2, h4)); sts32(wa + 14 * 272, e3); sts32(wa + 15 * 272, hmul(e3, h4));
    sts32(wa + 16 * 272, ch(d4)); sts32(wa + 17 * 272, ch(d5)); sts32(wa + 18 * 272, ch(d6)); sts32(wa + 19 * 272, ch(d7));
    sts32(wa + 20 * 272, ch(d8));
  }
  asm volatile("" ::: "memory");
  v4u xx[8];
#pragma unroll
  for (int s = 0; s < 8; ++s) xx[s] = lds128(ra + s * 32);
  SCHED();
  f16v acc = {};
#pragma unroll
  for (int s = 0; s < 8; ++s) acc = mfma32h(xx[s], y[s], acc);
  const int j = lane & 31, h = lane >> 5;
  await(E_LNPUB, fp_free);
  sts128(fp + (8 * j + 4 * h) * 4, (v4u){f2u(acc[0]), f2u(acc[1]), f2u(acc[2]), f2u(acc[3])});
  sts64(fp + (h ? 384 + 2 * j : 256 + 4 * j) * 4, (v2u){f2u(acc[4]), f2u(acc[5])});
  sts64(fp + (h ? 448 + 2 * j : 258 + 4 * j) * 4, (v2u){f2u(acc[6]), f2u(acc[7])});
  if (lane <= 32) sts32(fp + (512 + 128 * h + j) * 4, f2u(acc[8]));
  if (h == 0) {
#pragma unroll
    for (int i = 1; i < 4; ++i) sts32(fp + (512 + j + 32 * i) * 4, f2u(acc[8 + i]));
  }
  return __builtin_amdgcn_ldexpf(1.f, ex);
}
DEV void kg_table0(unsigned cb, unsigned fp, int g, int lane) {
  auto f1 = [&](int i) { return opq(u2f(lds32(fp + i * 4)) + u2f(lds32(fp + FPOS + i * 4))); };
  if (g == 0) {
    const int j = lane & 31, h = lane >> 5;
    const unsigned a = fp + (h ? 256 + 4 * j : 8 * j) * 4;
    const v4u l0 = lds128(a), h0 = lds128(a + 16), l1 = lds128(a + FPOS), h1 = lds128(a + FPOS + 16);
    const float e0 = opq(u2f(l0.x) + u2f(l1.x)), e1 = opq(u2f(l0.y) + u2f(l1.y)), e2 = opq(u2f(l0.z) + u2f(l1.z)), e3 = opq(u2f(l0.w) + u2f(l1.w));
    const float e4 = opq(u2f(h0.x) + u2f(h1.x)), e5 = opq(u2f(h0.y) + u2f(h1.y)), e6 = opq(u2f(h0.z) + u2f(h1.z)), e7 = opq(u2f(h0.w) + u2f(h1.w));
    const v4u o0 = {pkh(e0, e1), pkh(e2, e3), pkh(e4, e5), pkh(e6, e7)};
    const v4u o1 = {pkh(e0, e0), pkh(e1, e2), pkh(e2, e2), pkh(e3, e4)};
    sts128(cb + 32 + lane * 16, h ? o1 : o0);
    const float fa = f1(512), fb = f1(513);
    const unsigned aa = pkh(fa, fa), bb = pkh(fb, fb);
    if (lane < 4) sts128(cb + 32 + (128 + lane) * 16, lane == 0 ? (v4u){aa, aa, pkh(fa, fb), bb} : lane == 1 ? (v4u){bb, bb, 0u, 0u} : (v4u){0u, 0u, 0u, 0u});
  } else {
    const float a = f1(384 + 2 * lane), b = f1(385 + 2 * lane), c = f1(386 + 2 * lane);
    sts128(cb + 32 + (64 + lane) * 16, (v4u){pkh(a, a), pkh(a, b), pkh(b, b), pkh(c, c)});
    const float lo = f1(640 - 2 * lane), hi = f1(lane ? 641 - 2 * lane : 640);
    sts32(cb + CH_R3 + lane * 4, pkh(lane ? hi : 0.f, lo));
    if (lane < 16) sts32(cb + CH_R3 + 256 + lane * 4, 0u);
    if (lane < 32) sts32(cb + (lane >> 3) * UCP + (lane & 7) * 4, 0u);
  }
}
DEV void kg_copies(unsigned cb, int g, int lane) {
  auto one = [&](int chunk) {
    const unsigned o = cb + 32 + chunk * 16;
    const v2u pv = lds64(o - 8);
    const v4u e = lds128(o);
    const unsigned a0 = __builtin_amdgcn_alignbit(pv.y, pv.x, 16), a1 = __builtin_amdgcn_alignbit(e.x, pv.y, 16), a2 = __builtin_amdgcn_alignbit(e.y, e.x, 16),
                   a3 = __builtin_amdgcn_alignbit(e.z, e.y, 16), a4 = __builtin_amdgcn_alignbit(e.w, e.z, 16);
    sts128(o + UCP, (v4u){a1, a2, a3, a4});
    sts128(o + 2 * UCP, (v4u){pv.y, e.x, e.y, e.z});
    sts128(o + 3 * UCP, (v4u){a0, a1, a2, a3});
  };
  one(64 * g + lane);
  if (lane < 2) one(128 + 2 * g + lane);
}
DEV void conv_load_u(rsrc_t ws, unsigned grp, int d0, int w, int lane) {
  v4u v[2];
#pragma unroll
  for (int i = 0; i < 2; ++i)
    v[i] = __builtin_amdgcn_raw_buffer_load_b128(ws, (lane >> 1) * 10240 + (lane & 1) * 16, WS_XNT + (64 * w + 32 * i) * 10240 + d0 * 16, SC1);
  if (lane < 16) sts128(grp + (w >> 1) * CH_BYTES + CH_U + (w & 1) * 256 + lane * 16, (v4u){0u, 0u, 0u, 0u});
#pragma unroll
  for (int i = 0; i < 2; ++i) sts128(grp + (lane & 1) * CH_BYTES + CH_U + 512 + (64 * w + 32 * i + (lane >> 1)) * 16, v[i]);
}
template <class F1>
DEV float ssm_prepare(rsrc_t ws, const KgIn& k, unsigned grp, unsigned fp, unsigned fp_free, unsigned cnt, int d0, int w, int lane, F1 f1) {
  const int ci = w >> 1, g = w & 1;
  const float isc = kg_wave(k, g, grp + w * KSCR, fp + w * FPOS, fp_free, lane);
  group_sync(cnt, 4, lane);
  kg_table0(grp + ci * CH_BYTES, fp + 2 * ci * FPOS, g, lane);
  group_sync(cnt + 4 + ci * 4, 2, lane);
  kg_copies(grp + ci * CH_BYTES, g, lane);
  f1();
  conv_load_u(ws, grp, d0, w, lane);
  group_sync(cnt + 12, 4, lane);
  return isc;
}

template <int B> DEV void conv_tail(f4v (&acc)[8], unsigned ua, unsigned r3, v4u w7, v4u w9) {
  const f4v b7 = mfma16h(lds128(ua + 512 * B), w7, (f4v){}), b9 = mfma16h(lds128(ua + 512 * B + 16), w9, (f4v){});
  const v4u bx = {pkh(b7[0], b9[0]), pkh(b7[1], b9[1]), pkh(b7[2], b9[2]), pkh(b7[3], b9[3])};
#pragma unroll
  for (int dl = 4; dl + B < 8; ++dl) {
    const unsigned a = r3 - 64 * dl;
    acc[dl + B] = mfma16h(bx, (v4u){lds32(a), lds32(a + 4), lds32(a + 8), lds32(a + 12)}, acc[dl + B]);
  }
}
DEV void conv_channel(rsrc_t ws, unsigned cb, unsigned cnt, int d, int eh, float isc, float dsk, int lane) {
  const int g = lane >> 4, c16 = lane & 15;
  const unsigned ga = cb + (c16 >> 2) * UCP + 64 - 16 * g + 8 * (lane & 3) + eh * 256;
  const unsigned ba = cb + CH_U + 480 + 32 * c16 + 16 * g - eh * 256;
  f4v acc[8] = {};
  v4u G[2][4], B[2][8];
#pragma unroll
  for (int i = 0; i < 8; ++i) { B[0][i] = lds128(ba + 512 * i); if (i < 4) G[0][i] = ldstr2(ga + 512 * i, ga + 512 * i - 8); }
  {
    unsigned w7[4], w9[4];
#pragma unroll
    for (int i = 0; i < 4; ++i) {
      const int k0 = 8 * g + 2 * i - c16;
      w7[i] = ((k0 >= 5 && k0 <= 11) ? 0x3c00u : 0u) | ((k0 >= 4 && k0 <= 10) ? 0x3c000000u : 0u);
      w9[i] = ((k0 >= 4 && k0 <= 12) ? 0x3c00u : 0u) | ((k0 >= 3 && k0 <= 11) ? 0x3c000000u : 0u);
    }
    const v4u W7 = {w7[0], w7[1], w7[2], w7[3]}, W9 = {w9[0], w9[1], w9[2], w9[3]};
    const unsigned ua = cb + CH_U + 448 + 32 * c16 + 16 * g, r3 = cb + CH_R3 + (127 - c16 + 4 * g) * 4;
    if (eh) {
      conv_tail<0>(acc, ua, r3, W7, W9);
      conv_tail<3>(acc, ua, r3, W7, W9);
    } else {
      conv_tail<1>(acc, ua, r3, W7, W9);
      conv_tail<2>(acc, ua, r3, W7, W9);
      const v4u g4 = ldstr2(ga + 2048, ga + 2048 - 8);
#pragma unroll
      for (int j = 4; j < 8; ++j) acc[j] = mfma16h(g4, B[0][j - 4], acc[j]);
    }
  }
  SCHED();
#pragma unroll
  for (int e = 0; e < 4; ++e) {
    int m = 0;
#pragma unroll
    for (int cc = 0; cc < 4; ++cc)
#pragma unroll
      for (int j = cc; j < 8; ++j) {
        acc[j] = mfma16h(G[e & 1][cc], B[e & 1][j - cc], acc[j]);
        if (e < 3 && (m & 1) == 0 && m < 24) {
          const int i = m >> 1;
          if (i == 1 || i > 8) { const int c = i == 1 ? 0 : i - 8; G[(e + 1) & 1][c] = ldstr2(ga + 512 * c + 64 * (e + 1), ga + 512 * c + 64 * (e + 1) - 8); }
          else { const int c = i == 0 ? 0 : i - 1; B[(e + 1) & 1][c] = lds128(ba + 512 * c - 64 * (e + 1)); }
        }
        ++m;
        SCHED();
      }
  }
  const unsigned to = 2 * (1024 * eh + 16 * c16 + 4 * g);
  v2u uu[4];
#pragma unroll
  for (int j = 0; j < 4; ++j) uu[j] = lds64(cb + CH_U + 512 + to + 512 * j);
  group_sync<1>(cnt, 2, lane);
  f4v mine[4];
  if (eh) {
#pragma unroll
    for (int j = 0; j < 4; ++j) { sts128(cb + 4096 + j * 1024 + lane * 16, __builtin_bit_cast(v4u, acc[j])); mine[j] = acc[4 + j]; }
  } else {
#pragma unroll
    for (int j = 0; j < 4; ++j) { sts128(cb + j * 1024 + lane * 16, __builtin_bit_cast(v4u, acc[4 + j])); mine[j] = acc[j]; }
  }
  group_sync<1>(cnt + 8, 2, lane);
#pragma unroll
  for (int j = 0; j < 4; ++j) {
    const f4v o = __builtin_bit_cast(f4v, lds128(cb + (eh ^ 1) * 4096 + j * 1024 + lane * 16));
    const h2v u0 = u2h(uu[j].x), u1 = u2h(uu[j].y);
    const float s0 = opq(fmaf(mine[j][0] + o[0], isc, dsk * (float)u0.x)), s1 = opq(fmaf(mine[j][1] + o[1], isc, dsk * (float)u0.y));
    const float s2 = opq(fmaf(mine[j][2] + o[2], isc, dsk * (float)u1.x)), s3 = opq(fmaf(mine[j][3] + o[3], isc, dsk * (float)u1.y));
    __builtin_amdgcn_raw_buffer_store_b64((v2u){pkbf(s0, s1), pkbf(s2, s3)}, ws, to + 512 * j, WS_ST + d * 4096, SC1);
  }
}

template <int PACE> DEV void w2_chunk(v4u wss, int ct, int lw, int c, int lane) {
  const unsigned po = (lw & 1) * 7168;
  const unsigned so = WS_W2T + (ct * 54 + 27 * (lw >> 1) + 3 * c) * 5120 + po, lb = (c % 5) * RING2 + (lw >> 1) * 15360 + po;
  dma4_lin_sc1(wss, lane * 16, so, lb);
  if (PACE) __builtin_amdgcn_s_sleep(PACE);
  dma4_lin_sc1(wss, lane * 16, so + 4096, lb + 4096);
  if (PACE) __builtin_amdgcn_s_sleep(PACE);
}
DEV void retile_load(const Params& p, int blk, unsigned stg, int lane) {
  const unsigned v0 = (lane >> 3) * 1280 + 16 * ((lane & 7) ^ (((lane >> 4) & 3) << 1));
  const unsigned v[4] = {v0, v0 + 9216, v0 + 2 * 9216, v0 + 3 * 9216};
  dma4(make_srd(p.w2, W_BYTES), v, (blk / 10) * 40960 + (blk % 10) * 128, stg);
}
struct Frag4 { v4u f[4]; };
DEV Frag4 retile_read(unsigned stg, int lane) {
  const int g = lane >> 4, pp = lane & 3, row = 16 * (g >> 1) + 4 * (g & 1) + ((lane & 15) >> 2);
  Frag4 r;
#pragma unroll
  for (int j = 0; j < 4; ++j) {
    const unsigned a = stg + row * 128 + 16 * ((2 * j + (pp >> 1)) ^ (((row >> 1) & 3) << 1)) + 8 * (pp & 1);
    r.f[j] = ldstr2(a, a + 1024);
  }
  asm volatile("s_waitcnt lgkmcnt(0)" ::: "memory");
  return r;
}
DEV void retile_write(rsrc_t ws, int blk, const Frag4& r, int lane) {
#pragma unroll
  for (int j = 0; j < 4; ++j) {
    const int ng = 4 * (blk % 10) + j;
    __builtin_amdgcn_raw_buffer_store_b128(r.f[j], ws, lane * 16 + WS_W2T + (((ng / 5) * 54 + blk / 10) * 5 + ng % 5) * 1024, 0, SC1);
  }
}

__global__ __launch_bounds__(768, 3) void fused_kernel(Params p) {
  extern __shared__ __attribute__((aligned(16))) unsigned char lds_base[];
  unsigned long long pc;
  unsigned code_bytes;
  asm volatile("s_getpc_b64 %0\n.Lfused_begin:\n\ts_mov_b32 %1, .Lfused_end_a-.Lfused_begin\n\ts_max_i32 %1, %1, .Lfused_end_b-.Lfused_begin"
               : "=s"(pc), "=s"(code_bytes) : : "scc");
  const int lane = threadIdx.x & 63, wave = __builtin_amdgcn_readfirstlane(threadIdx.x >> 6), role = wave >> 2, w = wave & 3;
  const int xcd = blockIdx.x & 7, slot = blockIdx.x >> 3, tb = 32 * xcd + slot;
  const bool gemm_wg = slot < 28;
  const int rp = 4 * (xcd >> 1) + slot / 7, cp = 7 * (xcd & 1) + slot % 7, t0 = 128 * rp, f0 = 128 * cp;
  STAMP_INIT();
  STAMP(0);
  asm volatile("" ::"v"((LAS unsigned char*)lds_base));

  const unsigned vx0 = (lane >> 3) * 1280 + 16 * ((lane & 7) ^ (lane >> 4));
  const unsigned vw0 = (lane >> 4) * 3456 + 16 * ((lane & 15) ^ ((lane >> 4) << 2));
  const unsigned vx[4] = {vx0, (vx0 ^ 64) + 9216, vx0 + 2 * 9216, (vx0 ^ 64) + 3 * 9216};
  const unsigned vw[4] = {vw0, vw0 + 12800, vw0 + 2 * 12800, vw0 + 3 * 12800};
  auto stage_x = [&](int i) { dma4(make_srd(p.x, X_BYTES), vx, (LT0 + 32 * w) * 1280 + 128 * ((i + ROTV) % 10), ((2 * i) % 7) * UNIT + w * 4096); };
  auto stage_w = [&](int i) { dma4(make_srd(p.w1, W_BYTES), vw, (64 * ((i + ROTV) % 10) + 16 * w) * 3456 + 2 * LF0, ((2 * i + 1) % 7) * UNIT + w * 4096); };
  const rsrc_t ws = make_rsrc(p.ws, WS_BYTES), xr = make_rsrc(p.x, X_BYTES);
  unsigned ev = 0;
  LnIn ln = {};
  if (role == 1) {
    PRIO(3);
    if (gemm_wg) { stage_x(0); stage_w(0); }
    ev = epoch_load(ws);
    ln = ln_load(xr, 8 * tb + 2 * w + (lane >> 5), lane);
    if (w < 2) retile_load(p, 2 * tb + w, gemm_wg ? RSTG + w * 4096 : 98304 + w * 8192, lane);
  }
  if (gemm_wg && role == 0) { stage_x(2); stage_w(2); }
  if (!gemm_wg) {
    const int tw = (slot - 28) * 12 + wave;
    const unsigned tx = (512 * (xcd >> 1) + (tw % 8) * 64 + lane) * 1280 + (3 + tw / 8) * 128;
    touch(make_srd(p.x, X_BYTES), tx);
    if (tw < 8) touch(make_srd(p.x, X_BYTES), tx + 6 * 128);
#pragma unroll
    for (int j = 0; j < 2; ++j) {
      const int id = (tw * 2 + j) * 64 + lane;
      touch(make_srd(p.w1, W_BYTES), id < 6272 ? (192 + id / 14) * 3456 + (xcd & 1) * 1792 + (id % 14) * 128 : 0u);
    }
    if (tw == 46) touch(make_srd(p.w1, W_BYTES), (192 + (6144 + lane) / 14) * 3456 + (xcd & 1) * 1792 + ((6144 + lane) % 14) * 128);
    if (tw == 47) touch(make_srd(p.w1, W_BYTES), (192 + (6208 + lane) / 14) * 3456 + (xcd & 1) * 1792 + ((6208 + lane) % 14) * 128);
    if (tw == 8) touch(make_srd(p.b1, F_ * 2), lane * 128);
    if (tw == 9) touch(make_srd(p.b2, D_ * 2), lane * 128);
    if (tw == 10) touch(make_srd(p.ws, WS_BYTES), lane * 262144);
    if (tw == 11) touch(make_srd(p.out, X_BYTES), lane * 65536);
  }
  if (wave < 2) dma1(make_srd((const void*)pc, code_bytes & ~15u), lane * 16, (2 * slot + wave) * 1024, JUNK);

  const v4u wss = make_srd(p.ws, WS_BYTES);
  const int dg = 80 * xcd + (gemm_wg ? 2 * slot : 56 + 6 * (slot - 28) + 2 * role);
  const int tt = 4 * xcd + (slot >> 3), ct = slot & 7;
  const unsigned fslot = (tb >> 2) * 128 + (tb & 3) * 4;
  if (threadIdx.x < N_ZERO) sts32(CNT + threadIdx.x * 4, 0u);

  if (role == 2) {
    const KgIn kin = kg_load(p, dg + (w >> 1), lane);
    BAR();
    const float isc = ssm_prepare(ws, kin, GRP, LNST, 2, C_GRP + 96, dg, w, lane, [&] { STAMP(2); await(E_F1OK, 1); STAMP(3); });
    STAMP(6);
    await(E_GELU, 8);
    STAMP(7);
    conv_channel(ws, GRP + (w >> 1) * CH_BYTES, C_GRP + 96 + 16 + (w >> 1) * 4, dg + (w >> 1), w & 1, isc, kin.dsk, lane);
    STAMP(8);
    signal(E_CONV, lane);
    const unsigned tag = lds_tag();
    publish(ws, C_GRP + 96 + 36, 4, WS_F3 + fslot, tag, lane);
    STAMP(9);
    if (w < 2) {
      await<8>(E_DONE + 14 * 4, 4);
      poll_words4(ws, WS_F3 + (ct * 8 + (lane & 7)) * 128, tag);
      v4u sv[5];
#pragma unroll
      for (int i = 0; i < 5; ++i)
        sv[i] = __builtin_amdgcn_raw_buffer_load_b128(ws, (lane >> 3) * 4096 + (lane & 7) * 16, WS_ST + (80 * ct + 40 * w + 8 * i) * 4096 + tt * 128, SC1);
#pragma unroll
      for (int i = 0; i < 5; ++i) sts128(STILE + (40 * w + 8 * i + (lane >> 3)) * 160 + (lane & 7) * 16, sv[i]);
      signal(E_SRDY, lane);
    }
    STAMP(10);
    STAMP_FLUSH();
    asm volatile(".Lfused_end_a:");
    return;
  }

  auto poll_f1 = [&](unsigned tag) {
    poll_words4(ws, WS_F1 + lane * 128, tag);
    signal(E_F1OK, lane);
    if (blockIdx.x == 0 && lane == 0) __builtin_amdgcn_raw_buffer_store_b32(lds32(W_TAG + 4) + 1u, ws, WS_EPOCH, 0, SC1);
  };
  if (role == 1) {
    const int h = lane >> 5, fi = 4 * xcd + slot - 28;
    const bool extra = !gemm_wg && w == 0 && fi < 28;
    const unsigned stg = gemm_wg ? RSTG + w * 4096 : 98304 + w * 8192;
    Frag4 rt = {};
    BAR();
    if (gemm_wg) {
      stage_x(1); stage_w(1);
      WAITV(8); signal(E_FULL, lane);
      STAMP(1);
      if (w < 2) rt = retile_read(stg, lane);
      WAITV(0); signal(E_FULL + 4, lane);
    }
    STAMP(2);
    PRIO(LPRIO);
    if (extra) retile_load(p, 512 + fi, stg + 4096, lane);
    if (!gemm_wg && w < 2) { WAITV(0); rt = retile_read(stg, lane); }
    const unsigned tag = epoch_tag(ev, w == 0, lane);
    ln_compute(ln, ws, 8 * tb + 2 * w + h, 2 * w + h, tag, lane);
    signal(E_LN, lane);
    if (w >= 2) {
      await(E_LN, 4);
      ln_publish(ws, tb, w - 2, lane);
      signal(E_LNPUB, lane);
      STAMP(3);
      publish(ws, C_F1, 2, WS_F1 + fslot, tag, lane);
      STAMP(4);
      PRIO(0);
      if (gemm_wg && w == 2) { await<4>(E_VMQ, 4); poll_f1(tag); }
    } else {
      PRIO(0);
      if (gemm_wg) await<4>(E_VMQ, 4);
      retile_write(ws, 2 * tb + w, rt, lane);
      if (extra) retile_write(ws, 512 + fi, retile_read(stg + 4096, lane), lane);
      STAMP(3);
      publish(ws, C_FW, 2, WS_FW + fslot, tag, lane);
      STAMP(4);
    }
  }

  if (!gemm_wg) {
    const unsigned grp = role * (4 * KSCR + 4 * FPOS), cnt = C_GRP + role * 48;
    const KgIn kin = kg_load(p, dg + (w >> 1), lane);
    if (role == 0) BAR();
    const float isc = ssm_prepare(ws, kin, grp, grp + 4 * KSCR, 0, cnt, dg, w, lane, [&] {
      if (wave == 0) { await(E_LN, 4); poll_f1(lds_tag()); } else { await<4>(E_F1OK, 1); }
    });
    STAMP(16);
    conv_channel(ws, grp + (w >> 1) * CH_BYTES, cnt + 16 + (w >> 1) * 4, dg + (w >> 1), w & 1, isc, kin.dsk, lane);
    WAITV(0);
    group_sync(C_H, 8, lane);
    if (role == 0) { signal(E_GELU, lane); signal(E_GELU, lane); }
    STAMP(5);
  } else if (role == 0) {
    const int wf = w & 1, wt = w >> 1, h = lane >> 5;
    BAR();
    PRIO(2);
    unsigned aa[2][2], ba[2][4];
    {
      const int q = (lane & 15) >> 2, pp = lane & 3, fb = (lane >> 4) & 1;
#pragma unroll
      for (int mi = 0; mi < 2; ++mi) {
        aa[0][mi] = (8 * h + q) * 256 + 16 * ((8 * wf + 4 * (mi ^ wt) + 2 * fb + (pp >> 1)) ^ (q << 2)) + 8 * (pp & 1);
        aa[1][mi] = aa[0][mi] + 4 * UNIT;
        asm volatile("" : "+v"(aa[1][mi]));
      }
#pragma unroll
      for (int s = 0; s < 4; ++s) {
        ba[0][s] = (64 * wt + (lane & 31)) * 128 + 16 * ((2 * s + h) ^ ((lane >> 1) & 7));
        ba[1][s] = ba[0][s] + 4 * UNIT;
        asm volatile("" : "+v"(ba[1][s]));
      }
    }
    f16v acc[2][2] = {};
    v4u A[3][2], B[3][2], st[2];
    unsigned b1v[2], fl = 0, dn = 0, cbase = CNT, sbase = sig_base(lane), one = 1u, vxo = vx0 ^ 64;
    asm volatile("" : "+v"(cbase), "+v"(sbase), "+v"(one), "+v"(vxo));
    f4v cs4 = {};
    const unsigned sel1 = (((lane >> 2) ^ (lane >> 4)) & 1) ? 0u : 0x3f803f80u;
    const v4u sel = {sel1, sel1, sel1, sel1};
    auto rdA = [&](int n, int mi) {
      const int u = (2 * (n >> 2) + 1) % 7;
      const unsigned a = aa[u >> 2][mi] + (u & 3) * UNIT + (n & 3) * 4096;
      A[n % 3][mi] = ldstr2(a, a + 1024);
    };
    auto rdB = [&](int n, int ni) {
      const int u = (2 * (n >> 2)) % 7;
      B[n % 3][ni] = lds128(ba[u >> 2][n & 3] + (u & 3) * UNIT + ni * 4096);
    };
    const v4u xs = make_srd(p.x, X_BYTES), w1s = make_srd(p.w1, W_BYTES);
    unsigned sx[4], sw[4], mb = w * 4096;
#pragma unroll
    for (int j = 0; j < 4; ++j) {
      sx[j] = (LT0 + 32 * w) * 1280 + ((3 + ROTV) % 10) * 128 + j * 9216;
      sw[j] = (((3 + ROTV) % 10) * 64 + 16 * w) * 3456 + 2 * LF0 + j * 12800;
      asm volatile("" : "+s"(sx[j]), "+s"(sw[j]));
    }
    auto piece = [&](auto qc) {
      constexpr int q = qc, i = (q + 8) / 8 + 2 + ((q >> 2) & 1), j = q & 3;
      if constexpr (q >= -4 && q < 52) {
        if constexpr (q & 4) {
          if constexpr (j == 0) dma1(xs, vx0, sx[0], mb + ((2 * i) % 7) * UNIT);
          else dma_next<j * 1024>(xs, (j & 1) ? vxo : vx0, sx[j]);
          sx[j] += (i + ROTV) % 10 == 9 ? -9 * 128 : 128;
          asm volatile("" : "+s"(sx[j]));
        } else {
          if constexpr (j == 0) dma1(w1s, vw0, sw[0], mb + ((2 * i + 1) % 7) * UNIT);
          else dma_next<j * 1024>(w1s, vw0, sw[j]);
          sw[j] += (i + ROTV) % 10 == 9 ? -9 * 64 * 3456 : 64 * 3456;
          asm volatile("" : "+s"(sw[j]));
        }
      }
    };
    auto load_stats = [&] {
#pragma unroll
      for (int i = 0; i < 2; ++i) st[i] = __builtin_amdgcn_raw_buffer_load_b128(ws, (lane & 31) * 16, WS_STAT + (t0 + 64 * wt + 32 * i) * 16, SC1);
    };
    await(E_FULL, 4);
    STAMP(2); STAMPC(11);
    rdA(0, 0); rdB(0, 0); rdB(0, 1); rdA(0, 1); rdA(1, 0); rdB(1, 0); rdB(1, 1); rdA(1, 1);
    SCHED();
    unsigned tag = 0, tf = 0, td = 0, tv = 0;
    v4u eb[2];
    static_for<40>([&](auto nc) {
      constexpr int n = nc, sg = n >> 2, r = n & 3;
      acc[0][0] = mfma32b(A[n % 3][0], B[n % 3][0], acc[0][0]);
      SCHED();
      if (n < 38) { rdA(n + 2, 0); rdB(n + 2, 0); }
      SCHED();
      acc[0][1] = mfma32b(A[n % 3][0], B[n % 3][1], acc[0][1]);
      SCHED();
      if constexpr (n == 2) { piece(IC<-4>{}); piece(IC<-3>{}); } else if constexpr (n > 2) piece(IC<2 * (n - 3)>{});
      if (r == 1 && sg < 9) fl = *(volatile LAS unsigned*)lp(cbase + (E_FULL - CNT) + (sg + 1) * 4);
      if (r == 2 && sg < 7) dn = *(volatile LAS unsigned*)lp(cbase + (E_DONE - CNT) + sg * 4);
      if (n == 34) { dn = *(volatile LAS unsigned*)lp(cbase + (E_LN - CNT)); tag = *(volatile LAS unsigned*)lp(cbase + (W_TAG - CNT)); }
      SCHED();
      cs4 = mfma16b(sel, A[n % 3][0], cs4);
      SCHED();
      acc[1][0] = mfma32b(A[n % 3][1], B[n % 3][0], acc[1][0]);
      SCHED();
      if (n < 38) { rdB(n + 2, 1); rdA(n + 2, 1); }
      if (r == 1) signal_at<E_DONE - CNT + sg * 4>(sbase, one);
      if (n == 39) { sts32(SEXC + w * 128 + (lane & 31) * 4, f2u(cs4[0])); signal(E_SX + w * 4, lane); }
      SCHED();
      acc[1][1] = mfma32b(A[n % 3][1], B[n % 3][1], acc[1][1]);
      SCHED();
      if constexpr (n == 2) { piece(IC<-2>{}); piece(IC<-1>{}); } else if constexpr (n > 2) piece(IC<2 * (n - 3) + 1>{});
      if (r == 1 && sg < 9) { CHK(fl, E_FULL + (sg + 1) * 4, 4, tf); if (sg & 1) STAMPL(16 + (sg >> 1)); }
      if (r == 2 && sg < 7) CHK(dn, E_DONE + sg * 4, 4, td);
      if (r == 0 && sg >= 1 && sg < 9) {
        if (sg < 8) TACC(tv, WAITV(8)); else TACC(tv, WAITV(4));
        signal_at<E_FULL - CNT + (sg + 1) * 4>(sbase, one);
      }
      if (n == 29) load_stats();
      if (n == 30) {
#pragma unroll
        for (int i = 0; i < 2; ++i)
          b1v[i] = __builtin_amdgcn_raw_buffer_load_b16(make_rsrc(p.b1, F_ * 2), (lane & 31) * 2, (f0 + 64 * wf + 32 * (i ^ wt)) * 2, 0);
        signal_at<E_VMQ - CNT>(sbase, one);
      }
      if (n == 35) {
        if (__builtin_expect(__builtin_amdgcn_readfirstlane(dn) < 4, 0)) { await(E_LN, 4); tag = peek(W_TAG); }
        tag = __builtin_amdgcn_readfirstlane(tag);
      }
      if (n == 37) {
        if (__builtin_expect(!__all(st[0].y == tag && st[0].w == tag && st[1].y == tag && st[1].w == tag), 0)) {
#pragma unroll 1
          for (unsigned s = 0; s < SPIN_CAP; ++s) {
            asm volatile("" ::: "memory");
            load_stats();
            if (__all(st[0].y == tag && st[0].w == tag && st[1].y == tag && st[1].w == tag)) break;
          }
        }
      }
      if (n == 37 || n == 38) {
        constexpr int i = n == 38;
        const float mu = -u2f(st[i].x), ir = u2f(st[i].z), mh = bfr(mu), ih = bfr(ir);
        eb[i] = h ? (v4u){0u, 0u, 0u, 0u} : (v4u){pkbf(mh, opq(mu - mh)), pkbf(mh, 0.f), pkbf(ih, opq(ir - ih)), 0u};
        asm volatile("" : "+v"(eb[i]));
      }
      SCHED();
    });
    STAMP(3); STAMPC(12); STAMPV(29, tf); STAMPV(30, td); STAMPV(31, tv);
    auto ext = [&](int mi, float S) {
      const float sh = bfr(S);
      const v4u ea = h ? (v4u){0u, 0u, 0u, 0u} : (v4u){pkbf(sh, sh), pkbf(S - sh, 0.f), b1v[mi] | (b1v[mi] << 16), 0u};
      acc[mi][0] = mfma32b(ea, eb[0], acc[mi][0]);
      acc[mi][1] = mfma32b(ea, eb[1], acc[mi][1]);
    };
    fl = peek(E_SX + (w ^ 2) * 4);
    float so = u2f(peek(SEXC + (w ^ 2) * 128 + (lane & 31) * 4));
    ext(0, cs4[0]);
    SCHED();
    if (__builtin_expect(__builtin_amdgcn_readfirstlane(fl) < 1, 0)) {
      await(E_SX + (w ^ 2) * 4, 1);
      so = u2f(peek(SEXC + (w ^ 2) * 128 + (lane & 31) * 4));
    }
    ext(1, so);
    STAMP(1);
    if (f0 + 64 * wf < F_) {
      const unsigned hv = ((lane >> 4) & 1) * 55296 + h * 256 + (lane & 15) * 16;
#pragma unroll
      for (int mi = 0; mi < 2; ++mi) {
#pragma unroll
        for (int ni = 0; ni < 2; ++ni) {
          const float ir = u2f(st[ni].z), rho = __builtin_amdgcn_rcpf(ir);
          const float k1 = -2.302208198f * rho, k2 = k1 * 0.044715f * rho * rho;
          const unsigned hs = WS_H + ((t0 + 64 * wt + 32 * ni) / 16) * 55296 + ((f0 + 64 * wf) / 32 + (mi ^ wt)) * 1024;
#pragma unroll
          for (int hf = 0; hf < 2; ++hf) {
            unsigned o[4];
#pragma unroll
            for (int r = 0; r < 4; ++r) {
              const f2v a = {acc[mi][ni][8 * hf + 2 * r], acc[mi][ni][8 * hf + 2 * r + 1]};
              const f2v x = a * (a * a * k2 + k1);
              const f2v e = {__builtin_amdgcn_exp2f(x.x), __builtin_amdgcn_exp2f(x.y)};
              const f2v dd = e * ir + ir;
              const f2v rr = {__builtin_amdgcn_rcpf(dd.x), __builtin_amdgcn_rcpf(dd.y)};
              const f2v gl = a * rr;
              o[r] = pkbf(gl.x, gl.y);
            }
            __builtin_amdgcn_raw_buffer_store_b128((v4u){o[0], o[1], o[2], o[3]}, ws, HADDR, 0, SC1);
            SCHED();
          }
        }
        if (mi == 0) signal(E_GELU, lane);
      }
    } else {
      signal(E_GELU, lane);
    }
    signal(E_GELU, lane);
    STAMP(4); STAMPC(13);
    publish(ws, C_H, 4, WS_F2 + (rp * 16 + cp) * 128, tag, lane);
    STAMP(5);
  }

  if (role == 0) {
    const unsigned tag = lds_tag();
    const int mh = w & 1, kh = w >> 1, g = lane >> 4;
    const int tok = 64 * tt + 32 * mh + 16 * kh + (lane & 15);
    poll_words(ws, WS_F2 + ((tt >> 1) * 16 + min(lane, 13)) * 128, tag);
    STAMP(6);
    PRIO(2);
    constexpr int W = 6;
    v4u H[W][2], A[2][5];
    f4v acc[2][5] = {};
    const unsigned hs = WS_H + (4 * tt + 2 * mh) * 55296 + 27 * kh * 1024;
    const unsigned hv[2] = {lane * 16 + kh * 55296u, lane * 16 + (kh ^ 1) * 55296u};
#pragma unroll
    for (int k = 0; k < W; ++k)
#pragma unroll
      for (int ti = 0; ti < 2; ++ti) H[k][ti] = __builtin_amdgcn_raw_buffer_load_b128(ws, hv[ti], hs + k * 1024, SC1);
    SCHED();
    v2u xv[5], bv[5];
    unsigned ab[3];
#pragma unroll
    for (int i = 0; i < 3; ++i) { ab[i] = kh * 15360 + lane * 16 + 2 * i * RING2; asm volatile("" : "+v"(ab[i])); }
    auto rd = [&](int k, int n) { A[k & 1][n] = lds128(ab[((k / 3) % 5) >> 1] + (((k / 3) % 5) & 1) * RING2 + (k % 3) * 5120 + n * 1024); };
    unsigned fl = 0, cbase = CNT, sbase = sig_base(lane), one = 1u;
    asm volatile("" : "+v"(cbase), "+v"(sbase), "+v"(one));
    await<1>(E_FULL + 10 * 4, 4);
#pragma unroll
    for (int n = 0; n < 5; ++n) rd(0, n);
    STAMP(7); STAMPC(14);
    SCHED();
    static_for<27>([&](auto kc) {
      constexpr int k = kc;
#pragma unroll
      for (int ti = 0; ti < 2; ++ti) {
#pragma unroll
        for (int n = 0; n < 5; ++n) {
          acc[ti][n] = mfma16b(A[k & 1][n], H[k % W][ti], acc[ti][n]);
          SCHED();
          if (ti == 0 && k < 26) {
            rd(k + 1, n);
            if (n == 4 && k % 3 == 1) signal_at<E_DONE - CNT + (10 + k / 3) * 4>(sbase, one);
            SCHED();
          }
          if (ti == 1 && n == 0 && k % 3 == 1 && k < 24) { fl = *(volatile LAS unsigned*)lp(cbase + (E_FULL - CNT) + (11 + k / 3) * 4); SCHED(); }
        }
        if (k + W < 27) H[k % W][ti] = __builtin_amdgcn_raw_buffer_load_b128(ws, hv[ti], hs + (k + W) * 1024, SC1);
        if constexpr (k >= 21 && k < 26) {
          if (ti == 0) xv[k - 21] = __builtin_amdgcn_raw_buffer_load_b64(xr, tok * 1280 + 8 * g + 32 * (k - 21), 160 * ct, 0);
          else bv[k - 21] = __builtin_amdgcn_raw_buffer_load_b64(make_rsrc(p.b2, D_ * 2), 8 * g + 32 * (k - 21), 160 * ct, 0);
        }
        SCHED();
      }
      if (k % 3 == 1 && k < 24) { check(fl, E_FULL + (11 + k / 3) * 4, 4); STAMPL(22 + k / 3); SCHED(); }
    });
    STAMP(8); STAMPC(15);
    await<1>(E_DONE + 14 * 4, 4);
#pragma unroll
    for (int n = 0; n < 5; ++n) sts128(RED2 + w * 5120 + n * 1024 + lane * 16, __builtin_bit_cast(v4u, acc[1][n]));
    signal(E_RED + w * 4, lane);
    await<1>(E_SRDY, 2);
    await<1>(E_RED + (w ^ 2) * 4, 1);
    STAMP(9);
    const unsigned sa = STILE + (4 * g + ((lane & 15) >> 2)) * 160 + (32 * mh + 16 * kh + 4 * (lane & 3)) * 2;
#pragma unroll
    for (int n = 0; n < 5; ++n) {
      const f4v o = __builtin_bit_cast(f4v, lds128(RED2 + (w ^ 2) * 5120 + n * 1024 + lane * 16));
      const v2u sv = ldstr(sa + n * 2560);
      const float bz[4] = {blo(bv[n].x), bhi(bv[n].x), blo(bv[n].y), bhi(bv[n].y)};
      const float sz[4] = {blo(sv.x), bhi(sv.x), blo(sv.y), bhi(sv.y)};
      const float xz[4] = {blo(xv[n].x), bhi(xv[n].x), blo(xv[n].y), bhi(xv[n].y)};
      float r[4];
#pragma unroll
      for (int i = 0; i < 4; ++i) {
        const float z = acc[0][n][i] + o[i] + bz[i];
        const float gt = z * __builtin_amdgcn_rcpf(1.f + __builtin_amdgcn_exp2f(z * -1.44269504088896f));
        r[i] = fmaf(sz[i], gt, xz[i]);
      }
      *(GAS v2u*)(p.out + tok * 640 + 80 * ct + 16 * n + 4 * g) = (v2u){pkbf(r[0], r[1]), pkbf(r[2], r[3])};
    }
    STAMP(10);
    asm volatile(".Lfused_end_b:");
  } else {
    if (gemm_wg) await<4>(E_GELU, FWPOLL_AT);
    if (w == 0) { poll_words4(ws, WS_FW + lane * 128, lds_tag()); signal(E_FWOK, lane); } else { await<2>(E_FWOK, 1); }
    if (gemm_wg) await<2>(E_GELU, 8);
    STAMP(5);
    PRIO(3);
#pragma unroll 1
    for (int c = 0; c < 3; ++c) w2_chunk<PACE0>(wss, ct, w, c, lane);
    WAITV(16); signal(E_FULL + 10 * 4, lane);
    WAITV(8); signal(E_FULL + 11 * 4, lane);
    WAITV(0); signal(E_FULL + 12 * 4, lane);
    STAMP(6);
    await<4>(E_CONV, 4);
    STAMP(7);
    w2_chunk<0>(wss, ct, w, 3, lane);
    w2_chunk<0>(wss, ct, w, 4, lane);
    WAITV(8);
    signal(E_FULL + 13 * 4, lane);
#pragma unroll 1
    for (int c = 5; c < 9; ++c) {
      await(E_DONE + (5 + c) * 4, 4);
      w2_chunk<0>(wss, ct, w, c, lane);
      WAITV(8);
      signal(E_FULL + (9 + c) * 4, lane);
      STAMP(19 + c);
    }
    WAITV(0);
    signal(E_FULL + 18 * 4, lane);
    STAMP(10);
  }
  STAMP_FLUSH();
}

}

extern "C" void kernel_launch(void* const* d_in, const int* in_sizes, int n_in, void* d_out, int out_size, void* d_ws, size_t ws_size,
                              hipStream_t stream) {
  Params p;
  p.x = (const unsigned short*)d_in[0];
  p.lA = (const unsigned short*)d_in[1];
  p.Ai = (const unsigned short*)d_in[2];
  p.Cr = (const unsigned short*)d_in[3];
  p.Ci = (const unsigned short*)d_in[4];
  p.ldt = (const unsigned short*)d_in[5];
  p.Dsk = (const unsigned short*)d_in[6];
  p.w1 = (const unsigned short*)d_in[8];
  p.b1 = (const unsigned short*)d_in[9];
  p.w2 = (const unsigned short*)d_in[10];
  p.b2 = (const unsigned short*)d_in[11];
  p.out = (unsigned short*)d_out;
  p.ws = (unsigned char*)d_ws;
  (void)hipFuncSetAttribute((const void*)fused_kernel, hipFuncAttributeMaxDynamicSharedMemorySize, LDS_BYTES);
  fused_kernel<<<256, 768, LDS_BYTES, stream>>>(p);
}
```

```cpp
#define HOLD_AT 1
#define HOLD_US 14
#include <hip/hip_runtime.h>
#include <hip/hip_bf16.h>

namespace {

typedef unsigned v4u __attribute__((ext_vector_type(4)));
typedef unsigned v2u __attribute__((ext_vector_type(2)));
typedef short s4v __attribute__((ext_vector_type(4)));
typedef float f2v __attribute__((ext_vector_type(2)));
typedef float f4v __attribute__((ext_vector_type(4)));
typedef float f16v __attribute__((ext_vector_type(16)));
typedef _Float16 h2v __attribute__((ext_vector_type(2)));
typedef _Float16 h8v __attribute__((ext_vector_type(8)));
typedef __bf16 b2v __attribute__((ext_vector_type(2)));
typedef __bf16 b8v __attribute__((ext_vector_type(8)));
#define GAS __attribute__((address_space(1)))
#define LAS __attribute__((address_space(3)))
#define DEV __device__ __forceinline__
typedef __amdgpu_buffer_rsrc_t rsrc_t;

constexpr int L_ = 2048, D_ = 640, F_ = 1728;
constexpr unsigned X_BYTES = L_ * D_ * 2, W_BYTES = D_ * F_ * 2;
constexpr int SC1 = 16;
constexpr unsigned SPIN_CAP = 1u << 16;

constexpr unsigned WS_EPOCH = 0;
constexpr unsigned WS_F1 = 4096;
constexpr unsigned WS_F2 = 65536;
constexpr unsigned WS_FW = 24576;
constexpr unsigned WS_F3 = 16384;
constexpr unsigned WS_STAT = 32768;
constexpr unsigned WS_XNT = 131072;
constexpr unsigned WS_W2T = 2752512;
constexpr unsigned WS_H = 5242880;
constexpr unsigned WS_ST = 12582912;
constexpr unsigned WS_STAMP = 15204352;
constexpr unsigned WS_BYTES = WS_STAMP + 256 * 12 * 64 * 8;

constexpr unsigned LDS_BYTES = 163840;
constexpr unsigned UNIT = 16384;
constexpr unsigned KSCR = 8704;
constexpr unsigned FPOS = 2576;
constexpr unsigned GRP = 7 * UNIT;
constexpr unsigned LNST = GRP + 4 * KSCR;
constexpr unsigned CNT = LNST + 10752;
constexpr unsigned SEXC = CNT + 512;
constexpr unsigned JUNK = CNT + 1024;
constexpr unsigned RSTG = 6 * UNIT;
constexpr unsigned UCP = 2368;
constexpr unsigned CH_R3 = 4 * UCP, CH_U = 9856, CH_BYTES = 2 * KSCR;
constexpr unsigned RING2 = 30720;
constexpr unsigned STILE = 4 * RING2, RED2 = STILE + 12800;
constexpr unsigned E_FULL = CNT, E_DONE = CNT + 80;
constexpr unsigned E_GELU = CNT + 160, E_LN = CNT + 164, E_LNPUB = CNT + 168, E_SRDY = CNT + 172, E_RED = CNT + 176, E_SX = CNT + 192;
constexpr unsigned C_H = CNT + 208, C_F1 = CNT + 212, E_F1OK = CNT + 216, E_CONV = CNT + 220, E_VMQ = CNT + 76, C_FW = CNT + 156, E_FWOK = CNT + 360;
constexpr unsigned C_GRP = CNT + 224;
constexpr unsigned W_TAG = CNT + 368;
constexpr int N_ZERO = 92;

struct Params {
  const unsigned short *x, *lA, *Ai, *Cr, *Ci, *ldt, *Dsk, *w1, *b1, *w2, *b2;
  unsigned short* out;
  unsigned char* ws;
};

#define STAMP(i) do { } while (0)
#define STAMPC(i) do { } while (0)
#define STAMPV(i, v) do { } while (0)
#define STAMP_INIT() do { } while (0)
#define STAMP_FLUSH() do { } while (0)

#define PRIO(x) __builtin_amdgcn_s_setprio(x)
#define PACE0 1
#define APRIO 0
#define LT0 t0
#define LF0 f0
#undef TACC
#define TACC(acc, stmt) do { stmt; } while (0)
#define LPRIO 1
#define FWPOLL_AT 4
#define STAMPL(i) do { } while (0)
#define CHK(e, a, n, t) check(e, a, n)
#define HADDR (hv + hs + hf * 512)
#define ROTV (((slot / 7 * 5 + 1) / 2 + slot % 7 * 3) % 10)
#define BAR() asm volatile("s_waitcnt lgkmcnt(0)\n\ts_barrier" ::: "memory")
#define WAITV(n) asm volatile("s_waitcnt vmcnt(" #n ")" ::: "memory")
#define SCHED() __builtin_amdgcn_sched_barrier(0)

template <int I> struct IC { constexpr operator int() const { return I; } };
template <int N, int I = 0, class F> DEV void static_for(F&& f) {
  if constexpr (I < N) { f(IC<I>{}); static_for<N, I + 1>(f); }
}
DEV float u2f(unsigned u) { return __builtin_bit_cast(float, u); }
DEV unsigned f2u(float f) { return __builtin_bit_cast(unsigned, f); }
DEV h2v u2h(unsigned u) { return __builtin_bit_cast(h2v, u); }
DEV float blo(unsigned u) { return u2f(u << 16); }
DEV float bhi(unsigned u) { return u2f(u & 0xffff0000u); }
DEV unsigned pkbf(float a, float b) { return __builtin_bit_cast(unsigned, __builtin_convertvector((f2v){a, b}, b2v)); }
DEV unsigned pkh(float a, float b) { return __builtin_bit_cast(unsigned, __builtin_convertvector((f2v){a, b}, h2v)); }
DEV float opq(float x) { asm("" : "+v"(x)); return x; }
DEV float bfr(float a) { return blo(pkbf(a, a)); }
DEV f16v mfma32b(v4u a, v4u b, f16v c) { return __builtin_amdgcn_mfma_f32_32x32x16_bf16(__builtin_bit_cast(b8v, a), __builtin_bit_cast(b8v, b), c, 0, 0, 0); }
DEV f16v mfma32h(v4u a, v4u b, f16v c) { return __builtin_amdgcn_mfma_f32_32x32x16_f16(__builtin_bit_cast(h8v, a), __builtin_bit_cast(h8v, b), c, 0, 0, 0); }
DEV f4v mfma16b(v4u a, v4u b, f4v c) { return __builtin_amdgcn_mfma_f32_16x16x32_bf16(__builtin_bit_cast(b8v, a), __builtin_bit_cast(b8v, b), c, 0, 0, 0); }
DEV f4v mfma16h(v4u a, v4u b, f4v c) { return __builtin_amdgcn_mfma_f32_16x16x32_f16(__builtin_bit_cast(h8v, a), __builtin_bit_cast(h8v, b), c, 0, 0, 0); }

DEV LAS unsigned char* lp(unsigned a) { return (LAS unsigned char*)(size_t)a; }
DEV v4u lds128(unsigned a) { return *(LAS v4u*)lp(a); }
DEV v2u lds64(unsigned a) { return *(LAS v2u*)lp(a); }
DEV unsigned lds32(unsigned a) { return *(LAS unsigned*)lp(a); }
DEV void sts128(unsigned a, v4u v) { *(LAS v4u*)lp(a) = v; }
DEV void sts64(unsigned a, v2u v) { *(LAS v2u*)lp(a) = v; }
DEV void sts32(unsigned a, unsigned v) { *(LAS unsigned*)lp(a) = v; }
DEV v2u ldstr(unsigned a) { return __builtin_bit_cast(v2u, __builtin_amdgcn_ds_read_tr16_b64_v4i16((LAS s4v*)lp(a))); }
DEV v4u ldstr2(unsigned a0, unsigned a1) { const v2u x = ldstr(a0), y = ldstr(a1); return (v4u){x.x, x.y, y.x, y.y}; }

DEV v4u make_srd(const void* ptr, unsigned bytes) {
  return (v4u){(unsigned)(size_t)ptr, (unsigned)((size_t)ptr >> 32) & 0xffffu, bytes, 0x00020000u};
}
DEV rsrc_t make_rsrc(const void* ptr, unsigned bytes) { return __builtin_amdgcn_make_buffer_rsrc((void*)ptr, 0, bytes, 0x00020000); }
#define DMA_HEAD "s_mov_b32 m0, %0\n\ts_nop 0\n\t"
DEV void dma1(v4u srd, unsigned v, unsigned soff, unsigned lds) {
  asm volatile(DMA_HEAD "buffer_load_dwordx4 %1, %2, %3 offen lds" ::"s"(lds), "v"(v), "s"(srd), "s"(soff) : "memory");
}
template <int OFF> DEV void dma_next(v4u srd, unsigned v, unsigned soff) {
  asm volatile("buffer_load_dwordx4 %0, %1, %2 offen offset:%3 lds" ::"v"(v), "s"(srd), "s"(soff), "i"(OFF) : "memory");
}
DEV void touch(v4u srd, unsigned v) {
}
DEV void dma4(v4u srd, const unsigned (&v)[4], unsigned soff, unsigned lds) {
  asm volatile(DMA_HEAD "buffer_load_dwordx4 %1, %5, %6 offen lds\n\tbuffer_load_dwordx4 %2, %5, %6 offen offset:1024 lds\n\t"
               "buffer_load_dwordx4 %3, %5, %6 offen offset:2048 lds\n\tbuffer_load_dwordx4 %4, %5, %6 offen offset:3072 lds"
               ::"s"(lds), "v"(v[0]), "v"(v[1]), "v"(v[2]), "v"(v[3]), "s"(srd), "s"(soff) : "memory");
}
DEV void dma4_lin_sc1(v4u srd, unsigned v, unsigned soff, unsigned lds) {
  asm volatile(DMA_HEAD "buffer_load_dwordx4 %1, %2, %3 offen sc1 lds\n\tbuffer_load_dwordx4 %1, %2, %3 offen offset:1024 sc1 lds\n\t"
               "buffer_load_dwordx4 %1, %2, %3 offen offset:2048 sc1 lds\n\tbuffer_load_dwordx4 %1, %2, %3 offen offset:3072 sc1 lds"
               ::"s"(lds), "v"(v), "s"(srd), "s"(soff) : "memory");
}

DEV void poll_words(rsrc_t ws, unsigned off, unsigned tag) {
#pragma unroll 1
  for (unsigned s = 0; s < SPIN_CAP; ++s) {
    asm volatile("" ::: "memory");
    if (__all(__builtin_amdgcn_raw_buffer_load_b32(ws, off, 0, SC1) == tag)) break;
  }
  asm volatile("" ::: "memory");
}
DEV void poll_words4(rsrc_t ws, unsigned off, unsigned tag) {
#pragma unroll 1
  for (unsigned s = 0; s < SPIN_CAP; ++s) {
    asm volatile("" ::: "memory");
    const v4u v = __builtin_amdgcn_raw_buffer_load_b128(ws, off, 0, SC1);
    if (__all(v.x == tag && v.y == tag && v.z == tag && v.w == tag)) break;
    __builtin_amdgcn_s_sleep(8);
  }
  asm volatile("" ::: "memory");
}
DEV unsigned sig_base(int lane) { return lane == 0 ? CNT : JUNK + lane * 4; }
template <int OFF> DEV void signal_at(unsigned base, unsigned one) {
  asm volatile("ds_add_u32 %0, %1 offset:%2" ::"v"(base), "v"(one), "i"(OFF) : "memory");
}
DEV void signal(unsigned a, int lane) {
  asm volatile("ds_add_u32 %0, %1" ::"v"(sig_base(lane) + (a - CNT)), "v"(1u) : "memory");
}
DEV unsigned peek(unsigned a) { return *(volatile LAS unsigned*)lp(a); }
template <int SLEEP = 1> DEV void await(unsigned a, unsigned need) {
#pragma unroll 1
  for (unsigned s = 0; s < (SPIN_CAP << 2); ++s) {
    asm volatile("" : "+s"(s));
    if (__builtin_amdgcn_readfirstlane(peek(a)) >= need) break;
    __builtin_amdgcn_s_sleep(SLEEP);
  }
  asm volatile("" ::: "memory");
}
DEV void check(unsigned early, unsigned a, unsigned need) {
  if (__builtin_expect(__builtin_amdgcn_readfirstlane(early) < need, 0)) await<1>(a, need);
  asm volatile("" ::: "memory");
}
DEV unsigned lds_arrive(unsigned cnt, int lane) {
  unsigned old;
  asm volatile("ds_add_rtn_u32 %0, %1, %2\n\ts_waitcnt lgkmcnt(0)" : "=&v"(old) : "v"(sig_base(lane) + (cnt - CNT)), "v"(1u) : "memory");
  return __builtin_amdgcn_readfirstlane(old);
}
template <int SLEEP = 1> DEV void group_sync(unsigned cnt, unsigned target, int lane) {
  if (lds_arrive(cnt, lane) + 1 < target) await<SLEEP>(cnt, target);
}
DEV void publish(rsrc_t ws, unsigned cnt, unsigned nwaves, unsigned flag_off, unsigned tag, int lane) {
  WAITV(0);
  if (lds_arrive(cnt, lane) + 1 == nwaves && lane == 0) __builtin_amdgcn_raw_buffer_store_b32(tag, ws, flag_off, 0, SC1);
}
DEV unsigned epoch_load(rsrc_t ws) { return __builtin_amdgcn_raw_buffer_load_b32(ws, WS_EPOCH, 0, SC1); }
DEV unsigned epoch_tag(unsigned ev, bool share, int lane) {
  const unsigned e = __builtin_amdgcn_readfirstlane(ev);
  const unsigned tag = e * 0x9E3779B1u + 0x7F4A7C15u;
  if (share && lane == 0) sts64(W_TAG, (v2u){tag, e});
  return tag;
}
DEV unsigned lds_tag() { return __builtin_amdgcn_readfirstlane(lds32(W_TAG)); }

template <int CTRL> DEV float dpp_add(float v) {
  return v + u2f(__builtin_amdgcn_update_dpp(0u, f2u(v), CTRL, 0xf, 0xf, false));
}
template <int CTRL> DEV float dpp_max(float v) {
  return fmaxf(v, u2f(__builtin_amdgcn_update_dpp(0u, f2u(v), CTRL, 0xf, 0xf, false)));
}
DEV float half_sum(float v) {
  v = dpp_add<0x128>(v); v = dpp_add<0x124>(v); v = dpp_add<0x122>(v); v = dpp_add<0x121>(v);
  const auto r = __builtin_amdgcn_permlane16_swap(f2u(v), f2u(v), false, false);
  return u2f(r[0]) + u2f(r[1]);
}
DEV float wave_max(float v) {
  v = dpp_max<0x128>(v); v = dpp_max<0x124>(v); v = dpp_max<0x122>(v); v = dpp_max<0x121>(v);
  const auto r = __builtin_amdgcn_permlane16_swap(f2u(v), f2u(v), false, false);
  v = fmaxf(u2f(r[0]), u2f(r[1]));
  const auto q = __builtin_amdgcn_permlane32_swap(f2u(v), f2u(v), false, false);
  return fmaxf(u2f(q[0]), u2f(q[1]));
}

struct LnIn { v2u v[5]; };
DEV LnIn ln_load(rsrc_t xr, int token, int lane) {
  LnIn r;
#pragma unroll
  for (int m = 0; m < 5; ++m) r.v[m] = __builtin_amdgcn_raw_buffer_load_b64(xr, token * 1280 + (lane & 31) * 8 + m * 256, 0, 0);
  return r;
}
DEV void ln_compute(const LnIn& in, rsrc_t ws, int token, int row, unsigned tag, int lane) {
  float xv[20], s = 0.f, q = 0.f;
#pragma unroll
  for (int m = 0; m < 5; ++m) { xv[4 * m] = blo(in.v[m].x); xv[4 * m + 1] = bhi(in.v[m].x); xv[4 * m + 2] = blo(in.v[m].y); xv[4 * m + 3] = bhi(in.v[m].y); }
#pragma unroll
  for (int m = 0; m < 20; ++m) { s = opq(s + xv[m]); q = opq(fmaf(xv[m], xv[m], q)); }
  s = half_sum(s); q = half_sum(q);
  const float mean = s * (1.f / 640.f);
  const float var = fmaxf(fmaf(-mean, mean, q * (1.f / 640.f)), 0.f);
  const float m1 = bfr(mean), s1 = bfr(__builtin_amdgcn_sqrtf(bfr(bfr(var) + 1e-5f)));
  const float r1 = __builtin_amdgcn_rcpf(s1);
  const float m2 = bfr((mean - m1) * r1), s2 = bfr(__builtin_amdgcn_sqrtf(bfr(bfr(var * r1 * r1) + 1e-5f)));
  if ((lane & 31) == 0)
    __builtin_amdgcn_raw_buffer_store_b128((v4u){f2u(fmaf(m2, s1, m1)), tag, f2u(s1 * s2), tag}, ws, WS_STAT + token * 16, 0, SC1);
  const float c = -m1 * r1;
#pragma unroll
  for (int m = 0; m < 5; ++m) {
    const v2u o = {pkh(opq(fmaf(xv[4 * m], r1, c)), opq(fmaf(xv[4 * m + 1], r1, c))), pkh(opq(fmaf(xv[4 * m + 2], r1, c)), opq(fmaf(xv[4 * m + 3], r1, c)))};
    sts64(LNST + row * 1344 + (lane & 31) * 8 + m * 256, o);
  }
}
DEV void ln_publish(rsrc_t ws, int tb, int aw, int lane) {
  const unsigned a = LNST + ((lane & 15) >> 2) * 1344 + (lane >> 4) * 32 + (lane & 3) * 8 + aw * 640;
#pragma unroll
  for (int i = 0; i < 5; ++i) {
    const v4u v = ldstr2(a + i * 128, a + i * 128 + 4 * 1344);
    __builtin_amdgcn_raw_buffer_store_b128(v, ws, WS_XNT + tb * 10240 + (aw * 320 + i * 64 + lane) * 16, 0, SC1);
  }
}

struct Cx { float r, i; };
DEV Cx cmul(Cx a, Cx b) { return {opq(fmaf(a.r, b.r, -a.i * b.i)), opq(fmaf(a.r, b.i, a.i * b.r))}; }
DEV Cx csqr(Cx a) { return {opq(fmaf(a.r, a.r, -a.i * a.i)), opq((a.r + a.r) * a.i)}; }
DEV unsigned ch(Cx a) { return pkh(a.r, a.i); }
DEV unsigned hmul(unsigned a, unsigned b) {
  unsigned t, r;
  asm("v_pk_mul_f16 %0, %1, %2 op_sel:[0,0] op_sel_hi:[0,1]" : "=v"(t) : "v"(a), "v"(b));
  asm("v_pk_fma_f16 %0, %1, %2, %3 op_sel:[1,1,0] op_sel_hi:[1,0,1] neg_lo:[0,1,0]" : "=v"(r) : "v"(a), "v"(b), "v"(t));
  return r;
}
DEV float exp_bf(float x) {
  const float t = x * 1.44268798828125f, lo = x * 7.0526077138765e-06f;
  const float n = __builtin_rintf(t);
  return bfr(__builtin_amdgcn_ldexpf(__builtin_amdgcn_exp2f((t - n) + lo), (int)fminf(fmaxf(n, -200.f), 200.f)));
}
DEV Cx cexp_at(float lg, float rh, float rl, float P) {
  const float m = __builtin_amdgcn_exp2f(lg * P), ph = fmaf(rl, P, __builtin_amdgcn_fractf(rh * P));
  return {opq(m * __builtin_amdgcn_cosf(ph)), opq(m * __builtin_amdgcn_sinf(ph))};
}
struct KgIn { unsigned lA, Ai, Cr, Ci; float dt, dsk; };
DEV KgIn kg_load(const Params& p, int d, int lane) {
  KgIn k;
  k.lA = ((const GAS unsigned*)p.lA)[d * 64 + lane]; k.Ai = ((const GAS unsigned*)p.Ai)[d * 64 + lane];
  k.Cr = ((const GAS unsigned*)p.Cr)[d * 64 + lane]; k.Ci = ((const GAS unsigned*)p.Ci)[d * 64 + lane];
  k.dt = u2f((unsigned)((const GAS unsigned short*)p.ldt)[d] << 16);
  k.dsk = u2f((unsigned)((const GAS unsigned short*)p.Dsk)[d] << 16);
  return k;
}
DEV float kg_wave(const KgIn& k, int g, unsigned scr, unsigned fp, unsigned fp_free, int lane) {
  const float dt = exp_bf(k.dt), c2 = dt + dt;
  const float mx = wave_max(fmaxf(fmaxf(fabsf(blo(k.Cr)), fabsf(bhi(k.Cr))), fmaxf(fabsf(blo(k.Ci)), fabsf(bhi(k.Ci))))) * c2;
  int ex = __builtin_amdgcn_frexp_expf(mx);
  ex = ex < -120 ? -120 : (ex > 120 ? 120 : ex);
  const float cs = c2 * __builtin_amdgcn_ldexpf(1.f, -ex);
  const int sh = g ? 0 : 16;
  const float lg = -(exp_bf(bhi(k.lA << sh)) * dt) * 1.44269504088896f, ai = bhi(k.Ai << sh) * dt;
  const Cx c0 = {opq(bhi(k.Cr << sh) * cs), opq(-bhi(k.Ci << sh) * cs)};
  const float rh = ai * 0.15915494f, rl = fmaf(ai, 0.15915494f, -rh) + ai * 6.4206382e-09f;
  const Cx v1 = cexp_at(lg, -rh, -rl, 1.f), w1 = cexp_at(lg, rh, rl, 8.f), G = cexp_at(lg, -rh, -rl, 256.f);
  const unsigned wa = scr + lane * 4, ra = scr + (lane & 31) * 272 + (lane >> 5) * 16;
  {
    const Cx w2 = csqr(w1), w3 = cmul(w2, w1), w4 = csqr(w2), w8 = csqr(w4), w12 = cmul(w8, w4), w16 = csqr(w8);
    const Cx w20 = cmul(w16, w4), w24 = csqr(w12), w28 = cmul(w24, w4);
    const unsigned o[3] = {ch(w1), ch(w2), ch(w3)};
    const unsigned bs[7] = {ch(w4), ch(w8), ch(w12), ch(w16), ch(w20), ch(w24), ch(w28)};
    sts32(wa, 0x00003c00u);
#pragma unroll
    for (int j = 0; j < 3; ++j) sts32(wa + (1 + j) * 272, o[j]);
#pragma unroll
    for (int b = 0; b < 7; ++b) {
      sts32(wa + (4 + 4 * b) * 272, bs[b]);
#pragma unroll
      for (int j = 0; j < 3; ++j) sts32(wa + (5 + 4 * b + j) * 272, hmul(bs[b], o[j]));
    }
  }
  asm volatile("" ::: "memory");
  v4u y[8];
#pragma unroll
  for (int s = 0; s < 8; ++s) y[s] = lds128(ra + s * 32);
  asm volatile("" ::: "memory");
  {
    const Cx v2 = csqr(v1), v3 = cmul(v2, v1), v4 = csqr(v2), v6 = csqr(v3), x4 = cmul(c0, v4);
    const unsigned h1 = ch(v1), h2 = ch(v2), h3 = ch(v3), h4 = ch(v4), h6 = ch(v6), hx0 = ch(c0), hx4 = ch(x4);
    sts32(wa + 0 * 272, hx0); sts32(wa + 1 * 272, hmul(hx0, h1)); sts32(wa + 2 * 272, hmul(hx0, h2)); sts32(wa + 3 * 272, hmul(hx0, h3));
    sts32(wa + 4 * 272, hx4); sts32(wa + 5 * 272, hmul(hx4, h1)); sts32(wa + 6 * 272, hmul(hx4, h2)); sts32(wa + 7 * 272, hmul(hx4, h3));
    const Cx d1 = cmul(c0, G), d2 = cmul(d1, G), d3 = cmul(d2, G), d4 = cmul(d3, G);
    const Cx G4 = csqr(csqr(G));
    const Cx d5 = cmul(d1, G4), d6 = cmul(d2, G4), d7 = cmul(d3, G4), d8 = cmul(d4, G4);
    const unsigned e1 = ch(d1), e2 = ch(d2), e3 = ch(d3);
    sts32(wa + 8 * 272, e1); sts32(wa + 9 * 272, hmul(e1, h2)); sts32(wa + 10 * 272, hmul(e1, h4)); sts32(wa + 11 * 272, hmul(e1, h6));
    sts32(wa + 12 * 272, e2); sts32(wa + 13 * 272, hmul(e2, h4)); sts32(wa + 14 * 272, e3); sts32(wa + 15 * 272, hmul(e3, h4));
    sts32(wa + 16 * 272, ch(d4)); sts32(wa + 17 * 272, ch(d5)); sts32(wa + 18 * 272, ch(d6)); sts32(wa + 19 * 272, ch(d7));
    sts32(wa + 20 * 272, ch(d8));
  }
  asm volatile("" ::: "memory");
  v4u xx[8];
#pragma unroll
  for (int s = 0; s < 8; ++s) xx[s] = lds128(ra + s * 32);
  SCHED();
  f16v acc = {};
#pragma unroll
  for (int s = 0; s < 8; ++s) acc = mfma32h(xx[s], y[s], acc);
  const int j = lane & 31, h = lane >> 5;
  await(E_LNPUB, fp_free);
  sts128(fp + (8 * j + 4 * h) * 4, (v4u){f2u(acc[0]), f2u(acc[1]), f2u(acc[2]), f2u(acc[3])});
  sts64(fp + (h ? 384 + 2 * j : 256 + 4 * j) * 4, (v2u){f2u(acc[4]), f2u(acc[5])});
  sts64(fp + (h ? 448 + 2 * j : 258 + 4 * j) * 4, (v2u){f2u(acc[6]), f2u(acc[7])});
  if (lane <= 32) sts32(fp + (512 + 128 * h + j) * 4, f2u(acc[8]));
  if (h == 0) {
#pragma unroll
    for (int i = 1; i < 4; ++i) sts32(fp + (512 + j + 32 * i) * 4, f2u(acc[8 + i]));
  }
  return __builtin_amdgcn_ldexpf(1.f, ex);
}
DEV void kg_table0(unsigned cb, unsigned fp, int g, int lane) {
  auto f1 = [&](int i) { return opq(u2f(lds32(fp + i * 4)) + u2f(lds32(fp + FPOS + i * 4))); };
  if (g == 0) {
    const int j = lane & 31, h = lane >> 5;
    const unsigned a = fp + (h ? 256 + 4 * j : 8 * j) * 4;
    const v4u l0 = lds128(a), h0 = lds128(a + 16), l1 = lds128(a + FPOS), h1 = lds128(a + FPOS + 16);
    const float e0 = opq(u2f(l0.x) + u2f(l1.x)), e1 = opq(u2f(l0.y) + u2f(l1.y)), e2 = opq(u2f(l0.z) + u2f(l1.z)), e3 = opq(u2f(l0.w) + u2f(l1.w));
    const float e4 = opq(u2f(h0.x) + u2f(h1.x)), e5 = opq(u2f(h0.y) + u2f(h1.y)), e6 = opq(u2f(h0.z) + u2f(h1.z)), e7 = opq(u2f(h0.w) + u2f(h1.w));
    const v4u o0 = {pkh(e0, e1), pkh(e2, e3), pkh(e4, e5), pkh(e6, e7)};
    const v4u o1 = {pkh(e0, e0), pkh(e1, e2), pkh(e2, e2), pkh(e3, e4)};
    sts128(cb + 32 + lane * 16, h ? o1 : o0);
    const float fa = f1(512), fb = f1(513);
    const unsigned aa = pkh(fa, fa), bb = pkh(fb, fb);
    if (lane < 4) sts128(cb + 32 + (128 + lane) * 16, lane == 0 ? (v4u){aa, aa, pkh(fa, fb), bb} : lane == 1 ? (v4u){bb, bb, 0u, 0u} : (v4u){0u, 0u, 0u, 0u});
  } else {
    const float a = f1(384 + 2 * lane), b = f1(385 + 2 * lane), c = f1(386 + 2 * lane);
    sts128(cb + 32 + (64 + lane) * 16, (v4u){pkh(a, a), pkh(a, b), pkh(b, b), pkh(c, c)});
    const float lo = f1(640 - 2 * lane), hi = f1(lane ? 641 - 2 * lane : 640);
    sts32(cb + CH_R3 + lane * 4, pkh(lane ? hi : 0.f, lo));
    if (lane < 16) sts32(cb + CH_R3 + 256 + lane * 4, 0u);
    if (lane < 32) sts32(cb + (lane >> 3) * UCP + (lane & 7) * 4, 0u);
  }
}
DEV void kg_copies(unsigned cb, int g, int lane) {
  auto one = [&](int chunk) {
    const unsigned o = cb + 32 + chunk * 16;
    const v2u pv = lds64(o - 8);
    const v4u e = lds128(o);
    const unsigned a0 = __builtin_amdgcn_alignbit(pv.y, pv.x, 16), a1 = __builtin_amdgcn_alignbit(e.x, pv.y, 16), a2 = __builtin_amdgcn_alignbit(e.y, e.x, 16),
                   a3 = __builtin_amdgcn_alignbit(e.z, e.y, 16), a4 = __builtin_amdgcn_alignbit(e.w, e.z, 16);
    sts128(o + UCP, (v4u){a1, a2, a3, a4});
    sts128(o + 2 * UCP, (v4u){pv.y, e.x, e.y, e.z});
    sts128(o + 3 * UCP, (v4u){a0, a1, a2, a3});
  };
  one(64 * g + lane);
  if (lane < 2) one(128 + 2 * g + lane);
}
DEV void conv_load_u(rsrc_t ws, unsigned grp, int d0, int w, int lane) {
  v4u v[2];
#pragma unroll
  for (int i = 0; i < 2; ++i)
    v[i] = __builtin_amdgcn_raw_buffer_load_b128(ws, (lane >> 1) * 10240 + (lane & 1) * 16, WS_XNT + (64 * w + 32 * i) * 10240 + d0 * 16, SC1);
  if (lane < 16) sts128(grp + (w >> 1) * CH_BYTES + CH_U + (w & 1) * 256 + lane * 16, (v4u){0u, 0u, 0u, 0u});
#pragma unroll
  for (int i = 0; i < 2; ++i) sts128(grp + (lane & 1) * CH_BYTES + CH_U + 512 + (64 * w + 32 * i + (lane >> 1)) * 16, v[i]);
}
template <class F1>
DEV float ssm_prepare(rsrc_t ws, const KgIn& k, unsigned grp, unsigned fp, unsigned fp_free, unsigned cnt, int d0, int w, int lane, F1 f1) {
  const int ci = w >> 1, g = w & 1;
  const float isc = kg_wave(k, g, grp + w * KSCR, fp + w * FPOS, fp_free, lane);
  group_sync(cnt, 4, lane);
  kg_table0(grp + ci * CH_BYTES, fp + 2 * ci * FPOS, g, lane);
  group_sync(cnt + 4 + ci * 4, 2, lane);
  kg_copies(grp + ci * CH_BYTES, g, lane);
  f1();
  conv_load_u(ws, grp, d0, w, lane);
  group_sync(cnt + 12, 4, lane);
  return isc;
}

template <int B> DEV void conv_tail(f4v (&acc)[8], unsigned ua, unsigned r3, v4u w7, v4u w9) {
  const f4v b7 = mfma16h(lds128(ua + 512 * B), w7, (f4v){}), b9 = mfma16h(lds128(ua + 512 * B + 16), w9, (f4v){});
  const v4u bx = {pkh(b7[0], b9[0]), pkh(b7[1], b9[1]), pkh(b7[2], b9[2]), pkh(b7[3], b9[3])};
#pragma unroll
  for (int dl = 4; dl + B < 8; ++dl) {
    const unsigned a = r3 - 64 * dl;
    acc[dl + B] = mfma16h(bx, (v4u){lds32(a), lds32(a + 4), lds32(a + 8), lds32(a + 12)}, acc[dl + B]);
  }
}
DEV void conv_channel(rsrc_t ws, unsigned cb, unsigned cnt, int d, int eh, float isc, float dsk, int lane) {
  const int g = lane >> 4, c16 = lane & 15;
  const unsigned ga = cb + (c16 >> 2) * UCP + 64 - 16 * g + 8 * (lane & 3) + eh * 256;
  const unsigned ba = cb + CH_U + 480 + 32 * c16 + 16 * g - eh * 256;
  f4v acc[8] = {};
  v4u G[2][4], B[2][8];
#pragma unroll
  for (int i = 0; i < 8; ++i) { B[0][i] = lds128(ba + 512 * i); if (i < 4) G[0][i] = ldstr2(ga + 512 * i, ga + 512 * i - 8); }
  {
    unsigned w7[4], w9[4];
#pragma unroll
    for (int i = 0; i < 4; ++i) {
      const int k0 = 8 * g + 2 * i - c16;
      w7[i] = ((k0 >= 5 && k0 <= 11) ? 0x3c00u : 0u) | ((k0 >= 4 && k0 <= 10) ? 0x3c000000u : 0u);
      w9[i] = ((k0 >= 4 && k0 <= 12) ? 0x3c00u : 0u) | ((k0 >= 3 && k0 <= 11) ? 0x3c000000u : 0u);
    }
    const v4u W7 = {w7[0], w7[1], w7[2], w7[3]}, W9 = {w9[0], w9[1], w9[2], w9[3]};
    const unsigned ua = cb + CH_U + 448 + 32 * c16 + 16 * g, r3 = cb + CH_R3 + (127 - c16 + 4 * g) * 4;
    if (eh) {
      conv_tail<0>(acc, ua, r3, W7, W9);
      conv_tail<3>(acc, ua, r3, W7, W9);
    } else {
      conv_tail<1>(acc, ua, r3, W7, W9);
      conv_tail<2>(acc, ua, r3, W7, W9);
      const v4u g4 = ldstr2(ga + 2048, ga + 2048 - 8);
#pragma unroll
      for (int j = 4; j < 8; ++j) acc[j] = mfma16h(g4, B[0][j - 4], acc[j]);
    }
  }
  SCHED();
#pragma unroll
  for (int e = 0; e < 4; ++e) {
    int m = 0;
#pragma unroll
    for (int cc = 0; cc < 4; ++cc)
#pragma unroll
      for (int j = cc; j < 8; ++j) {
        acc[j] = mfma16h(G[e & 1][cc], B[e & 1][j - cc], acc[j]);
        if (e < 3 && (m & 1) == 0 && m < 24) {
          const int i = m >> 1;
          if (i == 1 || i > 8) { const int c = i == 1 ? 0 : i - 8; G[(e + 1) & 1][c] = ldstr2(ga + 512 * c + 64 * (e + 1), ga + 512 * c + 64 * (e + 1) - 8); }
          else { const int c = i == 0 ? 0 : i - 1; B[(e + 1) & 1][c] = lds128(ba + 512 * c - 64 * (e + 1)); }
        }
        ++m;
        SCHED();
      }
  }
  const unsigned to = 2 * (1024 * eh + 16 * c16 + 4 * g);
  v2u uu[4];
#pragma unroll
  for (int j = 0; j < 4; ++j) uu[j] = lds64(cb + CH_U + 512 + to + 512 * j);
  group_sync<1>(cnt, 2, lane);
  f4v mine[4];
  if (eh) {
#pragma unroll
    for (int j = 0; j < 4; ++j) { sts128(cb + 4096 + j * 1024 + lane * 16, __builtin_bit_cast(v4u, acc[j])); mine[j] = acc[4 + j]; }
  } else {
#pragma unroll
    for (int j = 0; j < 4; ++j) { sts128(cb + j * 1024 + lane * 16, __builtin_bit_cast(v4u, acc[4 + j])); mine[j] = acc[j]; }
  }
  group_sync<1>(cnt + 8, 2, lane);
#pragma unroll
  for (int j = 0; j < 4; ++j) {
    const f4v o = __builtin_bit_cast(f4v, lds128(cb + (eh ^ 1) * 4096 + j * 1024 + lane * 16));
    const h2v u0 = u2h(uu[j].x), u1 = u2h(uu[j].y);
    const float s0 = opq(fmaf(mine[j][0] + o[0], isc, dsk * (float)u0.x)), s1 = opq(fmaf(mine[j][1] + o[1], isc, dsk * (float)u0.y));
    const float s2 = opq(fmaf(mine[j][2] + o[2], isc, dsk * (float)u1.x)), s3 = opq(fmaf(mine[j][3] + o[3], isc, dsk * (float)u1.y));
    __builtin_amdgcn_raw_buffer_store_b64((v2u){pkbf(s0, s1), pkbf(s2, s3)}, ws, to + 512 * j, WS_ST + d * 4096, SC1);
  }
}

template <int PACE> DEV void w2_chunk(v4u wss, int ct, int lw, int c, int lane) {
  const unsigned po = (lw & 1) * 7168;
  const unsigned so = WS_W2T + (ct * 54 + 27 * (lw >> 1) + 3 * c) * 5120 + po, lb = (c % 5) * RING2 + (lw >> 1) * 15360 + po;
  dma4_lin_sc1(wss, lane * 16, so, lb);
  if (PACE) __builtin_amdgcn_s_sleep(PACE);
  dma4_lin_sc1(wss, lane * 16, so + 4096, lb + 4096);
  if (PACE) __builtin_amdgcn_s_sleep(PACE);
}
DEV void retile_load(const Params& p, int blk, unsigned stg, int lane) {
  const unsigned v0 = (lane >> 3) * 1280 + 16 * ((lane & 7) ^ (((lane >> 4) & 3) << 1));
  const unsigned v[4] = {v0, v0 + 9216, v0 + 2 * 9216, v0 + 3 * 9216};
  dma4(make_srd(p.w2, W_BYTES), v, (blk / 10) * 40960 + (blk % 10) * 128, stg);
}
struct Frag4 { v4u f[4]; };
DEV Frag4 retile_read(unsigned stg, int lane) {
  const int g = lane >> 4, pp = lane & 3, row = 16 * (g >> 1) + 4 * (g & 1) + ((lane & 15) >> 2);
  Frag4 r;
#pragma unroll
  for (int j = 0; j < 4; ++j) {
    const unsigned a = stg + row * 128 + 16 * ((2 * j + (pp >> 1)) ^ (((row >> 1) & 3) << 1)) + 8 * (pp & 1);
    r.f[j] = ldstr2(a, a + 1024);
  }
  asm volatile("s_waitcnt lgkmcnt(0)" ::: "memory");
  return r;
}
DEV void retile_write(rsrc_t ws, int blk, const Frag4& r, int lane) {
#pragma unroll
  for (int j = 0; j < 4; ++j) {
    const int ng = 4 * (blk % 10) + j;
    __builtin_amdgcn_raw_buffer_store_b128(r.f[j], ws, lane * 16 + WS_W2T + (((ng / 5) * 54 + blk / 10) * 5 + ng % 5) * 1024, 0, SC1);
  }
}

__global__ __launch_bounds__(768, 3) void fused_kernel(Params p) {
  extern __shared__ __attribute__((aligned(16))) unsigned char lds_base[];
  unsigned long long pc;
  unsigned code_bytes;
  asm volatile("s_getpc_b64 %0\n.Lfused_begin:\n\ts_mov_b32 %1, .Lfused_end_a-.Lfused_begin\n\ts_max_i32 %1, %1, .Lfused_end_b-.Lfused_begin"
               : "=s"(pc), "=s"(code_bytes) : : "scc");
  const int lane = threadIdx.x & 63, wave = __builtin_amdgcn_readfirstlane(threadIdx.x >> 6), role = wave >> 2, w = wave & 3;
  const int xcd = blockIdx.x & 7, slot = blockIdx.x >> 3, tb = 32 * xcd + slot;
  const bool gemm_wg = slot < 28;
  const int rp = 4 * (xcd >> 1) + slot / 7, cp = 7 * (xcd & 1) + slot % 7, t0 = 128 * rp, f0 = 128 * cp;
  const unsigned long long t_entry = __builtin_amdgcn_s_memrealtime();
#define HOLD(P) do { if (HOLD_AT == P) { for (int i_ = 0; i_ < 100000 && __builtin_amdgcn_s_memrealtime() - t_entry < HOLD_US * 100ull; ++i_) __builtin_amdgcn_s_sleep(1); } } while (0)
  STAMP_INIT();
  STAMP(0);
  asm volatile("" ::"v"((LAS unsigned char*)lds_base));

  const unsigned vx0 = (lane >> 3) * 1280 + 16 * ((lane & 7) ^ (lane >> 4));
  const unsigned vw0 = (lane >> 4) * 3456 + 16 * ((lane & 15) ^ ((lane >> 4) << 2));
  const unsigned vx[4] = {vx0, (vx0 ^ 64) + 9216, vx0 + 2 * 9216, (vx0 ^ 64) + 3 * 9216};
  const unsigned vw[4] = {vw0, vw0 + 12800, vw0 + 2 * 12800, vw0 + 3 * 12800};
  auto stage_x = [&](int i) { dma4(make_srd(p.x, X_BYTES), vx, (LT0 + 32 * w) * 1280 + 128 * ((i + ROTV) % 10), ((2 * i) % 7) * UNIT + w * 4096); };
  auto stage_w = [&](int i) { dma4(make_srd(p.w1, W_BYTES), vw, (64 * ((i + ROTV) % 10) + 16 * w) * 3456 + 2 * LF0, ((2 * i + 1) % 7) * UNIT + w * 4096); };
  const rsrc_t ws = make_rsrc(p.ws, WS_BYTES), xr = make_rsrc(p.x, X_BYTES);
  unsigned ev = 0;
  LnIn ln = {};
  if (role == 1) {
    PRIO(3);
    if (gemm_wg) { stage_x(0); stage_w(0); }
    ev = epoch_load(ws);
    ln = ln_load(xr, 8 * tb + 2 * w + (lane >> 5), lane);
    if (w < 2) retile_load(p, 2 * tb + w, gemm_wg ? RSTG + w * 4096 : 98304 + w * 8192, lane);
  }
  if (gemm_wg && role == 0) { stage_x(2); stage_w(2); }
  if (!gemm_wg) {
    const int tw = (slot - 28) * 12 + wave;
    const unsigned tx = (512 * (xcd >> 1) + (tw % 8) * 64 + lane) * 1280 + (3 + tw / 8) * 128;
    touch(make_srd(p.x, X_BYTES), tx);
    if (tw < 8) touch(make_srd(p.x, X_BYTES), tx + 6 * 128);
#pragma unroll
    for (int j = 0; j < 2; ++j) {
      const int id = (tw * 2 + j) * 64 + lane;
      touch(make_srd(p.w1, W_BYTES), id < 6272 ? (192 + id / 14) * 3456 + (xcd & 1) * 1792 + (id % 14) * 128 : 0u);
    }
    if (tw == 46) touch(make_srd(p.w1, W_BYTES), (192 + (6144 + lane) / 14) * 3456 + (xcd & 1) * 1792 + ((6144 + lane) % 14) * 128);
    if (tw == 47) touch(make_srd(p.w1, W_BYTES), (192 + (6208 + lane) / 14) * 3456 + (xcd & 1) * 1792 + ((6208 + lane) % 14) * 128);
    if (tw == 8) touch(make_srd(p.b1, F_ * 2), lane * 128);
    if (tw == 9) touch(make_srd(p.b2, D_ * 2), lane * 128);
    if (tw == 10) touch(make_srd(p.ws, WS_BYTES), lane * 262144);
    if (tw == 11) touch(make_srd(p.out, X_BYTES), lane * 65536);
  }
  if (wave < 2) dma1(make_srd((const void*)pc, code_bytes & ~15u), lane * 16, (2 * slot + wave) * 1024, JUNK);

  const v4u wss = make_srd(p.ws, WS_BYTES);
  const int dg = 80 * xcd + (gemm_wg ? 2 * slot : 56 + 6 * (slot - 28) + 2 * role);
  const int tt = 4 * xcd + (slot >> 3), ct = slot & 7;
  const unsigned fslot = (tb >> 2) * 128 + (tb & 3) * 4;
  if (threadIdx.x < N_ZERO) sts32(CNT + threadIdx.x * 4, 0u);

  if (role == 2) {
    const KgIn kin = kg_load(p, dg + (w >> 1), lane);
    BAR();
    const float isc = ssm_prepare(ws, kin, GRP, LNST, 2, C_GRP + 96, dg, w, lane, [&] { STAMP(2); await(E_F1OK, 1); STAMP(3); });
    STAMP(6);
    await(E_GELU, 8);
    STAMP(7);
    conv_channel(ws, GRP + (w >> 1) * CH_BYTES, C_GRP + 96 + 16 + (w >> 1) * 4, dg + (w >> 1), w & 1, isc, kin.dsk, lane);
    STAMP(8);
    signal(E_CONV, lane);
    const unsigned tag = lds_tag();
    publish(ws, C_GRP + 96 + 36, 4, WS_F3 + fslot, tag, lane);
    STAMP(9);
    if (w < 2) {
      await<8>(E_DONE + 14 * 4, 4);
      poll_words4(ws, WS_F3 + (ct * 8 + (lane & 7)) * 128, tag);
      v4u sv[5];
#pragma unroll
      for (int i = 0; i < 5; ++i)
        sv[i] = __builtin_amdgcn_raw_buffer_load_b128(ws, (lane >> 3) * 4096 + (lane & 7) * 16, WS_ST + (80 * ct + 40 * w + 8 * i) * 4096 + tt * 128, SC1);
#pragma unroll
      for (int i = 0; i < 5; ++i) sts128(STILE + (40 * w + 8 * i + (lane >> 3)) * 160 + (lane & 7) * 16, sv[i]);
      signal(E_SRDY, lane);
    }
    STAMP(10);
    STAMP_FLUSH();
    asm volatile(".Lfused_end_a:");
    return;
  }

  auto poll_f1 = [&](unsigned tag) {
    poll_words4(ws, WS_F1 + lane * 128, tag);
    signal(E_F1OK, lane);
    if (blockIdx.x == 0 && lane == 0) __builtin_amdgcn_raw_buffer_store_b32(lds32(W_TAG + 4) + 1u, ws, WS_EPOCH, 0, SC1);
  };
  if (role == 1) {
    const int h = lane >> 5, fi = 4 * xcd + slot - 28;
    const bool extra = !gemm_wg && w == 0 && fi < 28;
    const unsigned stg = gemm_wg ? RSTG + w * 4096 : 98304 + w * 8192;
    Frag4 rt = {};
    BAR();
    if (gemm_wg) {
      stage_x(1); stage_w(1);
      WAITV(8); signal(E_FULL, lane);
      STAMP(1);
      if (w < 2) rt = retile_read(stg, lane);
      WAITV(0); signal(E_FULL + 4, lane);
    }
    STAMP(2);
    PRIO(LPRIO);
    if (extra) retile_load(p, 512 + fi, stg + 4096, lane);
    if (!gemm_wg && w < 2) { WAITV(0); rt = retile_read(stg, lane); }
    const unsigned tag = epoch_tag(ev, w == 0, lane);
    ln_compute(ln, ws, 8 * tb + 2 * w + h, 2 * w + h, tag, lane);
    signal(E_LN, lane);
    if (w >= 2) {
      await(E_LN, 4);
      ln_publish(ws, tb, w - 2, lane);
      signal(E_LNPUB, lane);
      STAMP(3);
      publish(ws, C_F1, 2, WS_F1 + fslot, tag, lane);
      STAMP(4);
      PRIO(0);
      if (gemm_wg && w == 2) { await<4>(E_VMQ, 4); poll_f1(tag); }
    } else {
      PRIO(0);
      if (gemm_wg) await<4>(E_VMQ, 4);
      retile_write(ws, 2 * tb + w, rt, lane);
      if (extra) retile_write(ws, 512 + fi, retile_read(stg + 4096, lane), lane);
      STAMP(3);
      publish(ws, C_FW, 2, WS_FW + fslot, tag, lane);
      STAMP(4);
    }
  }

  if (!gemm_wg) {
    const unsigned grp = role * (4 * KSCR + 4 * FPOS), cnt = C_GRP + role * 48;
    const KgIn kin = kg_load(p, dg + (w >> 1), lane);
    if (role == 0) BAR();
    const float isc = ssm_prepare(ws, kin, grp, grp + 4 * KSCR, 0, cnt, dg, w, lane, [&] {
      if (wave == 0) { await(E_LN, 4); poll_f1(lds_tag()); } else { await<4>(E_F1OK, 1); }
    });
    STAMP(16);
    conv_channel(ws, grp + (w >> 1) * CH_BYTES, cnt + 16 + (w >> 1) * 4, dg + (w >> 1), w & 1, isc, kin.dsk, lane);
    WAITV(0);
    group_sync(C_H, 8, lane);
    if (role == 0) { signal(E_GELU, lane); signal(E_GELU, lane); }
    STAMP(5);
  } else if (role == 0) {
    const int wf = w & 1, wt = w >> 1, h = lane >> 5;
    BAR();
    PRIO(2);
    unsigned aa[2][2], ba[2][4];
    {
      const int q = (lane & 15) >> 2, pp = lane & 3, fb = (lane >> 4) & 1;
#pragma unroll
      for (int mi = 0; mi < 2; ++mi) {
        aa[0][mi] = (8 * h + q) * 256 + 16 * ((8 * wf + 4 * (mi ^ wt) + 2 * fb + (pp >> 1)) ^ (q << 2)) + 8 * (pp & 1);
        aa[1][mi] = aa[0][mi] + 4 * UNIT;
        asm volatile("" : "+v"(aa[1][mi]));
      }
#pragma unroll
      for (int s = 0; s < 4; ++s) {
        ba[0][s] = (64 * wt + (lane & 31)) * 128 + 16 * ((2 * s + h) ^ ((lane >> 1) & 7));
        ba[1][s] = ba[0][s] + 4 * UNIT;
        asm volatile("" : "+v"(ba[1][s]));
      }
    }
    f16v acc[2][2] = {};
    v4u A[3][2], B[3][2], st[2];
    unsigned b1v[2], fl = 0, dn = 0, cbase = CNT, sbase = sig_base(lane), one = 1u, vxo = vx0 ^ 64;
    asm volatile("" : "+v"(cbase), "+v"(sbase), "+v"(one), "+v"(vxo));
    f4v cs4 = {};
    const unsigned sel1 = (((lane >> 2) ^ (lane >> 4)) & 1) ? 0u : 0x3f803f80u;
    const v4u sel = {sel1, sel1, sel1, sel1};
    auto rdA = [&](int n, int mi) {
      const int u = (2 * (n >> 2) + 1) % 7;
      const unsigned a = aa[u >> 2][mi] + (u & 3) * UNIT + (n & 3) * 4096;
      A[n % 3][mi] = ldstr2(a, a + 1024);
    };
    auto rdB = [&](int n, int ni) {
      const int u = (2 * (n >> 2)) % 7;
      B[n % 3][ni] = lds128(ba[u >> 2][n & 3] + (u & 3) * UNIT + ni * 4096);
    };
    const v4u xs = make_srd(p.x, X_BYTES), w1s = make_srd(p.w1, W_BYTES);
    unsigned sx[4], sw[4], mb = w * 4096;
#pragma unroll
    for (int j = 0; j < 4; ++j) {
      sx[j] = (LT0 + 32 * w) * 1280 + ((3 + ROTV) % 10) * 128 + j * 9216;
      sw[j] = (((3 + ROTV) % 10) * 64 + 16 * w) * 3456 + 2 * LF0 + j * 12800;
      asm volatile("" : "+s"(sx[j]), "+s"(sw[j]));
    }
    auto piece = [&](auto qc) {
      constexpr int q = qc, i = (q + 8) / 8 + 2 + ((q >> 2) & 1), j = q & 3;
      if constexpr (q >= -4 && q < 52) {
        if constexpr (q & 4) {
          if constexpr (j == 0) dma1(xs, vx0, sx[0], mb + ((2 * i) % 7) * UNIT);
          else dma_next<j * 1024>(xs, (j & 1) ? vxo : vx0, sx[j]);
          sx[j] += (i + ROTV) % 10 == 9 ? -9 * 128 : 128;
          asm volatile("" : "+s"(sx[j]));
        } else {
          if constexpr (j == 0) dma1(w1s, vw0, sw[0], mb + ((2 * i + 1) % 7) * UNIT);
          else dma_next<j * 1024>(w1s, vw0, sw[j]);
          sw[j] += (i + ROTV) % 10 == 9 ? -9 * 64 * 3456 : 64 * 3456;
          asm volatile("" : "+s"(sw[j]));
        }
      }
    };
    auto load_stats = [&] {
#pragma unroll
      for (int i = 0; i < 2; ++i) st[i] = __builtin_amdgcn_raw_buffer_load_b128(ws, (lane & 31) * 16, WS_STAT + (t0 + 64 * wt + 32 * i) * 16, SC1);
    };
    await(E_FULL, 4);
    HOLD(1);
    rdA(0, 0); rdB(0, 0); rdB(0, 1); rdA(0, 1); rdA(1, 0); rdB(1, 0); rdB(1, 1); rdA(1, 1);
    SCHED();
    unsigned tag = 0, tf = 0, td = 0, tv = 0;
    v4u eb[2];
    static_for<40>([&](auto nc) {
      constexpr int n = nc, sg = n >> 2, r = n & 3;
      acc[0][0] = mfma32b(A[n % 3][0], B[n % 3][0], acc[0][0]);
      SCHED();
      if (n < 38) { rdA(n + 2, 0); rdB(n + 2, 0); }
      SCHED();
      acc[0][1] = mfma32b(A[n % 3][0], B[n % 3][1], acc[0][1]);
      SCHED();
      if constexpr (n == 2) { piece(IC<-4>{}); piece(IC<-3>{}); } else if constexpr (n > 2) piece(IC<2 * (n - 3)>{});
      if (r == 1 && sg < 9) fl = *(volatile LAS unsigned*)lp(cbase + (E_FULL - CNT) + (sg + 1) * 4);
      if (r == 2 && sg < 7) dn = *(volatile LAS unsigned*)lp(cbase + (E_DONE - CNT) + sg * 4);
      if (n == 34) { dn = *(volatile LAS unsigned*)lp(cbase + (E_LN - CNT)); tag = *(volatile LAS unsigned*)lp(cbase + (W_TAG - CNT)); }
      SCHED();
      cs4 = mfma16b(sel, A[n % 3][0], cs4);
      SCHED();
      acc[1][0] = mfma32b(A[n % 3][1], B[n % 3][0], acc[1][0]);
      SCHED();
      if (n < 38) { rdB(n + 2, 1); rdA(n + 2, 1); }
      if (r == 1) signal_at<E_DONE - CNT + sg * 4>(sbase, one);
      if (n == 39) { sts32(SEXC + w * 128 + (lane & 31) * 4, f2u(cs4[0])); signal(E_SX + w * 4, lane); }
      SCHED();
      acc[1][1] = mfma32b(A[n % 3][1], B[n % 3][1], acc[1][1]);
      SCHED();
      if constexpr (n == 2) { piece(IC<-2>{}); piece(IC<-1>{}); } else if constexpr (n > 2) piece(IC<2 * (n - 3) + 1>{});
      if (r == 1 && sg < 9) { CHK(fl, E_FULL + (sg + 1) * 4, 4, tf); if (sg & 1) STAMPL(16 + (sg >> 1)); }
      if (r == 2 && sg < 7) CHK(dn, E_DONE + sg * 4, 4, td);
      if (r == 0 && sg >= 1 && sg < 9) {
        if (sg < 8) TACC(tv, WAITV(8)); else TACC(tv, WAITV(4));
        signal_at<E_FULL - CNT + (sg + 1) * 4>(sbase, one);
      }
      if (n == 29) load_stats();
      if (n == 30) {
#pragma unroll
        for (int i = 0; i < 2; ++i)
          b1v[i] = __builtin_amdgcn_raw_buffer_load_b16(make_rsrc(p.b1, F_ * 2), (lane & 31) * 2, (f0 + 64 * wf + 32 * (i ^ wt)) * 2, 0);
        signal_at<E_VMQ - CNT>(sbase, one);
      }
      if (n == 35) {
        if (__builtin_expect(__builtin_amdgcn_readfirstlane(dn) < 4, 0)) { await(E_LN, 4); tag = peek(W_TAG); }
        tag = __builtin_amdgcn_readfirstlane(tag);
      }
      if (n == 37) {
        if (__builtin_expect(!__all(st[0].y == tag && st[0].w == tag && st[1].y == tag && st[1].w == tag), 0)) {
#pragma unroll 1
          for (unsigned s = 0; s < SPIN_CAP; ++s) {
            asm volatile("" ::: "memory");
            load_stats();
            if (__all(st[0].y == tag && st[0].w == tag && st[1].y == tag && st[1].w == tag)) break;
          }
        }
      }
      if (n == 37 || n == 38) {
        constexpr int i = n == 38;
        const float mu = -u2f(st[i].x), ir = u2f(st[i].z), mh = bfr(mu), ih = bfr(ir);
        eb[i] = h ? (v4u){0u, 0u, 0u, 0u} : (v4u){pkbf(mh, opq(mu - mh)), pkbf(mh, 0.f), pkbf(ih, opq(ir - ih)), 0u};
        asm volatile("" : "+v"(eb[i]));
      }
      SCHED();
    });
    HOLD(2);
    auto ext = [&](int mi, float S) {
      const float sh = bfr(S);
      const v4u ea = h ? (v4u){0u, 0u, 0u, 0u} : (v4u){pkbf(sh, sh), pkbf(S - sh, 0.f), b1v[mi] | (b1v[mi] << 16), 0u};
      acc[mi][0] = mfma32b(ea, eb[0], acc[mi][0]);
      acc[mi][1] = mfma32b(ea, eb[1], acc[mi][1]);
    };
    fl = peek(E_SX + (w ^ 2) * 4);
    float so = u2f(peek(SEXC + (w ^ 2) * 128 + (lane & 31) * 4));
    ext(0, cs4[0]);
    SCHED();
    if (__builtin_expect(__builtin_amdgcn_readfirstlane(fl) < 1, 0)) {
      await(E_SX + (w ^ 2) * 4, 1);
      so = u2f(peek(SEXC + (w ^ 2) * 128 + (lane & 31) * 4));
    }
    ext(1, so);
    STAMP(1);
    if (f0 + 64 * wf < F_) {
      const unsigned hv = ((lane >> 4) & 1) * 55296 + h * 256 + (lane & 15) * 16;
#pragma unroll
      for (int mi = 0; mi < 2; ++mi) {
#pragma unroll
        for (int ni = 0; ni < 2; ++ni) {
          const float ir = u2f(st[ni].z), rho = __builtin_amdgcn_rcpf(ir);
          const float k1 = -2.302208198f * rho, k2 = k1 * 0.044715f * rho * rho;
          const unsigned hs = WS_H + ((t0 + 64 * wt + 32 * ni) / 16) * 55296 + ((f0 + 64 * wf) / 32 + (mi ^ wt)) * 1024;
#pragma unroll
          for (int hf = 0; hf < 2; ++hf) {
            unsigned o[4];
#pragma unroll
            for (int r = 0; r < 4; ++r) {
              const f2v a = {acc[mi][ni][8 * hf + 2 * r], acc[mi][ni][8 * hf + 2 * r + 1]};
              const f2v x = a * (a * a * k2 + k1);
              const f2v e = {__builtin_amdgcn_exp2f(x.x), __builtin_amdgcn_exp2f(x.y)};
              const f2v dd = e * ir + ir;
              const f2v rr = {__builtin_amdgcn_rcpf(dd.x), __builtin_amdgcn_rcpf(dd.y)};
              const f2v gl = a * rr;
              o[r] = pkbf(gl.x, gl.y);
            }
            __builtin_amdgcn_raw_buffer_store_b128((v4u){o[0], o[1], o[2], o[3]}, ws, HADDR, 0, SC1);
            SCHED();
          }
        }
        if (mi == 0) signal(E_GELU, lane);
      }
    } else {
      signal(E_GELU, lane);
    }
    signal(E_GELU, lane);
    STAMP(4); STAMPC(13);
    publish(ws, C_H, 4, WS_F2 + (rp * 16 + cp) * 128, tag, lane);
    STAMP(5);
  }

  if (role == 0) {
    const unsigned tag = lds_tag();
    const int mh = w & 1, kh = w >> 1, g = lane >> 4;
    const int tok = 64 * tt + 32 * mh + 16 * kh + (lane & 15);
    poll_words(ws, WS_F2 + ((tt >> 1) * 16 + min(lane, 13)) * 128, tag);
    HOLD(3);
    PRIO(2);
    constexpr int W = 6;
    v4u H[W][2], A[2][5];
    f4v acc[2][5] = {};
    const unsigned hs = WS_H + (4 * tt + 2 * mh) * 55296 + 27 * kh * 1024;
    const unsigned hv[2] = {lane * 16 + kh * 55296u, lane * 16 + (kh ^ 1) * 55296u};
#pragma unroll
    for (int k = 0; k < W; ++k)
#pragma unroll
      for (int ti = 0; ti < 2; ++ti) H[k][ti] = __builtin_amdgcn_raw_buffer_load_b128(ws, hv[ti], hs + k * 1024, SC1);
    SCHED();
    v2u xv[5], bv[5];
    unsigned ab[3];
#pragma unroll
    for (int i = 0; i < 3; ++i) { ab[i] = kh * 15360 + lane * 16 + 2 * i * RING2; asm volatile("" : "+v"(ab[i])); }
    auto rd = [&](int k, int n) { A[k & 1][n] = lds128(ab[((k / 3) % 5) >> 1] + (((k / 3) % 5) & 1) * RING2 + (k % 3) * 5120 + n * 1024); };
    unsigned fl = 0, cbase = CNT, sbase = sig_base(lane), one = 1u;
    asm volatile("" : "+v"(cbase), "+v"(sbase), "+v"(one));
    await<1>(E_FULL + 10 * 4, 4);
#pragma unroll
    for (int n = 0; n < 5; ++n) rd(0, n);
    STAMP(7); STAMPC(14);
    SCHED();
    static_for<27>([&](auto kc) {
      constexpr int k = kc;
#pragma unroll
      for (int ti = 0; ti < 2; ++ti) {
#pragma unroll
        for (int n = 0; n < 5; ++n) {
          acc[ti][n] = mfma16b(A[k & 1][n], H[k % W][ti], acc[ti][n]);
          SCHED();
          if (ti == 0 && k < 26) {
            rd(k + 1, n);
            if (n == 4 && k % 3 == 1) signal_at<E_DONE - CNT + (10 + k / 3) * 4>(sbase, one);
            SCHED();
          }
          if (ti == 1 && n == 0 && k % 3 == 1 && k < 24) { fl = *(volatile LAS unsigned*)lp(cbase + (E_FULL - CNT) + (11 + k / 3) * 4); SCHED(); }
        }
        if (k + W < 27) H[k % W][ti] = __builtin_amdgcn_raw_buffer_load_b128(ws, hv[ti], hs + (k + W) * 1024, SC1);
        if constexpr (k >= 21 && k < 26) {
          if (ti == 0) xv[k - 21] = __builtin_amdgcn_raw_buffer_load_b64(xr, tok * 1280 + 8 * g + 32 * (k - 21), 160 * ct, 0);
          else bv[k - 21] = __builtin_amdgcn_raw_buffer_load_b64(make_rsrc(p.b2, D_ * 2), 8 * g + 32 * (k - 21), 160 * ct, 0);
        }
        SCHED();
      }
      if (k % 3 == 1 && k < 24) { check(fl, E_FULL + (11 + k / 3) * 4, 4); STAMPL(22 + k / 3); SCHED(); }
    });
    HOLD(4);
    await<1>(E_DONE + 14 * 4, 4);
#pragma unroll
    for (int n = 0; n < 5; ++n) sts128(RED2 + w * 5120 + n * 1024 + lane * 16, __builtin_bit_cast(v4u, acc[1][n]));
    signal(E_RED + w * 4, lane);
    await<1>(E_SRDY, 2);
    await<1>(E_RED + (w ^ 2) * 4, 1);
    STAMP(9);
    const unsigned sa = STILE + (4 * g + ((lane & 15) >> 2)) * 160 + (32 * mh + 16 * kh + 4 * (lane & 3)) * 2;
#pragma unroll
    for (int n = 0; n < 5; ++n) {
      const f4v o = __builtin_bit_cast(f4v, lds128(RED2 + (w ^ 2) * 5120 + n * 1024 + lane * 16));
      const v2u sv = ldstr(sa + n * 2560);
      const float bz[4] = {blo(bv[n].x), bhi(bv[n].x), blo(bv[n].y), bhi(bv[n].y)};
      const float sz[4] = {blo(sv.x), bhi(sv.x), blo(sv.y), bhi(sv.y)};
      const float xz[4] = {blo(xv[n].x), bhi(xv[n].x), blo(xv[n].y), bhi(xv[n].y)};
      float r[4];
#pragma unroll
      for (int i = 0; i < 4; ++i) {
        const float z = acc[0][n][i] + o[i] + bz[i];
        const float gt = z * __builtin_amdgcn_rcpf(1.f + __builtin_amdgcn_exp2f(z * -1.44269504088896f));
        r[i] = fmaf(sz[i], gt, xz[i]);
      }
      *(GAS v2u*)(p.out + tok * 640 + 80 * ct + 16 * n + 4 * g) = (v2u){pkbf(r[0], r[1]), pkbf(r[2], r[3])};
    }
    HOLD(5);
    asm volatile(".Lfused_end_b:");
  } else {
    if (gemm_wg) await<4>(E_GELU, FWPOLL_AT);
    if (w == 0) { poll_words4(ws, WS_FW + lane * 128, lds_tag()); signal(E_FWOK, lane); } else { await<2>(E_FWOK, 1); }
    if (gemm_wg) await<2>(E_GELU, 8);
    STAMP(5);
    PRIO(3);
#pragma unroll 1
    for (int c = 0; c < 3; ++c) w2_chunk<PACE0>(wss, ct, w, c, lane);
    WAITV(16); signal(E_FULL + 10 * 4, lane);
    WAITV(8); signal(E_FULL + 11 * 4, lane);
    WAITV(0); signal(E_FULL + 12 * 4, lane);
    STAMP(6);
    await<4>(E_CONV, 4);
    STAMP(7);
    w2_chunk<0>(wss, ct, w, 3, lane);
    w2_chunk<0>(wss, ct, w, 4, lane);
    WAITV(8);
    signal(E_FULL + 13 * 4, lane);
#pragma unroll 1
    for (int c = 5; c < 9; ++c) {
      await(E_DONE + (5 + c) * 4, 4);
      w2_chunk<0>(wss, ct, w, c, lane);
      WAITV(8);
      signal(E_FULL + (9 + c) * 4, lane);
      STAMP(19 + c);
    }
    WAITV(0);
    signal(E_FULL + 18 * 4, lane);
    STAMP(10);
  }
  STAMP_FLUSH();
}

}

extern "C" void kernel_launch(void* const* d_in, const int* in_sizes, int n_in, void* d_out, int out_size, void* d_ws, size_t ws_size,
                              hipStream_t stream) {
  Params p;
  p.x = (const unsigned short*)d_in[0];
  p.lA = (const unsigned short*)d_in[1];
  p.Ai = (const unsigned short*)d_in[2];
  p.Cr = (const unsigned short*)d_in[3];
  p.Ci = (const unsigned short*)d_in[4];
  p.ldt = (const unsigned short*)d_in[5];
  p.Dsk = (const unsigned short*)d_in[6];
  p.w1 = (const unsigned short*)d_in[8];
  p.b1 = (const unsigned short*)d_in[9];
  p.w2 = (const unsigned short*)d_in[10];
  p.b2 = (const unsigned short*)d_in[11];
  p.out = (unsigned short*)d_out;
  p.ws = (unsigned char*)d_ws;
  (void)hipFuncSetAttribute((const void*)fused_kernel, hipFuncAttributeMaxDynamicSharedMemorySize, LDS_BYTES);
  fused_kernel<<<256, 768, LDS_BYTES, stream>>>(p);
}
```

```cpp
#define HOLD_AT 5
#define HOLD_US 40
#include <hip/hip_runtime.h>
#include <hip/hip_bf16.h>

namespace {

typedef unsigned v4u __attribute__((ext_vector_type(4)));
typedef unsigned v2u __attribute__((ext_vector_type(2)));
typedef short s4v __attribute__((ext_vector_type(4)));
typedef float f2v __attribute__((ext_vector_type(2)));
typedef float f4v __attribute__((ext_vector_type(4)));
typedef float f16v __attribute__((ext_vector_type(16)));
typedef _Float16 h2v __attribute__((ext_vector_type(2)));
typedef _Float16 h8v __attribute__((ext_vector_type(8)));
typedef __bf16 b2v __attribute__((ext_vector_type(2)));
typedef __bf16 b8v __attribute__((ext_vector_type(8)));
#define GAS __attribute__((address_space(1)))
#define LAS __attribute__((address_space(3)))
#define DEV __device__ __forceinline__
typedef __amdgpu_buffer_rsrc_t rsrc_t;

constexpr int L_ = 2048, D_ = 640, F_ = 1728;
constexpr unsigned X_BYTES = L_ * D_ * 2, W_BYTES = D_ * F_ * 2;
constexpr int SC1 = 16;
constexpr unsigned SPIN_CAP = 1u << 16;

constexpr unsigned WS_EPOCH = 0;
constexpr unsigned WS_F1 = 4096;
constexpr unsigned WS_F2 = 65536;
constexpr unsigned WS_FW = 24576;
constexpr unsigned WS_F3 = 16384;
constexpr unsigned WS_STAT = 32768;
constexpr unsigned WS_XNT = 131072;
constexpr unsigned WS_W2T = 2752512;
constexpr unsigned WS_H = 5242880;
constexpr unsigned WS_ST = 12582912;
constexpr unsigned WS_STAMP = 15204352;
constexpr unsigned WS_BYTES = WS_STAMP + 256 * 12 * 64 * 8;

constexpr unsigned LDS_BYTES = 163840;
constexpr unsigned UNIT = 16384;
constexpr unsigned KSCR = 8704;
constexpr unsigned FPOS = 2576;
constexpr unsigned GRP = 7 * UNIT;
constexpr unsigned LNST = GRP + 4 * KSCR;
constexpr unsigned CNT = LNST + 10752;
constexpr unsigned SEXC = CNT + 512;
constexpr unsigned JUNK = CNT + 1024;
constexpr unsigned RSTG = 6 * UNIT;
constexpr unsigned UCP = 2368;
constexpr unsigned CH_R3 = 4 * UCP, CH_U = 9856, CH_BYTES = 2 * KSCR;
constexpr unsigned RING2 = 30720;
constexpr unsigned STILE = 4 * RING2, RED2 = STILE + 12800;
constexpr unsigned E_FULL = CNT, E_DONE = CNT + 80;
constexpr unsigned E_GELU = CNT + 160, E_LN = CNT + 164, E_LNPUB = CNT + 168, E_SRDY = CNT + 172, E_RED = CNT + 176, E_SX = CNT + 192;
constexpr unsigned C_H = CNT + 208, C_F1 = CNT + 212, E_F1OK = CNT + 216, E_CONV = CNT + 220, E_VMQ = CNT + 76, C_FW = CNT + 156, E_FWOK = CNT + 360;
constexpr unsigned C_GRP = CNT + 224;
constexpr unsigned W_TAG = CNT + 368;
constexpr int N_ZERO = 92;

struct Params {
  const unsigned short *x, *lA, *Ai, *Cr, *Ci, *ldt, *Dsk, *w1, *b1, *w2, *b2;
  unsigned short* out;
  unsigned char* ws;
};

#define STAMP(i) do { } while (0)
#define STAMPC(i) do { } while (0)
#define STAMPV(i, v) do { } while (0)
#define STAMP_INIT() do { } while (0)
#define STAMP_FLUSH() do { } while (0)

#define PRIO(x) __builtin_amdgcn_s_setprio(x)
#define PACE0 1
#define APRIO 0
#define LT0 t0
#define LF0 f0
#undef TACC
#define TACC(acc, stmt) do { stmt; } while (0)
#define LPRIO 1
#define FWPOLL_AT 4
#define STAMPL(i) do { } while (0)
#define CHK(e, a, n, t) check(e, a, n)
#define HADDR (hv + hs + hf * 512)
#define ROTV (((slot / 7 * 5 + 1) / 2 + slot % 7 * 3) % 10)
#define BAR() asm volatile("s_waitcnt lgkmcnt(0)\n\ts_barrier" ::: "memory")
#define WAITV(n) asm volatile("s_waitcnt vmcnt(" #n ")" ::: "memory")
#define SCHED() __builtin_amdgcn_sched_barrier(0)

template <int I> struct IC { constexpr operator int() const { return I; } };
template <int N, int I = 0, class F> DEV void static_for(F&& f) {
  if constexpr (I < N) { f(IC<I>{}); static_for<N, I + 1>(f); }
}
DEV float u2f(unsigned u) { return __builtin_bit_cast(float, u); }
DEV unsigned f2u(float f) { return __builtin_bit_cast(unsigned, f); }
DEV h2v u2h(unsigned u) { return __builtin_bit_cast(h2v, u); }
DEV float blo(unsigned u) { return u2f(u << 16); }
DEV float bhi(unsigned u) { return u2f(u & 0xffff0000u); }
DEV unsigned pkbf(float a, float b) { return __builtin_bit_cast(unsigned, __builtin_convertvector((f2v){a, b}, b2v)); }
DEV unsigned pkh(float a, float b) { return __builtin_bit_cast(unsigned, __builtin_convertvector((f2v){a, b}, h2v)); }
DEV float opq(float x) { asm("" : "+v"(x)); return x; }
DEV float bfr(float a) { return blo(pkbf(a, a)); }
DEV f16v mfma32b(v4u a, v4u b, f16v c) { return __builtin_amdgcn_mfma_f32_32x32x16_bf16(__builtin_bit_cast(b8v, a), __builtin_bit_cast(b8v, b), c, 0, 0, 0); }
DEV f16v mfma32h(v4u a, v4u b, f16v c) { return __builtin_amdgcn_mfma_f32_32x32x16_f16(__builtin_bit_cast(h8v, a), __builtin_bit_cast(h8v, b), c, 0, 0, 0); }
DEV f4v mfma16b(v4u a, v4u b, f4v c) { return __builtin_amdgcn_mfma_f32_16x16x32_bf16(__builtin_bit_cast(b8v, a), __builtin_bit_cast(b8v, b), c, 0, 0, 0); }
DEV f4v mfma16h(v4u a, v4u b, f4v c) { return __builtin_amdgcn_mfma_f32_16x16x32_f16(__builtin_bit_cast(h8v, a), __builtin_bit_cast(h8v, b), c, 0, 0, 0); }

DEV LAS unsigned char* lp(unsigned a) { return (LAS unsigned char*)(size_t)a; }
DEV v4u lds128(unsigned a) { return *(LAS v4u*)lp(a); }
DEV v2u lds64(unsigned a) { return *(LAS v2u*)lp(a); }
DEV unsigned lds32(unsigned a) { return *(LAS unsigned*)lp(a); }
DEV void sts128(unsigned a, v4u v) { *(LAS v4u*)lp(a) = v; }
DEV void sts64(unsigned a, v2u v) { *(LAS v2u*)lp(a) = v; }
DEV void sts32(unsigned a, unsigned v) { *(LAS unsigned*)lp(a) = v; }
DEV v2u ldstr(unsigned a) { return __builtin_bit_cast(v2u, __builtin_amdgcn_ds_read_tr16_b64_v4i16((LAS s4v*)lp(a))); }
DEV v4u ldstr2(unsigned a0, unsigned a1) { const v2u x = ldstr(a0), y = ldstr(a1); return (v4u){x.x, x.y, y.x, y.y}; }

DEV v4u make_srd(const void* ptr, unsigned bytes) {
  return (v4u){(unsigned)(size_t)ptr, (unsigned)((size_t)ptr >> 32) & 0xffffu, bytes, 0x00020000u};
}
DEV rsrc_t make_rsrc(const void* ptr, unsigned bytes) { return __builtin_amdgcn_make_buffer_rsrc((void*)ptr, 0, bytes, 0x00020000); }
#define DMA_HEAD "s_mov_b32 m0, %0\n\ts_nop 0\n\t"
DEV void dma1(v4u srd, unsigned v, unsigned soff, unsigned lds) {
  asm volatile(DMA_HEAD "buffer_load_dwordx4 %1, %2, %3 offen lds" ::"s"(lds), "v"(v), "s"(srd), "s"(soff) : "memory");
}
template <int OFF> DEV void dma_next(v4u srd, unsigned v, unsigned soff) {
  asm volatile("buffer_load_dwordx4 %0, %1, %2 offen offset:%3 lds" ::"v"(v), "s"(srd), "s"(soff), "i"(OFF) : "memory");
}
DEV void touch(v4u srd, unsigned v) {
}
DEV void dma4(v4u srd, const unsigned (&v)[4], unsigned soff, unsigned lds) {
  asm volatile(DMA_HEAD "buffer_load_dwordx4 %1, %5, %6 offen lds\n\tbuffer_load_dwordx4 %2, %5, %6 offen offset:1024 lds\n\t"
               "buffer_load_dwordx4 %3, %5, %6 offen offset:2048 lds\n\tbuffer_load_dwordx4 %4, %5, %6 offen offset:3072 lds"
               ::"s"(lds), "v"(v[0]), "v"(v[1]), "v"(v[2]), "v"(v[3]), "s"(srd), "s"(soff) : "memory");
}
DEV void dma4_lin_sc1(v4u srd, unsigned v, unsigned soff, unsigned lds) {
  asm volatile(DMA_HEAD "buffer_load_dwordx4 %1, %2, %3 offen sc1 lds\n\tbuffer_load_dwordx4 %1, %2, %3 offen offset:1024 sc1 lds\n\t"
               "buffer_load_dwordx4 %1, %2, %3 offen offset:2048 sc1 lds\n\tbuffer_load_dwordx4 %1, %2, %3 offen offset:3072 sc1 lds"
               ::"s"(lds), "v"(v), "s"(srd), "s"(soff) : "memory");
}

DEV void poll_words(rsrc_t ws, unsigned off, unsigned tag) {
#pragma unroll 1
  for (unsigned s = 0; s < SPIN_CAP; ++s) {
    asm volatile("" ::: "memory");
    if (__all(__builtin_amdgcn_raw_buffer_load_b32(ws, off, 0, SC1) == tag)) break;
  }
  asm volatile("" ::: "memory");
}
DEV void poll_words4(rsrc_t ws, unsigned off, unsigned tag) {
#pragma unroll 1
  for (unsigned s = 0; s < SPIN_CAP; ++s) {
    asm volatile("" ::: "memory");
    const v4u v = __builtin_amdgcn_raw_buffer_load_b128(ws, off, 0, SC1);
    if (__all(v.x == tag && v.y == tag && v.z == tag && v.w == tag)) break;
    __builtin_amdgcn_s_sleep(8);
  }
  asm volatile("" ::: "memory");
}
DEV unsigned sig_base(int lane) { return lane == 0 ? CNT : JUNK + lane * 4; }
template <int OFF> DEV void signal_at(unsigned base, unsigned one) {
  asm volatile("ds_add_u32 %0, %1 offset:%2" ::"v"(base), "v"(one), "i"(OFF) : "memory");
}
DEV void signal(unsigned a, int lane) {
  asm volatile("ds_add_u32 %0, %1" ::"v"(sig_base(lane) + (a - CNT)), "v"(1u) : "memory");
}
DEV unsigned peek(unsigned a) { return *(volatile LAS unsigned*)lp(a); }
template <int SLEEP = 1> DEV void await(unsigned a, unsigned need) {
#pragma unroll 1
  for (unsigned s = 0; s < (SPIN_CAP << 2); ++s) {
    asm volatile("" : "+s"(s));
    if (__builtin_amdgcn_readfirstlane(peek(a)) >= need) break;
    __builtin_amdgcn_s_sleep(SLEEP);
  }
  asm volatile("" ::: "memory");
}
DEV void check(unsigned early, unsigned a, unsigned need) {
  if (__builtin_expect(__builtin_amdgcn_readfirstlane(early) < need, 0)) await<1>(a, need);
  asm volatile("" ::: "memory");
}
DEV unsigned lds_arrive(unsigned cnt, int lane) {
  unsigned old;
  asm volatile("ds_add_rtn_u32 %0, %1, %2\n\ts_waitcnt lgkmcnt(0)" : "=&v"(old) : "v"(sig_base(lane) + (cnt - CNT)), "v"(1u) : "memory");
  return __builtin_amdgcn_readfirstlane(old);
}
template <int SLEEP = 1> DEV void group_sync(unsigned cnt, unsigned target, int lane) {
  if (lds_arrive(cnt, lane) + 1 < target) await<SLEEP>(cnt, target);
}
DEV void publish(rsrc_t ws, unsigned cnt, unsigned nwaves, unsigned flag_off, unsigned tag, int lane) {
  WAITV(0);
  if (lds_arrive(cnt, lane) + 1 == nwaves && lane == 0) __builtin_amdgcn_raw_buffer_store_b32(tag, ws, flag_off, 0, SC1);
}
DEV unsigned epoch_load(rsrc_t ws) { return __builtin_amdgcn_raw_buffer_load_b32(ws, WS_EPOCH, 0, SC1); }
DEV unsigned epoch_tag(unsigned ev, bool share, int lane) {
  const unsigned e = __builtin_amdgcn_readfirstlane(ev);
  const unsigned tag = e * 0x9E3779B1u + 0x7F4A7C15u;
  if (share && lane == 0) sts64(W_TAG, (v2u){tag, e});
  return tag;
}
DEV unsigned lds_tag() { return __builtin_amdgcn_readfirstlane(lds32(W_TAG)); }

template <int CTRL> DEV float dpp_add(float v) {
  return v + u2f(__builtin_amdgcn_update_dpp(0u, f2u(v), CTRL, 0xf, 0xf, false));
}
template <int CTRL> DEV float dpp_max(float v) {
  return fmaxf(v, u2f(__builtin_amdgcn_update_dpp(0u, f2u(v), CTRL, 0xf, 0xf, false)));
}
DEV float half_sum(float v) {
  v = dpp_add<0x128>(v); v = dpp_add<0x124>(v); v = dpp_add<0x122>(v); v = dpp_add<0x121>(v);
  const auto r = __builtin_amdgcn_permlane16_swap(f2u(v), f2u(v), false, false);
  return u2f(r[0]) + u2f(r[1]);
}
DEV float wave_max(float v) {
  v = dpp_max<0x128>(v); v = dpp_max<0x124>(v); v = dpp_max<0x122>(v); v = dpp_max<0x121>(v);
  const auto r = __builtin_amdgcn_permlane16_swap(f2u(v), f2u(v), false, false);
  v = fmaxf(u2f(r[0]), u2f(r[1]));
  const auto q = __builtin_amdgcn_permlane32_swap(f2u(v), f2u(v), false, false);
  return fmaxf(u2f(q[0]), u2f(q[1]));
}

struct LnIn { v2u v[5]; };
DEV LnIn ln_load(rsrc_t xr, int token, int lane) {
  LnIn r;
#pragma unroll
  for (int m = 0; m < 5; ++m) r.v[m] = __builtin_amdgcn_raw_buffer_load_b64(xr, token * 1280 + (lane & 31) * 8 + m * 256, 0, 0);
  return r;
}
DEV void ln_compute(const LnIn& in, rsrc_t ws, int token, int row, unsigned tag, int lane) {
  float xv[20], s = 0.f, q = 0.f;
#pragma unroll
  for (int m = 0; m < 5; ++m) { xv[4 * m] = blo(in.v[m].x); xv[4 * m + 1] = bhi(in.v[m].x); xv[4 * m + 2] = blo(in.v[m].y); xv[4 * m + 3] = bhi(in.v[m].y); }
#pragma unroll
  for (int m = 0; m < 20; ++m) { s = opq(s + xv[m]); q = opq(fmaf(xv[m], xv[m], q)); }
  s = half_sum(s); q = half_sum(q);
  const float mean = s * (1.f / 640.f);
  const float var = fmaxf(fmaf(-mean, mean, q * (1.f / 640.f)), 0.f);
  const float m1 = bfr(mean), s1 = bfr(__builtin_amdgcn_sqrtf(bfr(bfr(var) + 1e-5f)));
  const float r1 = __builtin_amdgcn_rcpf(s1);
  const float m2 = bfr((mean - m1) * r1), s2 = bfr(__builtin_amdgcn_sqrtf(bfr(bfr(var * r1 * r1) + 1e-5f)));
  if ((lane & 31) == 0)
    __builtin_amdgcn_raw_buffer_store_b128((v4u){f2u(fmaf(m2, s1, m1)), tag, f2u(s1 * s2), tag}, ws, WS_STAT + token * 16, 0, SC1);
  const float c = -m1 * r1;
#pragma unroll
  for (int m = 0; m < 5; ++m) {
    const v2u o = {pkh(opq(fmaf(xv[4 * m], r1, c)), opq(fmaf(xv[4 * m + 1], r1, c))), pkh(opq(fmaf(xv[4 * m + 2], r1, c)), opq(fmaf(xv[4 * m + 3], r1, c)))};
    sts64(LNST + row * 1344 + (lane & 31) * 8 + m * 256, o);
  }
}
DEV void ln_publish(rsrc_t ws, int tb, int aw, int lane) {
  const unsigned a = LNST + ((lane & 15) >> 2) * 1344 + (lane >> 4) * 32 + (lane & 3) * 8 + aw * 640;
#pragma unroll
  for (int i = 0; i < 5; ++i) {
    const v4u v = ldstr2(a + i * 128, a + i * 128 + 4 * 1344);
    __builtin_amdgcn_raw_buffer_store_b128(v, ws, WS_XNT + tb * 10240 + (aw * 320 + i * 64 + lane) * 16, 0, SC1);
  }
}

struct Cx { float r, i; };
DEV Cx cmul(Cx a, Cx b) { return {opq(fmaf(a.r, b.r, -a.i * b.i)), opq(fmaf(a.r, b.i, a.i * b.r))}; }
DEV Cx csqr(Cx a) { return {opq(fmaf(a.r, a.r, -a.i * a.i)), opq((a.r + a.r) * a.i)}; }
DEV unsigned ch(Cx a) { return pkh(a.r, a.i); }
DEV unsigned hmul(unsigned a, unsigned b) {
  unsigned t, r;
  asm("v_pk_mul_f16 %0, %1, %2 op_sel:[0,0] op_sel_hi:[0,1]" : "=v"(t) : "v"(a), "v"(b));
  asm("v_pk_fma_f16 %0, %1, %2, %3 op_sel:[1,1,0] op_sel_hi:[1,0,1] neg_lo:[0,1,0]" : "=v"(r) : "v"(a), "v"(b), "v"(t));
  return r;
}
DEV float exp_bf(float x) {
  const float t = x * 1.44268798828125f, lo = x * 7.0526077138765e-06f;
  const float n = __builtin_rintf(t);
  return bfr(__builtin_amdgcn_ldexpf(__builtin_amdgcn_exp2f((t - n) + lo), (int)fminf(fmaxf(n, -200.f), 200.f)));
}
DEV Cx cexp_at(float lg, float rh, float rl, float P) {
  const float m = __builtin_amdgcn_exp2f(lg * P), ph = fmaf(rl, P, __builtin_amdgcn_fractf(rh * P));
  return {opq(m * __builtin_amdgcn_cosf(ph)), opq(m * __builtin_amdgcn_sinf(ph))};
}
struct KgIn { unsigned lA, Ai, Cr, Ci; float dt, dsk; };
DEV KgIn kg_load(const Params& p, int d, int lane) {
  KgIn k;
  k.lA = ((const GAS unsigned*)p.lA)[d * 64 + lane]; k.Ai = ((const GAS unsigned*)p.Ai)[d * 64 + lane];
  k.Cr = ((const GAS unsigned*)p.Cr)[d * 64 + lane]; k.Ci = ((const GAS unsigned*)p.Ci)[d * 64 + lane];
  k.dt = u2f((unsigned)((const GAS unsigned short*)p.ldt)[d] << 16);
  k.dsk = u2f((unsigned)((const GAS unsigned short*)p.Dsk)[d] << 16);
  return k;
}
DEV float kg_wave(const KgIn& k, int g, unsigned scr, unsigned fp, unsigned fp_free, int lane) {
  const float dt = exp_bf(k.dt), c2 = dt + dt;
  const float mx = wave_max(fmaxf(fmaxf(fabsf(blo(k.Cr)), fabsf(bhi(k.Cr))), fmaxf(fabsf(blo(k.Ci)), fabsf(bhi(k.Ci))))) * c2;
  int ex = __builtin_amdgcn_frexp_expf(mx);
  ex = ex < -120 ? -120 : (ex > 120 ? 120 : ex);
  const float cs = c2 * __builtin_amdgcn_ldexpf(1.f, -ex);
  const int sh = g ? 0 : 16;
  const float lg = -(exp_bf(bhi(k.lA << sh)) * dt) * 1.44269504088896f, ai = bhi(k.Ai << sh) * dt;
  const Cx c0 = {opq(bhi(k.Cr << sh) * cs), opq(-bhi(k.Ci << sh) * cs)};
  const float rh = ai * 0.15915494f, rl = fmaf(ai, 0.15915494f, -rh) + ai * 6.4206382e-09f;
  const Cx v1 = cexp_at(lg, -rh, -rl, 1.f), w1 = cexp_at(lg, rh, rl, 8.f), G = cexp_at(lg, -rh, -rl, 256.f);
  const unsigned wa = scr + lane * 4, ra = scr + (lane & 31) * 272 + (lane >> 5) * 16;
  {
    const Cx w2 = csqr(w1), w3 = cmul(w2, w1), w4 = csqr(w2), w8 = csqr(w4), w12 = cmul(w8, w4), w16 = csqr(w8);
    const Cx w20 = cmul(w16, w4), w24 = csqr(w12), w28 = cmul(w24, w4);
    const unsigned o[3] = {ch(w1), ch(w2), ch(w3)};
    const unsigned bs[7] = {ch(w4), ch(w8), ch(w12), ch(w16), ch(w20), ch(w24), ch(w28)};
    sts32(wa, 0x00003c00u);
#pragma unroll
    for (int j = 0; j < 3; ++j) sts32(wa + (1 + j) * 272, o[j]);
#pragma unroll
    for (int b = 0; b < 7; ++b) {
      sts32(wa + (4 + 4 * b) * 272, bs[b]);
#pragma unroll
      for (int j = 0; j < 3; ++j) sts32(wa + (5 + 4 * b + j) * 272, hmul(bs[b], o[j]));
    }
  }
  asm volatile("" ::: "memory");
  v4u y[8];
#pragma unroll
  for (int s = 0; s < 8; ++s) y[s] = lds128(ra + s * 32);
  asm volatile("" ::: "memory");
  {
    const Cx v2 = csqr(v1), v3 = cmul(v2, v1), v4 = csqr(v2), v6 = csqr(v3), x4 = cmul(c0, v4);
    const unsigned h1 = ch(v1), h2 = ch(v2), h3 = ch(v3), h4 = ch(v4), h6 = ch(v6), hx0 = ch(c0), hx4 = ch(x4);
    sts32(wa + 0 * 272, hx0); sts32(wa + 1 * 272, hmul(hx0, h1)); sts32(wa + 2 * 272, hmul(hx0, h2)); sts32(wa + 3 * 272, hmul(hx0, h3));
    sts32(wa + 4 * 272, hx4); sts32(wa + 5 * 272, hmul(hx4, h1)); sts32(wa + 6 * 272, hmul(hx4, h2)); sts32(wa + 7 * 272, hmul(hx4, h3));
    const Cx d1 = cmul(c0, G), d2 = cmul(d1, G), d3 = cmul(d2, G), d4 = cmul(d3, G);
    const Cx G4 = csqr(csqr(G));
    const Cx d5 = cmul(d1, G4), d6 = cmul(d2, G4), d7 = cmul(d3, G4), d8 = cmul(d4, G4);
    const unsigned e1 = ch(d1), e2 = ch(d2), e3 = ch(d3);
    sts32(wa + 8 * 272, e1); sts32(wa + 9 * 272, hmul(e1, h2)); sts32(wa + 10 * 272, hmul(e1, h4)); sts32(wa + 11 * 272, hmul(e1, h6));
    sts32(wa + 12 * 272, e2); sts32(wa + 13 * 272, hmul(e2, h4)); sts32(wa + 14 * 272, e3); sts32(wa + 15 * 272, hmul(e3, h4));
    sts32(wa + 16 * 272, ch(d4)); sts32(wa + 17 * 272, ch(d5)); sts32(wa + 18 * 272, ch(d6)); sts32(wa + 19 * 272, ch(d7));
    sts32(wa + 20 * 272, ch(d8));
  }
  asm volatile("" ::: "memory");
  v4u xx[8];
#pragma unroll
  for (int s = 0; s < 8; ++s) xx[s] = lds128(ra + s * 32);
  SCHED();
  f16v acc = {};
#pragma unroll
  for (int s = 0; s < 8; ++s) acc = mfma32h(xx[s], y[s], acc);
  const int j = lane & 31, h = lane >> 5;
  await(E_LNPUB, fp_free);
  sts128(fp + (8 * j + 4 * h) * 4, (v4u){f2u(acc[0]), f2u(acc[1]), f2u(acc[2]), f2u(acc[3])});
  sts64(fp + (h ? 384 + 2 * j : 256 + 4 * j) * 4, (v2u){f2u(acc[4]), f2u(acc[5])});
  sts64(fp + (h ? 448 + 2 * j : 258 + 4 * j) * 4, (v2u){f2u(acc[6]), f2u(acc[7])});
  if (lane <= 32) sts32(fp + (512 + 128 * h + j) * 4, f2u(acc[8]));
  if (h == 0) {
#pragma unroll
    for (int i = 1; i < 4; ++i) sts32(fp + (512 + j + 32 * i) * 4, f2u(acc[8 + i]));
  }
  return __builtin_amdgcn_ldexpf(1.f, ex);
}
DEV void kg_table0(unsigned cb, unsigned fp, int g, int lane) {
  auto f1 = [&](int i) { return opq(u2f(lds32(fp + i * 4)) + u2f(lds32(fp + FPOS + i * 4))); };
  if (g == 0) {
    const int j = lane & 31, h = lane >> 5;
    const unsigned a = fp + (h ? 256 + 4 * j : 8 * j) * 4;
    const v4u l0 = lds128(a), h0 = lds128(a + 16), l1 = lds128(a + FPOS), h1 = lds128(a + FPOS + 16);
    const float e0 = opq(u2f(l0.x) + u2f(l1.x)), e1 = opq(u2f(l0.y) + u2f(l1.y)), e2 = opq(u2f(l0.z) + u2f(l1.z)), e3 = opq(u2f(l0.w) + u2f(l1.w));
    const float e4 = opq(u2f(h0.x) + u2f(h1.x)), e5 = opq(u2f(h0.y) + u2f(h1.y)), e6 = opq(u2f(h0.z) + u2f(h1.z)), e7 = opq(u2f(h0.w) + u2f(h1.w));
    const v4u o0 = {pkh(e0, e1), pkh(e2, e3), pkh(e4, e5), pkh(e6, e7)};
    const v4u o1 = {pkh(e0, e0), pkh(e1, e2), pkh(e2, e2), pkh(e3, e4)};
    sts128(cb + 32 + lane * 16, h ? o1 : o0);
    const float fa = f1(512), fb = f1(513);
    const unsigned aa = pkh(fa, fa), bb = pkh(fb, fb);
    if (lane < 4) sts128(cb + 32 + (128 + lane) * 16, lane == 0 ? (v4u){aa, aa, pkh(fa, fb), bb} : lane == 1 ? (v4u){bb, bb, 0u, 0u} : (v4u){0u, 0u, 0u, 0u});
  } else {
    const float a = f1(384 + 2 * lane), b = f1(385 + 2 * lane), c = f1(386 + 2 * lane);
    sts128(cb + 32 + (64 + lane) * 16, (v4u){pkh(a, a), pkh(a, b), pkh(b, b), pkh(c, c)});
    const float lo = f1(640 - 2 * lane), hi = f1(lane ? 641 - 2 * lane : 640);
    sts32(cb + CH_R3 + lane * 4, pkh(lane ? hi : 0.f, lo));
    if (lane < 16) sts32(cb + CH_R3 + 256 + lane * 4, 0u);
    if (lane < 32) sts32(cb + (lane >> 3) * UCP + (lane & 7) * 4, 0u);
  }
}
DEV void kg_copies(unsigned cb, int g, int lane) {
  auto one = [&](int chunk) {
    const unsigned o = cb + 32 + chunk * 16;
    const v2u pv = lds64(o - 8);
    const v4u e = lds128(o);
    const unsigned a0 = __builtin_amdgcn_alignbit(pv.y, pv.x, 16), a1 = __builtin_amdgcn_alignbit(e.x, pv.y, 16), a2 = __builtin_amdgcn_alignbit(e.y, e.x, 16),
                   a3 = __builtin_amdgcn_alignbit(e.z, e.y, 16), a4 = __builtin_amdgcn_alignbit(e.w, e.z, 16);
    sts128(o + UCP, (v4u){a1, a2, a3, a4});
    sts128(o + 2 * UCP, (v4u){pv.y, e.x, e.y, e.z});
    sts128(o + 3 * UCP, (v4u){a0, a1, a2, a3});
  };
  one(64 * g + lane);
  if (lane < 2) one(128 + 2 * g + lane);
}
DEV void conv_load_u(rsrc_t ws, unsigned grp, int d0, int w, int lane) {
  v4u v[2];
#pragma unroll
  for (int i = 0; i < 2; ++i)
    v[i] = __builtin_amdgcn_raw_buffer_load_b128(ws, (lane >> 1) * 10240 + (lane & 1) * 16, WS_XNT + (64 * w + 32 * i) * 10240 + d0 * 16, SC1);
  if (lane < 16) sts128(grp + (w >> 1) * CH_BYTES + CH_U + (w & 1) * 256 + lane * 16, (v4u){0u, 0u, 0u, 0u});
#pragma unroll
  for (int i = 0; i < 2; ++i) sts128(grp + (lane & 1) * CH_BYTES + CH_U + 512 + (64 * w + 32 * i + (lane >> 1)) * 16, v[i]);
}
template <class F1>
DEV float ssm_prepare(rsrc_t ws, const KgIn& k, unsigned grp, unsigned fp, unsigned fp_free, unsigned cnt, int d0, int w, int lane, F1 f1) {
  const int ci = w >> 1, g = w & 1;
  const float isc = kg_wave(k, g, grp + w * KSCR, fp + w * FPOS, fp_free, lane);
  group_sync(cnt, 4, lane);
  kg_table0(grp + ci * CH_BYTES, fp + 2 * ci * FPOS, g, lane);
  group_sync(cnt + 4 + ci * 4, 2, lane);
  kg_copies(grp + ci * CH_BYTES, g, lane);
  f1();
  conv_load_u(ws, grp, d0, w, lane);
  group_sync(cnt + 12, 4, lane);
  return isc;
}

template <int B> DEV void conv_tail(f4v (&acc)[8], unsigned ua, unsigned r3, v4u w7, v4u w9) {
  const f4v b7 = mfma16h(lds128(ua + 512 * B), w7, (f4v){}), b9 = mfma16h(lds128(ua + 512 * B + 16), w9, (f4v){});
  const v4u bx = {pkh(b7[0], b9[0]), pkh(b7[1], b9[1]), pkh(b7[2], b9[2]), pkh(b7[3], b9[3])};
#pragma unroll
  for (int dl = 4; dl + B < 8; ++dl) {
    const unsigned a = r3 - 64 * dl;
    acc[dl + B] = mfma16h(bx, (v4u){lds32(a), lds32(a + 4), lds32(a + 8), lds32(a + 12)}, acc[dl + B]);
  }
}
DEV void conv_channel(rsrc_t ws, unsigned cb, unsigned cnt, int d, int eh, float isc, float dsk, int lane) {
  const int g = lane >> 4, c16 = lane & 15;
  const unsigned ga = cb + (c16 >> 2) * UCP + 64 - 16 * g + 8 * (lane & 3) + eh * 256;
  const unsigned ba = cb + CH_U + 480 + 32 * c16 + 16 * g - eh * 256;
  f4v acc[8] = {};
  v4u G[2][4], B[2][8];
#pragma unroll
  for (int i = 0; i < 8; ++i) { B[0][i] = lds128(ba + 512 * i); if (i < 4) G[0][i] = ldstr2(ga + 512 * i, ga + 512 * i - 8); }
  {
    unsigned w7[4], w9[4];
#pragma unroll
    for (int i = 0; i < 4; ++i) {
      const int k0 = 8 * g + 2 * i - c16;
      w7[i] = ((k0 >= 5 && k0 <= 11) ? 0x3c00u : 0u) | ((k0 >= 4 && k0 <= 10) ? 0x3c000000u : 0u);
      w9[i] = ((k0 >= 4 && k0 <= 12) ? 0x3c00u : 0u) | ((k0 >= 3 && k0 <= 11) ? 0x3c000000u : 0u);
    }
    const v4u W7 = {w7[0], w7[1], w7[2], w7[3]}, W9 = {w9[0], w9[1], w9[2], w9[3]};
    const unsigned ua = cb + CH_U + 448 + 32 * c16 + 16 * g, r3 = cb + CH_R3 + (127 - c16 + 4 * g) * 4;
    if (eh) {
      conv_tail<0>(acc, ua, r3, W7, W9);
      conv_tail<3>(acc, ua, r3, W7, W9);
    } else {
      conv_tail<1>(acc, ua, r3, W7, W9);
      conv_tail<2>(acc, ua, r3, W7, W9);
      const v4u g4 = ldstr2(ga + 2048, ga + 2048 - 8);
#pragma unroll
      for (int j = 4; j < 8; ++j) acc[j] = mfma16h(g4, B[0][j - 4], acc[j]);
    }
  }
  SCHED();
#pragma unroll
  for (int e = 0; e < 4; ++e) {
    int m = 0;
#pragma unroll
    for (int cc = 0; cc < 4; ++cc)
#pragma unroll
      for (int j = cc; j < 8; ++j) {
        acc[j] = mfma16h(G[e & 1][cc], B[e & 1][j - cc], acc[j]);
        if (e < 3 && (m & 1) == 0 && m < 24) {
          const int i = m >> 1;
          if (i == 1 || i > 8) { const int c = i == 1 ? 0 : i - 8; G[(e + 1) & 1][c] = ldstr2(ga + 512 * c + 64 * (e + 1), ga + 512 * c + 64 * (e + 1) - 8); }
          else { const int c = i == 0 ? 0 : i - 1; B[(e + 1) & 1][c] = lds128(ba + 512 * c - 64 * (e + 1)); }
        }
        ++m;
        SCHED();
      }
  }
  const unsigned to = 2 * (1024 * eh + 16 * c16 + 4 * g);
  v2u uu[4];
#pragma unroll
  for (int j = 0; j < 4; ++j) uu[j] = lds64(cb + CH_U + 512 + to + 512 * j);
  group_sync<1>(cnt, 2, lane);
  f4v mine[4];
  if (eh) {
#pragma unroll
    for (int j = 0; j < 4; ++j) { sts128(cb + 4096 + j * 1024 + lane * 16, __builtin_bit_cast(v4u, acc[j])); mine[j] = acc[4 + j]; }
  } else {
#pragma unroll
    for (int j = 0; j < 4; ++j) { sts128(cb + j * 1024 + lane * 16, __builtin_bit_cast(v4u, acc[4 + j])); mine[j] = acc[j]; }
  }
  group_sync<1>(cnt + 8, 2, lane);
#pragma unroll
  for (int j = 0; j < 4; ++j) {
    const f4v o = __builtin_bit_cast(f4v, lds128(cb + (eh ^ 1) * 4096 + j * 1024 + lane * 16));
    const h2v u0 = u2h(uu[j].x), u1 = u2h(uu[j].y);
    const float s0 = opq(fmaf(mine[j][0] + o[0], isc, dsk * (float)u0.x)), s1 = opq(fmaf(mine[j][1] + o[1], isc, dsk * (float)u0.y));
    const float s2 = opq(fmaf(mine[j][2] + o[2], isc, dsk * (float)u1.x)), s3 = opq(fmaf(mine[j][3] + o[3], isc, dsk * (float)u1.y));
    __builtin_amdgcn_raw_buffer_store_b64((v2u){pkbf(s0, s1), pkbf(s2, s3)}, ws, to + 512 * j, WS_ST + d * 4096, SC1);
  }
}

template <int PACE> DEV void w2_chunk(v4u wss, int ct, int lw, int c, int lane) {
  const unsigned po = (lw & 1) * 7168;
  const unsigned so = WS_W2T + (ct * 54 + 27 * (lw >> 1) + 3 * c) * 5120 + po, lb = (c % 5) * RING2 + (lw >> 1) * 15360 + po;
  dma4_lin_sc1(wss, lane * 16, so, lb);
  if (PACE) __builtin_amdgcn_s_sleep(PACE);
  dma4_lin_sc1(wss, lane * 16, so + 4096, lb + 4096);
  if (PACE) __builtin_amdgcn_s_sleep(PACE);
}
DEV void retile_load(const Params& p, int blk, unsigned stg, int lane) {
  const unsigned v0 = (lane >> 3) * 1280 + 16 * ((lane & 7) ^ (((lane >> 4) & 3) << 1));
  const unsigned v[4] = {v0, v0 + 9216, v0 + 2 * 9216, v0 + 3 * 9216};
  dma4(make_srd(p.w2, W_BYTES), v, (blk / 10) * 40960 + (blk % 10) * 128, stg);
}
struct Frag4 { v4u f[4]; };
DEV Frag4 retile_read(unsigned stg, int lane) {
  const int g = lane >> 4, pp = lane & 3, row = 16 * (g >> 1) + 4 * (g & 1) + ((lane & 15) >> 2);
  Frag4 r;
#pragma unroll
  for (int j = 0; j < 4; ++j) {
    const unsigned a = stg + row * 128 + 16 * ((2 * j + (pp >> 1)) ^ (((row >> 1) & 3) << 1)) + 8 * (pp & 1);
    r.f[j] = ldstr2(a, a + 1024);
  }
  asm volatile("s_waitcnt lgkmcnt(0)" ::: "memory");
  return r;
}
DEV void retile_write(rsrc_t ws, int blk, const Frag4& r, int lane) {
#pragma unroll
  for (int j = 0; j < 4; ++j) {
    const int ng = 4 * (blk % 10) + j;
    __builtin_amdgcn_raw_buffer_store_b128(r.f[j], ws, lane * 16 + WS_W2T + (((ng / 5) * 54 + blk / 10) * 5 + ng % 5) * 1024, 0, SC1);
  }
}

__global__ __launch_bounds__(768, 3) void fused_kernel(Params p_) {
  unsigned long long t_entry;
  const __attribute__((address_space(4))) Params* kp = (const __attribute__((address_space(4))) Params*)__builtin_amdgcn_kernarg_segment_ptr();
  asm volatile("s_memrealtime %0\n\ts_waitcnt lgkmcnt(0)" : "=s"(t_entry), "+s"(kp) : : "memory");
  const Params p = *kp;
  extern __shared__ __attribute__((aligned(16))) unsigned char lds_base[];
  unsigned long long pc;
  unsigned code_bytes;
  asm volatile("s_getpc_b64 %0\n.Lfused_begin:\n\ts_mov_b32 %1, .Lfused_end_a-.Lfused_begin\n\ts_max_i32 %1, %1, .Lfused_end_b-.Lfused_begin"
               : "=s"(pc), "=s"(code_bytes) : : "scc");
  const int lane = threadIdx.x & 63, wave = __builtin_amdgcn_readfirstlane(threadIdx.x >> 6), role = wave >> 2, w = wave & 3;
  const int xcd = blockIdx.x & 7, slot = blockIdx.x >> 3, tb = 32 * xcd + slot;
  const bool gemm_wg = slot < 28;
  const int rp = 4 * (xcd >> 1) + slot / 7, cp = 7 * (xcd & 1) + slot % 7, t0 = 128 * rp, f0 = 128 * cp;
#define HOLD(P) do { if (HOLD_AT == P && blockIdx.x == 128) { for (int i_ = 0; i_ < 100000 && __builtin_amdgcn_s_memrealtime() - t_entry < HOLD_US * 100ull; ++i_) __builtin_amdgcn_s_sleep(1); } } while (0)
  STAMP_INIT();
  STAMP(0);
  asm volatile("" ::"v"((LAS unsigned char*)lds_base));

  const unsigned vx0 = (lane >> 3) * 1280 + 16 * ((lane & 7) ^ (lane >> 4));
  const unsigned vw0 = (lane >> 4) * 3456 + 16 * ((lane & 15) ^ ((lane >> 4) << 2));
  const unsigned vx[4] = {vx0, (vx0 ^ 64) + 9216, vx0 + 2 * 9216, (vx0 ^ 64) + 3 * 9216};
  const unsigned vw[4] = {vw0, vw0 + 12800, vw0 + 2 * 12800, vw0 + 3 * 12800};
  auto stage_x = [&](int i) { dma4(make_srd(p.x, X_BYTES), vx, (LT0 + 32 * w) * 1280 + 128 * ((i + ROTV) % 10), ((2 * i) % 7) * UNIT + w * 4096); };
  auto stage_w = [&](int i) { dma4(make_srd(p.w1, W_BYTES), vw, (64 * ((i + ROTV) % 10) + 16 * w) * 3456 + 2 * LF0, ((2 * i + 1) % 7) * UNIT + w * 4096); };
  const rsrc_t ws = make_rsrc(p.ws, WS_BYTES), xr = make_rsrc(p.x, X_BYTES);
  unsigned ev = 0;
  LnIn ln = {};
  if (role == 1) {
    PRIO(3);
    if (gemm_wg) { stage_x(0); stage_w(0); }
    ev = epoch_load(ws);
    ln = ln_load(xr, 8 * tb + 2 * w + (lane >> 5), lane);
    if (w < 2) retile_load(p, 2 * tb + w, gemm_wg ? RSTG + w * 4096 : 98304 + w * 8192, lane);
  }
  if (gemm_wg && role == 0) { stage_x(2); stage_w(2); }
  if (!gemm_wg) {
    const int tw = (slot - 28) * 12 + wave;
    const unsigned tx = (512 * (xcd >> 1) + (tw % 8) * 64 + lane) * 1280 + (3 + tw / 8) * 128;
    touch(make_srd(p.x, X_BYTES), tx);
    if (tw < 8) touch(make_srd(p.x, X_BYTES), tx + 6 * 128);
#pragma unroll
    for (int j = 0; j < 2; ++j) {
      const int id = (tw * 2 + j) * 64 + lane;
      touch(make_srd(p.w1, W_BYTES), id < 6272 ? (192 + id / 14) * 3456 + (xcd & 1) * 1792 + (id % 14) * 128 : 0u);
    }
    if (tw == 46) touch(make_srd(p.w1, W_BYTES), (192 + (6144 + lane) / 14) * 3456 + (xcd & 1) * 1792 + ((6144 + lane) % 14) * 128);
    if (tw == 47) touch(make_srd(p.w1, W_BYTES), (192 + (6208 + lane) / 14) * 3456 + (xcd & 1) * 1792 + ((6208 + lane) % 14) * 128);
    if (tw == 8) touch(make_srd(p.b1, F_ * 2), lane * 128);
    if (tw == 9) touch(make_srd(p.b2, D_ * 2), lane * 128);
    if (tw == 10) touch(make_srd(p.ws, WS_BYTES), lane * 262144);
    if (tw == 11) touch(make_srd(p.out, X_BYTES), lane * 65536);
  }
  if (wave < 2) dma1(make_srd((const void*)pc, code_bytes & ~15u), lane * 16, (2 * slot + wave) * 1024, JUNK);

  const v4u wss = make_srd(p.ws, WS_BYTES);
  const int dg = 80 * xcd + (gemm_wg ? 2 * slot : 56 + 6 * (slot - 28) + 2 * role);
  const int tt = 4 * xcd + (slot >> 3), ct = slot & 7;
  const unsigned fslot = (tb >> 2) * 128 + (tb & 3) * 4;
  if (threadIdx.x < N_ZERO) sts32(CNT + threadIdx.x * 4, 0u);

  if (role == 2) {
    const KgIn kin = kg_load(p, dg + (w >> 1), lane);
    BAR();
    const float isc = ssm_prepare(ws, kin, GRP, LNST, 2, C_GRP + 96, dg, w, lane, [&] { STAMP(2); await(E_F1OK, 1); STAMP(3); });
    STAMP(6);
    await(E_GELU, 8);
    STAMP(7);
    conv_channel(ws, GRP + (w >> 1) * CH_BYTES, C_GRP + 96 + 16 + (w >> 1) * 4, dg + (w >> 1), w & 1, isc, kin.dsk, lane);
    STAMP(8);
    signal(E_CONV, lane);
    const unsigned tag = lds_tag();
    publish(ws, C_GRP + 96 + 36, 4, WS_F3 + fslot, tag, lane);
    STAMP(9);
    if (w < 2) {
      await<8>(E_DONE + 14 * 4, 4);
      poll_words4(ws, WS_F3 + (ct * 8 + (lane & 7)) * 128, tag);
      v4u sv[5];
#pragma unroll
      for (int i = 0; i < 5; ++i)
        sv[i] = __builtin_amdgcn_raw_buffer_load_b128(ws, (lane >> 3) * 4096 + (lane & 7) * 16, WS_ST + (80 * ct + 40 * w + 8 * i) * 4096 + tt * 128, SC1);
#pragma unroll
      for (int i = 0; i < 5; ++i) sts128(STILE + (40 * w + 8 * i + (lane >> 3)) * 160 + (lane & 7) * 16, sv[i]);
      signal(E_SRDY, lane);
    }
    STAMP(10);
    STAMP_FLUSH();
    asm volatile(".Lfused_end_a:");
    return;
  }

  auto poll_f1 = [&](unsigned tag) {
    poll_words4(ws, WS_F1 + lane * 128, tag);
    signal(E_F1OK, lane);
    if (blockIdx.x == 0 && lane == 0) __builtin_amdgcn_raw_buffer_store_b32(lds32(W_TAG + 4) + 1u, ws, WS_EPOCH, 0, SC1);
  };
  if (role == 1) {
    const int h = lane >> 5, fi = 4 * xcd + slot - 28;
    const bool extra = !gemm_wg && w == 0 && fi < 28;
    const unsigned stg = gemm_wg ? RSTG + w * 4096 : 98304 + w * 8192;
    Frag4 rt = {};
    BAR();
    if (gemm_wg) {
      stage_x(1); stage_w(1);
      WAITV(8); signal(E_FULL, lane);
      STAMP(1);
      if (w < 2) rt = retile_read(stg, lane);
      WAITV(0); signal(E_FULL + 4, lane);
    }
    STAMP(2);
    PRIO(LPRIO);
    if (extra) retile_load(p, 512 + fi, stg + 4096, lane);
    if (!gemm_wg && w < 2) { WAITV(0); rt = retile_read(stg, lane); }
    const unsigned tag = epoch_tag(ev, w == 0, lane);
    ln_compute(ln, ws, 8 * tb + 2 * w + h, 2 * w + h, tag, lane);
    signal(E_LN, lane);
    if (w >= 2) {
      await(E_LN, 4);
      ln_publish(ws, tb, w - 2, lane);
      signal(E_LNPUB, lane);
      STAMP(3);
      publish(ws, C_F1, 2, WS_F1 + fslot, tag, lane);
      STAMP(4);
      PRIO(0);
      if (gemm_wg && w == 2) { await<4>(E_VMQ, 4); poll_f1(tag); }
    } else {
      PRIO(0);
      if (gemm_wg) await<4>(E_VMQ, 4);
      retile_write(ws, 2 * tb + w, rt, lane);
      if (extra) retile_write(ws, 512 + fi, retile_read(stg + 4096, lane), lane);
      STAMP(3);
      publish(ws, C_FW, 2, WS_FW + fslot, tag, lane);
      STAMP(4);
    }
  }

  if (!gemm_wg) {
    const unsigned grp = role * (4 * KSCR + 4 * FPOS), cnt = C_GRP + role * 48;
    const KgIn kin = kg_load(p, dg + (w >> 1), lane);
    if (role == 0) BAR();
    const float isc = ssm_prepare(ws, kin, grp, grp + 4 * KSCR, 0, cnt, dg, w, lane, [&] {
      if (wave == 0) { await(E_LN, 4); poll_f1(lds_tag()); } else { await<4>(E_F1OK, 1); }
    });
    STAMP(16);
    conv_channel(ws, grp + (w >> 1) * CH_BYTES, cnt + 16 + (w >> 1) * 4, dg + (w >> 1), w & 1, isc, kin.dsk, lane);
    WAITV(0);
    group_sync(C_H, 8, lane);
    if (role == 0) { signal(E_GELU, lane); signal(E_GELU, lane); }
    STAMP(5);
  } else if (role == 0) {
    const int wf = w & 1, wt = w >> 1, h = lane >> 5;
    BAR();
    PRIO(2);
    unsigned aa[2][2], ba[2][4];
    {
      const int q = (lane & 15) >> 2, pp = lane & 3, fb = (lane >> 4) & 1;
#pragma unroll
      for (int mi = 0; mi < 2; ++mi) {
        aa[0][mi] = (8 * h + q) * 256 + 16 * ((8 * wf + 4 * (mi ^ wt) + 2 * fb + (pp >> 1)) ^ (q << 2)) + 8 * (pp & 1);
        aa[1][mi] = aa[0][mi] + 4 * UNIT;
        asm volatile("" : "+v"(aa[1][mi]));
      }
#pragma unroll
      for (int s = 0; s < 4; ++s) {
        ba[0][s] = (64 * wt + (lane & 31)) * 128 + 16 * ((2 * s + h) ^ ((lane >> 1) & 7));
        ba[1][s] = ba[0][s] + 4 * UNIT;
        asm volatile("" : "+v"(ba[1][s]));
      }
    }
    f16v acc[2][2] = {};
    v4u A[3][2], B[3][2], st[2];
    unsigned b1v[2], fl = 0, dn = 0, cbase = CNT, sbase = sig_base(lane), one = 1u, vxo = vx0 ^ 64;
    asm volatile("" : "+v"(cbase), "+v"(sbase), "+v"(one), "+v"(vxo));
    f4v cs4 = {};
    const unsigned sel1 = (((lane >> 2) ^ (lane >> 4)) & 1) ? 0u : 0x3f803f80u;
    const v4u sel = {sel1, sel1, sel1, sel1};
    auto rdA = [&](int n, int mi) {
      const int u = (2 * (n >> 2) + 1) % 7;
      const unsigned a = aa[u >> 2][mi] + (u & 3) * UNIT + (n & 3) * 4096;
      A[n % 3][mi] = ldstr2(a, a + 1024);
    };
    auto rdB = [&](int n, int ni) {
      const int u = (2 * (n >> 2)) % 7;
      B[n % 3][ni] = lds128(ba[u >> 2][n & 3] + (u & 3) * UNIT + ni * 4096);
    };
    const v4u xs = make_srd(p.x, X_BYTES), w1s = make_srd(p.w1, W_BYTES);
    unsigned sx[4], sw[4], mb = w * 4096;
#pragma unroll
    for (int j = 0; j < 4; ++j) {
      sx[j] = (LT0 + 32 * w) * 1280 + ((3 + ROTV) % 10) * 128 + j * 9216;
      sw[j] = (((3 + ROTV) % 10) * 64 + 16 * w) * 3456 + 2 * LF0 + j * 12800;
      asm volatile("" : "+s"(sx[j]), "+s"(sw[j]));
    }
    auto piece = [&](auto qc) {
      constexpr int q = qc, i = (q + 8) / 8 + 2 + ((q >> 2) & 1), j = q & 3;
      if constexpr (q >= -4 && q < 52) {
        if constexpr (q & 4) {
          if constexpr (j == 0) dma1(xs, vx0, sx[0], mb + ((2 * i) % 7) * UNIT);
          else dma_next<j * 1024>(xs, (j & 1) ? vxo : vx0, sx[j]);
          sx[j] += (i + ROTV) % 10 == 9 ? -9 * 128 : 128;
          asm volatile("" : "+s"(sx[j]));
        } else {
          if constexpr (j == 0) dma1(w1s, vw0, sw[0], mb + ((2 * i + 1) % 7) * UNIT);
          else dma_next<j * 1024>(w1s, vw0, sw[j]);
          sw[j] += (i + ROTV) % 10 == 9 ? -9 * 64 * 3456 : 64 * 3456;
          asm volatile("" : "+s"(sw[j]));
        }
      }
    };
    auto load_stats = [&] {
#pragma unroll
      for (int i = 0; i < 2; ++i) st[i] = __builtin_amdgcn_raw_buffer_load_b128(ws, (lane & 31) * 16, WS_STAT + (t0 + 64 * wt + 32 * i) * 16, SC1);
    };
    await(E_FULL, 4);
    HOLD(1);
    rdA(0, 0); rdB(0, 0); rdB(0, 1); rdA(0, 1); rdA(1, 0); rdB(1, 0); rdB(1, 1); rdA(1, 1);
    SCHED();
    unsigned tag = 0, tf = 0, td = 0, tv = 0;
    v4u eb[2];
    static_for<40>([&](auto nc) {
      constexpr int n = nc, sg = n >> 2, r = n & 3;
      acc[0][0] = mfma32b(A[n % 3][0], B[n % 3][0], acc[0][0]);
      SCHED();
      if (n < 38) { rdA(n + 2, 0); rdB(n + 2, 0); }
      SCHED();
      acc[0][1] = mfma32b(A[n % 3][0], B[n % 3][1], acc[0][1]);
      SCHED();
      if constexpr (n == 2) { piece(IC<-4>{}); piece(IC<-3>{}); } else if constexpr (n > 2) piece(IC<2 * (n - 3)>{});
      if (r == 1 && sg < 9) fl = *(volatile LAS unsigned*)lp(cbase + (E_FULL - CNT) + (sg + 1) * 4);
      if (r == 2 && sg < 7) dn = *(volatile LAS unsigned*)lp(cbase + (E_DONE - CNT) + sg * 4);
      if (n == 34) { dn = *(volatile LAS unsigned*)lp(cbase + (E_LN - CNT)); tag = *(volatile LAS unsigned*)lp(cbase + (W_TAG - CNT)); }
      SCHED();
      cs4 = mfma16b(sel, A[n % 3][0], cs4);
      SCHED();
      acc[1][0] = mfma32b(A[n % 3][1], B[n % 3][0], acc[1][0]);
      SCHED();
      if (n < 38) { rdB(n + 2, 1); rdA(n + 2, 1); }
      if (r == 1) signal_at<E_DONE - CNT + sg * 4>(sbase, one);
      if (n == 39) { sts32(SEXC + w * 128 + (lane & 31) * 4, f2u(cs4[0])); signal(E_SX + w * 4, lane); }
      SCHED();
      acc[1][1] = mfma32b(A[n % 3][1], B[n % 3][1], acc[1][1]);
      SCHED();
      if constexpr (n == 2) { piece(IC<-2>{}); piece(IC<-1>{}); } else if constexpr (n > 2) piece(IC<2 * (n - 3) + 1>{});
      if (r == 1 && sg < 9) { CHK(fl, E_FULL + (sg + 1) * 4, 4, tf); if (sg & 1) STAMPL(16 + (sg >> 1)); }
      if (r == 2 && sg < 7) CHK(dn, E_DONE + sg * 4, 4, td);
      if (r == 0 && sg >= 1 && sg < 9) {
        if (sg < 8) TACC(tv, WAITV(8)); else TACC(tv, WAITV(4));
        signal_at<E_FULL - CNT + (sg + 1) * 4>(sbase, one);
      }
      if (n == 29) load_stats();
      if (n == 30) {
#pragma unroll
        for (int i = 0; i < 2; ++i)
          b1v[i] = __builtin_amdgcn_raw_buffer_load_b16(make_rsrc(p.b1, F_ * 2), (lane & 31) * 2, (f0 + 64 * wf + 32 * (i ^ wt)) * 2, 0);
        signal_at<E_VMQ - CNT>(sbase, one);
      }
      if (n == 35) {
        if (__builtin_expect(__builtin_amdgcn_readfirstlane(dn) < 4, 0)) { await(E_LN, 4); tag = peek(W_TAG); }
        tag = __builtin_amdgcn_readfirstlane(tag);
      }
      if (n == 37) {
        if (__builtin_expect(!__all(st[0].y == tag && st[0].w == tag && st[1].y == tag && st[1].w == tag), 0)) {
#pragma unroll 1
          for (unsigned s = 0; s < SPIN_CAP; ++s) {
            asm volatile("" ::: "memory");
            load_stats();
            if (__all(st[0].y == tag && st[0].w == tag && st[1].y == tag && st[1].w == tag)) break;
          }
        }
      }
      if (n == 37 || n == 38) {
        constexpr int i = n == 38;
        const float mu = -u2f(st[i].x), ir = u2f(st[i].z), mh = bfr(mu), ih = bfr(ir);
        eb[i] = h ? (v4u){0u, 0u, 0u, 0u} : (v4u){pkbf(mh, opq(mu - mh)), pkbf(mh, 0.f), pkbf(ih, opq(ir - ih)), 0u};
        asm volatile("" : "+v"(eb[i]));
      }
      SCHED();
    });
    HOLD(2);
    auto ext = [&](int mi, float S) {
      const float sh = bfr(S);
      const v4u ea = h ? (v4u){0u, 0u, 0u, 0u} : (v4u){pkbf(sh, sh), pkbf(S - sh, 0.f), b1v[mi] | (b1v[mi] << 16), 0u};
      acc[mi][0] = mfma32b(ea, eb[0], acc[mi][0]);
      acc[mi][1] = mfma32b(ea, eb[1], acc[mi][1]);
    };
    fl = peek(E_SX + (w ^ 2) * 4);
    float so = u2f(peek(SEXC + (w ^ 2) * 128 + (lane & 31) * 4));
    ext(0, cs4[0]);
    SCHED();
    if (__builtin_expect(__builtin_amdgcn_readfirstlane(fl) < 1, 0)) {
      await(E_SX + (w ^ 2) * 4, 1);
      so = u2f(peek(SEXC + (w ^ 2) * 128 + (lane & 31) * 4));
    }
    ext(1, so);
    STAMP(1);
    if (f0 + 64 * wf < F_) {
      const unsigned hv = ((lane >> 4) & 1) * 55296 + h * 256 + (lane & 15) * 16;
#pragma unroll
      for (int mi = 0; mi < 2; ++mi) {
#pragma unroll
        for (int ni = 0; ni < 2; ++ni) {
          const float ir = u2f(st[ni].z), rho = __builtin_amdgcn_rcpf(ir);
          const float k1 = -2.302208198f * rho, k2 = k1 * 0.044715f * rho * rho;
          const unsigned hs = WS_H + ((t0 + 64 * wt + 32 * ni) / 16) * 55296 + ((f0 + 64 * wf) / 32 + (mi ^ wt)) * 1024;
#pragma unroll
          for (int hf = 0; hf < 2; ++hf) {
            unsigned o[4];
#pragma unroll
            for (int r = 0; r < 4; ++r) {
              const f2v a = {acc[mi][ni][8 * hf + 2 * r], acc[mi][ni][8 * hf + 2 * r + 1]};
              const f2v x = a * (a * a * k2 + k1);
              const f2v e = {__builtin_amdgcn_exp2f(x.x), __builtin_amdgcn_exp2f(x.y)};
              const f2v dd = e * ir + ir;
              const f2v rr = {__builtin_amdgcn_rcpf(dd.x), __builtin_amdgcn_rcpf(dd.y)};
              const f2v gl = a * rr;
              o[r] = pkbf(gl.x, gl.y);
            }
            __builtin_amdgcn_raw_buffer_store_b128((v4u){o[0], o[1], o[2], o[3]}, ws, HADDR, 0, SC1);
            SCHED();
          }
        }
        if (mi == 0) signal(E_GELU, lane);
      }
    } else {
      signal(E_GELU, lane);
    }
    signal(E_GELU, lane);
    STAMP(4); STAMPC(13);
    publish(ws, C_H, 4, WS_F2 + (rp * 16 + cp) * 128, tag, lane);
    STAMP(5);
  }

  if (role == 0) {
    const unsigned tag = lds_tag();
    const int mh = w & 1, kh = w >> 1, g = lane >> 4;
    const int tok = 64 * tt + 32 * mh + 16 * kh + (lane & 15);
    poll_words(ws, WS_F2 + ((tt >> 1) * 16 + min(lane, 13)) * 128, tag);
    HOLD(3);
    PRIO(2);
    constexpr int W = 6;
    v4u H[W][2], A[2][5];
    f4v acc[2][5] = {};
    const unsigned hs = WS_H + (4 * tt + 2 * mh) * 55296 + 27 * kh * 1024;
    const unsigned hv[2] = {lane * 16 + kh * 55296u, lane * 16 + (kh ^ 1) * 55296u};
#pragma unroll
    for (int k = 0; k < W; ++k)
#pragma unroll
      for (int ti = 0; ti < 2; ++ti) H[k][ti] = __builtin_amdgcn_raw_buffer_load_b128(ws, hv[ti], hs + k * 1024, SC1);
    SCHED();
    v2u xv[5], bv[5];
    unsigned ab[3];
#pragma unroll
    for (int i = 0; i < 3; ++i) { ab[i] = kh * 15360 + lane * 16 + 2 * i * RING2; asm volatile("" : "+v"(ab[i])); }
    auto rd = [&](int k, int n) { A[k & 1][n] = lds128(ab[((k / 3) % 5) >> 1] + (((k / 3) % 5) & 1) * RING2 + (k % 3) * 5120 + n * 1024); };
    unsigned fl = 0, cbase = CNT, sbase = sig_base(lane), one = 1u;
    asm volatile("" : "+v"(cbase), "+v"(sbase), "+v"(one));
    await<1>(E_FULL + 10 * 4, 4);
#pragma unroll
    for (int n = 0; n < 5; ++n) rd(0, n);
    STAMP(7); STAMPC(14);
    SCHED();
    static_for<27>([&](auto kc) {
      constexpr int k = kc;
#pragma unroll
      for (int ti = 0; ti < 2; ++ti) {
#pragma unroll
        for (int n = 0; n < 5; ++n) {
          acc[ti][n] = mfma16b(A[k & 1][n], H[k % W][ti], acc[ti][n]);
          SCHED();
          if (ti == 0 && k < 26) {
            rd(k + 1, n);
            if (n == 4 && k % 3 == 1) signal_at<E_DONE - CNT + (10 + k / 3) * 4>(sbase, one);
            SCHED();
          }
          if (ti == 1 && n == 0 && k % 3 == 1 && k < 24) { fl = *(volatile LAS unsigned*)lp(cbase + (E_FULL - CNT) + (11 + k / 3) * 4); SCHED(); }
        }
        if (k + W < 27) H[k % W][ti] = __builtin_amdgcn_raw_buffer_load_b128(ws, hv[ti], hs + (k + W) * 1024, SC1);
        if constexpr (k >= 21 && k < 26) {
          if (ti == 0) xv[k - 21] = __builtin_amdgcn_raw_buffer_load_b64(xr, tok * 1280 + 8 * g + 32 * (k - 21), 160 * ct, 0);
          else bv[k - 21] = __builtin_amdgcn_raw_buffer_load_b64(make_rsrc(p.b2, D_ * 2), 8 * g + 32 * (k - 21), 160 * ct, 0);
        }
        SCHED();
      }
      if (k % 3 == 1 && k < 24) { check(fl, E_FULL + (11 + k / 3) * 4, 4); STAMPL(22 + k / 3); SCHED(); }
    });
    HOLD(4);
    await<1>(E_DONE + 14 * 4, 4);
#pragma unroll
    for (int n = 0; n < 5; ++n) sts128(RED2 + w * 5120 + n * 1024 + lane * 16, __builtin_bit_cast(v4u, acc[1][n]));
    signal(E_RED + w * 4, lane);
    await<1>(E_SRDY, 2);
    await<1>(E_RED + (w ^ 2) * 4, 1);
    STAMP(9);
    const unsigned sa = STILE + (4 * g + ((lane & 15) >> 2)) * 160 + (32 * mh + 16 * kh + 4 * (lane & 3)) * 2;
#pragma unroll
    for (int n = 0; n < 5; ++n) {
      const f4v o = __builtin_bit_cast(f4v, lds128(RED2 + (w ^ 2) * 5120 + n * 1024 + lane * 16));
      const v2u sv = ldstr(sa + n * 2560);
      const float bz[4] = {blo(bv[n].x), bhi(bv[n].x), blo(bv[n].y), bhi(bv[n].y)};
      const float sz[4] = {blo(sv.x), bhi(sv.x), blo(sv.y), bhi(sv.y)};
      const float xz[4] = {blo(xv[n].x), bhi(xv[n].x), blo(xv[n].y), bhi(xv[n].y)};
      float r[4];
#pragma unroll
      for (int i = 0; i < 4; ++i) {
        const float z = acc[0][n][i] + o[i] + bz[i];
        const float gt = z * __builtin_amdgcn_rcpf(1.f + __builtin_amdgcn_exp2f(z * -1.44269504088896f));
        r[i] = fmaf(sz[i], gt, xz[i]);
      }
      *(GAS v2u*)(p.out + tok * 640 + 80 * ct + 16 * n + 4 * g) = (v2u){pkbf(r[0], r[1]), pkbf(r[2], r[3])};
    }
    HOLD(5);
    asm volatile(".Lfused_end_b:");
  } else {
    if (gemm_wg) await<4>(E_GELU, FWPOLL_AT);
    if (w == 0) { poll_words4(ws, WS_FW + lane * 128, lds_tag()); signal(E_FWOK, lane); } else { await<2>(E_FWOK, 1); }
    if (gemm_wg) await<2>(E_GELU, 8);
    STAMP(5);
    PRIO(3);
#pragma unroll 1
    for (int c = 0; c < 3; ++c) w2_chunk<PACE0>(wss, ct, w, c, lane);
    WAITV(16); signal(E_FULL + 10 * 4, lane);
    WAITV(8); signal(E_FULL + 11 * 4, lane);
    WAITV(0); signal(E_FULL + 12 * 4, lane);
    STAMP(6);
    await<4>(E_CONV, 4);
    STAMP(7);
    w2_chunk<0>(wss, ct, w, 3, lane);
    w2_chunk<0>(wss, ct, w, 4, lane);
    WAITV(8);
    signal(E_FULL + 13 * 4, lane);
#pragma unroll 1
    for (int c = 5; c < 9; ++c) {
      await(E_DONE + (5 + c) * 4, 4);
      w2_chunk<0>(wss, ct, w, c, lane);
      WAITV(8);
      signal(E_FULL + (9 + c) * 4, lane);
      STAMP(19 + c);
    }
    WAITV(0);
    signal(E_FULL + 18 * 4, lane);
    STAMP(10);
  }
  STAMP_FLUSH();
}

}

extern "C" void kernel_launch(void* const* d_in, const int* in_sizes, int n_in, void* d_out, int out_size, void* d_ws, size_t ws_size,
                              hipStream_t stream) {
  Params p;
  p.x = (const unsigned short*)d_in[0];
  p.lA = (const unsigned short*)d_in[1];
  p.Ai = (const unsigned short*)d_in[2];
  p.Cr = (const unsigned short*)d_in[3];
  p.Ci = (const unsigned short*)d_in[4];
  p.ldt = (const unsigned short*)d_in[5];
  p.Dsk = (const unsigned short*)d_in[6];
  p.w1 = (const unsigned short*)d_in[8];
  p.b1 = (const unsigned short*)d_in[9];
  p.w2 = (const unsigned short*)d_in[10];
  p.b2 = (const unsigned short*)d_in[11];
  p.out = (unsigned short*)d_out;
  p.ws = (unsigned char*)d_ws;
  (void)hipFuncSetAttribute((const void*)fused_kernel, hipFuncAttributeMaxDynamicSharedMemorySize, LDS_BYTES);
  fused_kernel<<<256, 768, LDS_BYTES, stream>>>(p);
}
```

```cpp
#define HOLD_AT 5
#define HOLD_US 40
#include <hip/hip_runtime.h>
#include <hip/hip_bf16.h>

namespace {

typedef unsigned v4u __attribute__((ext_vector_type(4)));
typedef unsigned v2u __attribute__((ext_vector_type(2)));
typedef short s4v __attribute__((ext_vector_type(4)));
typedef float f2v __attribute__((ext_vector_type(2)));
typedef float f4v __attribute__((ext_vector_type(4)));
typedef float f16v __attribute__((ext_vector_type(16)));
typedef _Float16 h2v __attribute__((ext_vector_type(2)));
typedef _Float16 h8v __attribute__((ext_vector_type(8)));
typedef __bf16 b2v __attribute__((ext_vector_type(2)));
typedef __bf16 b8v __attribute__((ext_vector_type(8)));
#define GAS __attribute__((address_space(1)))
#define LAS __attribute__((address_space(3)))
#define DEV __device__ __forceinline__
typedef __amdgpu_buffer_rsrc_t rsrc_t;

constexpr int L_ = 2048, D_ = 640, F_ = 1728;
constexpr unsigned X_BYTES = L_ * D_ * 2, W_BYTES = D_ * F_ * 2;
constexpr int SC1 = 16;
constexpr unsigned SPIN_CAP = 1u << 16;

constexpr unsigned WS_EPOCH = 0;
constexpr unsigned WS_F1 = 4096;
constexpr unsigned WS_F2 = 65536;
constexpr unsigned WS_FW = 24576;
constexpr unsigned WS_F3 = 16384;
constexpr unsigned WS_STAT = 32768;
constexpr unsigned WS_XNT = 131072;
constexpr unsigned WS_W2T = 2752512;
constexpr unsigned WS_H = 5242880;
constexpr unsigned WS_ST = 12582912;
constexpr unsigned WS_STAMP = 15204352;
constexpr unsigned WS_BYTES = WS_STAMP + 256 * 12 * 64 * 8;

constexpr unsigned LDS_BYTES = 163840;
constexpr unsigned UNIT = 16384;
constexpr unsigned KSCR = 8704;
constexpr unsigned FPOS = 2576;
constexpr unsigned GRP = 7 * UNIT;
constexpr unsigned LNST = GRP + 4 * KSCR;
constexpr unsigned CNT = LNST + 10752;
constexpr unsigned SEXC = CNT + 512;
constexpr unsigned JUNK = CNT + 1024;
constexpr unsigned RSTG = 6 * UNIT;
constexpr unsigned UCP = 2368;
constexpr unsigned CH_R3 = 4 * UCP, CH_U = 9856, CH_BYTES = 2 * KSCR;
constexpr unsigned RING2 = 30720;
constexpr unsigned STILE = 4 * RING2, RED2 = STILE + 12800;
constexpr unsigned E_FULL = CNT, E_DONE = CNT + 80;
constexpr unsigned E_GELU = CNT + 160, E_LN = CNT + 164, E_LNPUB = CNT + 168, E_SRDY = CNT + 172, E_RED = CNT + 176, E_SX = CNT + 192;
constexpr unsigned C_H = CNT + 208, C_F1 = CNT + 212, E_F1OK = CNT + 216, E_CONV = CNT + 220, E_VMQ = CNT + 76, C_FW = CNT + 156, E_FWOK = CNT + 360;
constexpr unsigned C_GRP = CNT + 224;
constexpr unsigned W_TAG = CNT + 368;
constexpr int N_ZERO = 92;

struct Params {
  const unsigned short *x, *lA, *Ai, *Cr, *Ci, *ldt, *Dsk, *w1, *b1, *w2, *b2;
  unsigned short* out;
  unsigned char* ws;
};

#define STAMP(i) do { } while (0)
#define STAMPC(i) do { } while (0)
#define STAMPV(i, v) do { } while (0)
#define STAMP_INIT() do { } while (0)
#define STAMP_FLUSH() do { } while (0)

#define PRIO(x) __builtin_amdgcn_s_setprio(x)
#define PACE0 1
#define APRIO 0
#define LT0 t0
#define LF0 f0
#undef TACC
#define TACC(acc, stmt) do { stmt; } while (0)
#define LPRIO 1
#define FWPOLL_AT 4
#define STAMPL(i) do { } while (0)
#define CHK(e, a, n, t) check(e, a, n)
#define HADDR (hv + hs + hf * 512)
#define ROTV (((slot / 7 * 5 + 1) / 2 + slot % 7 * 3) % 10)
#define BAR() asm volatile("s_waitcnt lgkmcnt(0)\n\ts_barrier" ::: "memory")
#define WAITV(n) asm volatile("s_waitcnt vmcnt(" #n ")" ::: "memory")
#define SCHED() __builtin_amdgcn_sched_barrier(0)

template <int I> struct IC { constexpr operator int() const { return I; } };
template <int N, int I = 0, class F> DEV void static_for(F&& f) {
  if constexpr (I < N) { f(IC<I>{}); static_for<N, I + 1>(f); }
}
DEV float u2f(unsigned u) { return __builtin_bit_cast(float, u); }
DEV unsigned f2u(float f) { return __builtin_bit_cast(unsigned, f); }
DEV h2v u2h(unsigned u) { return __builtin_bit_cast(h2v, u); }
DEV float blo(unsigned u) { return u2f(u << 16); }
DEV float bhi(unsigned u) { return u2f(u & 0xffff0000u); }
DEV unsigned pkbf(float a, float b) { return __builtin_bit_cast(unsigned, __builtin_convertvector((f2v){a, b}, b2v)); }
DEV unsigned pkh(float a, float b) { return __builtin_bit_cast(unsigned, __builtin_convertvector((f2v){a, b}, h2v)); }
DEV float opq(float x) { asm("" : "+v"(x)); return x; }
DEV float bfr(float a) { return blo(pkbf(a, a)); }
DEV f16v mfma32b(v4u a, v4u b, f16v c) { return __builtin_amdgcn_mfma_f32_32x32x16_bf16(__builtin_bit_cast(b8v, a), __builtin_bit_cast(b8v, b), c, 0, 0, 0); }
DEV f16v mfma32h(v4u a, v4u b, f16v c) { return __builtin_amdgcn_mfma_f32_32x32x16_f16(__builtin_bit_cast(h8v, a), __builtin_bit_cast(h8v, b), c, 0, 0, 0); }
DEV f4v mfma16b(v4u a, v4u b, f4v c) { return __builtin_amdgcn_mfma_f32_16x16x32_bf16(__builtin_bit_cast(b8v, a), __builtin_bit_cast(b8v, b), c, 0, 0, 0); }
DEV f4v mfma16h(v4u a, v4u b, f4v c) { return __builtin_amdgcn_mfma_f32_16x16x32_f16(__builtin_bit_cast(h8v, a), __builtin_bit_cast(h8v, b), c, 0, 0, 0); }

DEV LAS unsigned char* lp(unsigned a) { return (LAS unsigned char*)(size_t)a; }
DEV v4u lds128(unsigned a) { return *(LAS v4u*)lp(a); }
DEV v2u lds64(unsigned a) { return *(LAS v2u*)lp(a); }
DEV unsigned lds32(unsigned a) { return *(LAS unsigned*)lp(a); }
DEV void sts128(unsigned a, v4u v) { *(LAS v4u*)lp(a) = v; }
DEV void sts64(unsigned a, v2u v) { *(LAS v2u*)lp(a) = v; }
DEV void sts32(unsigned a, unsigned v) { *(LAS unsigned*)lp(a) = v; }
DEV v2u ldstr(unsigned a) { return __builtin_bit_cast(v2u, __builtin_amdgcn_ds_read_tr16_b64_v4i16((LAS s4v*)lp(a))); }
DEV v4u ldstr2(unsigned a0, unsigned a1) { const v2u x = ldstr(a0), y = ldstr(a1); return (v4u){x.x, x.y, y.x, y.y}; }

DEV v4u make_srd(const void* ptr, unsigned bytes) {
  return (v4u){(unsigned)(size_t)ptr, (unsigned)((size_t)ptr >> 32) & 0xffffu, bytes, 0x00020000u};
}
DEV rsrc_t make_rsrc(const void* ptr, unsigned bytes) { return __builtin_amdgcn_make_buffer_rsrc((void*)ptr, 0, bytes, 0x00020000); }
#define DMA_HEAD "s_mov_b32 m0, %0\n\ts_nop 0\n\t"
DEV void dma1(v4u srd, unsigned v, unsigned soff, unsigned lds) {
  asm volatile(DMA_HEAD "buffer_load_dwordx4 %1, %2, %3 offen lds" ::"s"(lds), "v"(v), "s"(srd), "s"(soff) : "memory");
}
template <int OFF> DEV void dma_next(v4u srd, unsigned v, unsigned soff) {
  asm volatile("buffer_load_dwordx4 %0, %1, %2 offen offset:%3 lds" ::"v"(v), "s"(srd), "s"(soff), "i"(OFF) : "memory");
}
DEV void touch(v4u srd, unsigned v) {
}
DEV void dma4(v4u srd, const unsigned (&v)[4], unsigned soff, unsigned lds) {
  asm volatile(DMA_HEAD "buffer_load_dwordx4 %1, %5, %6 offen lds\n\tbuffer_load_dwordx4 %2, %5, %6 offen offset:1024 lds\n\t"
               "buffer_load_dwordx4 %3, %5, %6 offen offset:2048 lds\n\tbuffer_load_dwordx4 %4, %5, %6 offen offset:3072 lds"
               ::"s"(lds), "v"(v[0]), "v"(v[1]), "v"(v[2]), "v"(v[3]), "s"(srd), "s"(soff) : "memory");
}
DEV void dma4_lin_sc1(v4u srd, unsigned v, unsigned soff, unsigned lds) {
  asm volatile(DMA_HEAD "buffer_load_dwordx4 %1, %2, %3 offen sc1 lds\n\tbuffer_load_dwordx4 %1, %2, %3 offen offset:1024 sc1 lds\n\t"
               "buffer_load_dwordx4 %1, %2, %3 offen offset:2048 sc1 lds\n\tbuffer_load_dwordx4 %1, %2, %3 offen offset:3072 sc1 lds"
               ::"s"(lds), "v"(v), "s"(srd), "s"(soff) : "memory");
}

DEV void poll_words(rsrc_t ws, unsigned off, unsigned tag) {
#pragma unroll 1
  for (unsigned s = 0; s < SPIN_CAP; ++s) {
    asm volatile("" ::: "memory");
    if (__all(__builtin_amdgcn_raw_buffer_load_b32(ws, off, 0, SC1) == tag)) break;
  }
  asm volatile("" ::: "memory");
}
DEV void poll_words4(rsrc_t ws, unsigned off, unsigned tag) {
#pragma unroll 1
  for (unsigned s = 0; s < SPIN_CAP; ++s) {
    asm volatile("" ::: "memory");
    const v4u v = __builtin_amdgcn_raw_buffer_load_b128(ws, off, 0, SC1);
    if (__all(v.x == tag && v.y == tag && v.z == tag && v.w == tag)) break;
    __builtin_amdgcn_s_sleep(8);
  }
  asm volatile("" ::: "memory");
}
DEV unsigned sig_base(int lane) { return lane == 0 ? CNT : JUNK + lane * 4; }
template <int OFF> DEV void signal_at(unsigned base, unsigned one) {
  asm volatile("ds_add_u32 %0, %1 offset:%2" ::"v"(base), "v"(one), "i"(OFF) : "memory");
}
DEV void signal(unsigned a, int lane) {
  asm volatile("ds_add_u32 %0, %1" ::"v"(sig_base(lane) + (a - CNT)), "v"(1u) : "memory");
}
DEV unsigned peek(unsigned a) { return *(volatile LAS unsigned*)lp(a); }
template <int SLEEP = 1> DEV void await(unsigned a, unsigned need) {
#pragma unroll 1
  for (unsigned s = 0; s < (SPIN_CAP << 2); ++s) {
    asm volatile("" : "+s"(s));
    if (__builtin_amdgcn_readfirstlane(peek(a)) >= need) break;
    __builtin_amdgcn_s_sleep(SLEEP);
  }
  asm volatile("" ::: "memory");
}
DEV void check(unsigned early, unsigned a, unsigned need) {
  if (__builtin_expect(__builtin_amdgcn_readfirstlane(early) < need, 0)) await<1>(a, need);
  asm volatile("" ::: "memory");
}
DEV unsigned lds_arrive(unsigned cnt, int lane) {
  unsigned old;
  asm volatile("ds_add_rtn_u32 %0, %1, %2\n\ts_waitcnt lgkmcnt(0)" : "=&v"(old) : "v"(sig_base(lane) + (cnt - CNT)), "v"(1u) : "memory");
  return __builtin_amdgcn_readfirstlane(old);
}
template <int SLEEP = 1> DEV void group_sync(unsigned cnt, unsigned target, int lane) {
  if (lds_arrive(cnt, lane) + 1 < target) await<SLEEP>(cnt, target);
}
DEV void publish(rsrc_t ws, unsigned cnt, unsigned nwaves, unsigned flag_off, unsigned tag, int lane) {
  WAITV(0);
  if (lds_arrive(cnt, lane) + 1 == nwaves && lane == 0) __builtin_amdgcn_raw_buffer_store_b32(tag, ws, flag_off, 0, SC1);
}
DEV unsigned epoch_load(rsrc_t ws) { return __builtin_amdgcn_raw_buffer_load_b32(ws, WS_EPOCH, 0, SC1); }
DEV unsigned epoch_tag(unsigned ev, bool share, int lane) {
  const unsigned e = __builtin_amdgcn_readfirstlane(ev);
  const unsigned tag = e * 0x9E3779B1u + 0x7F4A7C15u;
  if (share && lane == 0) sts64(W_TAG, (v2u){tag, e});
  return tag;
}
DEV unsigned lds_tag() { return __builtin_amdgcn_readfirstlane(lds32(W_TAG)); }

template <int CTRL> DEV float dpp_add(float v) {
  return v + u2f(__builtin_amdgcn_update_dpp(0u, f2u(v), CTRL, 0xf, 0xf, false));
}
template <int CTRL> DEV float dpp_max(float v) {
  return fmaxf(v, u2f(__builtin_amdgcn_update_dpp(0u, f2u(v), CTRL, 0xf, 0xf, false)));
}
DEV float half_sum(float v) {
  v = dpp_add<0x128>(v); v = dpp_add<0x124>(v); v = dpp_add<0x122>(v); v = dpp_add<0x121>(v);
  const auto r = __builtin_amdgcn_permlane16_swap(f2u(v), f2u(v), false, false);
  return u2f(r[0]) + u2f(r[1]);
}
DEV float wave_max(float v) {
  v = dpp_max<0x128>(v); v = dpp_max<0x124>(v); v = dpp_max<0x122>(v); v = dpp_max<0x121>(v);
  const auto r = __builtin_amdgcn_permlane16_swap(f2u(v), f2u(v), false, false);
  v = fmaxf(u2f(r[0]), u2f(r[1]));
  const auto q = __builtin_amdgcn_permlane32_swap(f2u(v), f2u(v), false, false);
  return fmaxf(u2f(q[0]), u2f(q[1]));
}

struct LnIn { v2u v[5]; };
DEV LnIn ln_load(rsrc_t xr, int token, int lane) {
  LnIn r;
#pragma unroll
  for (int m = 0; m < 5; ++m) r.v[m] = __builtin_amdgcn_raw_buffer_load_b64(xr, token * 1280 + (lane & 31) * 8 + m * 256, 0, 0);
  return r;
}
DEV void ln_compute(const LnIn& in, rsrc_t ws, int token, int row, unsigned tag, int lane) {
  float xv[20], s = 0.f, q = 0.f;
#pragma unroll
  for (int m = 0; m < 5; ++m) { xv[4 * m] = blo(in.v[m].x); xv[4 * m + 1] = bhi(in.v[m].x); xv[4 * m + 2] = blo(in.v[m].y); xv[4 * m + 3] = bhi(in.v[m].y); }
#pragma unroll
  for (int m = 0; m < 20; ++m) { s = opq(s + xv[m]); q = opq(fmaf(xv[m], xv[m], q)); }
  s = half_sum(s); q = half_sum(q);
  const float mean = s * (1.f / 640.f);
  const float var = fmaxf(fmaf(-mean, mean, q * (1.f / 640.f)), 0.f);
  const float m1 = bfr(mean), s1 = bfr(__builtin_amdgcn_sqrtf(bfr(bfr(var) + 1e-5f)));
  const float r1 = __builtin_amdgcn_rcpf(s1);
  const float m2 = bfr((mean - m1) * r1), s2 = bfr(__builtin_amdgcn_sqrtf(bfr(bfr(var * r1 * r1) + 1e-5f)));
  if ((lane & 31) == 0)
    __builtin_amdgcn_raw_buffer_store_b128((v4u){f2u(fmaf(m2, s1, m1)), tag, f2u(s1 * s2), tag}, ws, WS_STAT + token * 16, 0, SC1);
  const float c = -m1 * r1;
#pragma unroll
  for (int m = 0; m < 5; ++m) {
    const v2u o = {pkh(opq(fmaf(xv[4 * m], r1, c)), opq(fmaf(xv[4 * m + 1], r1, c))), pkh(opq(fmaf(xv[4 * m + 2], r1, c)), opq(fmaf(xv[4 * m + 3], r1, c)))};
    sts64(LNST + row * 1344 + (lane & 31) * 8 + m * 256, o);
  }
}
DEV void ln_publish(rsrc_t ws, int tb, int aw, int lane) {
  const unsigned a = LNST + ((lane & 15) >> 2) * 1344 + (lane >> 4) * 32 + (lane & 3) * 8 + aw * 640;
#pragma unroll
  for (int i = 0; i < 5; ++i) {
    const v4u v = ldstr2(a + i * 128, a + i * 128 + 4 * 1344);
    __builtin_amdgcn_raw_buffer_store_b128(v, ws, WS_XNT + tb * 10240 + (aw * 320 + i * 64 + lane) * 16, 0, SC1);
  }
}

struct Cx { float r, i; };
DEV Cx cmul(Cx a, Cx b) { return {opq(fmaf(a.r, b.r, -a.i * b.i)), opq(fmaf(a.r, b.i, a.i * b.r))}; }
DEV Cx csqr(Cx a) { return {opq(fmaf(a.r, a.r, -a.i * a.i)), opq((a.r + a.r) * a.i)}; }
DEV unsigned ch(Cx a) { return pkh(a.r, a.i); }
DEV unsigned hmul(unsigned a, unsigned b) {
  unsigned t, r;
  asm("v_pk_mul_f16 %0, %1, %2 op_sel:[0,0] op_sel_hi:[0,1]" : "=v"(t) : "v"(a), "v"(b));
  asm("v_pk_fma_f16 %0, %1, %2, %3 op_sel:[1,1,0] op_sel_hi:[1,0,1] neg_lo:[0,1,0]" : "=v"(r) : "v"(a), "v"(b), "v"(t));
  return r;
}
DEV float exp_bf(float x) {
  const float t = x * 1.44268798828125f, lo = x * 7.0526077138765e-06f;
  const float n = __builtin_rintf(t);
  return bfr(__builtin_amdgcn_ldexpf(__builtin_amdgcn_exp2f((t - n) + lo), (int)fminf(fmaxf(n, -200.f), 200.f)));
}
DEV Cx cexp_at(float lg, float rh, float rl, float P) {
  const float m = __builtin_amdgcn_exp2f(lg * P), ph = fmaf(rl, P, __builtin_amdgcn_fractf(rh * P));
  return {opq(m * __builtin_amdgcn_cosf(ph)), opq(m * __builtin_amdgcn_sinf(ph))};
}
struct KgIn { unsigned lA, Ai, Cr, Ci; float dt, dsk; };
DEV KgIn kg_load(const Params& p, int d, int lane) {
  KgIn k;
  k.lA = ((const GAS unsigned*)p.lA)[d * 64 + lane]; k.Ai = ((const GAS unsigned*)p.Ai)[d * 64 + lane];
  k.Cr = ((const GAS unsigned*)p.Cr)[d * 64 + lane]; k.Ci = ((const GAS unsigned*)p.Ci)[d * 64 + lane];
  k.dt = u2f((unsigned)((const GAS unsigned short*)p.ldt)[d] << 16);
  k.dsk = u2f((unsigned)((const GAS unsigned short*)p.Dsk)[d] << 16);
  return k;
}
DEV float kg_wave(const KgIn& k, int g, unsigned scr, unsigned fp, unsigned fp_free, int lane) {
  const float dt = exp_bf(k.dt), c2 = dt + dt;
  const float mx = wave_max(fmaxf(fmaxf(fabsf(blo(k.Cr)), fabsf(bhi(k.Cr))), fmaxf(fabsf(blo(k.Ci)), fabsf(bhi(k.Ci))))) * c2;
  int ex = __builtin_amdgcn_frexp_expf(mx);
  ex = ex < -120 ? -120 : (ex > 120 ? 120 : ex);
  const float cs = c2 * __builtin_amdgcn_ldexpf(1.f, -ex);
  const int sh = g ? 0 : 16;
  const float lg = -(exp_bf(bhi(k.lA << sh)) * dt) * 1.44269504088896f, ai = bhi(k.Ai << sh) * dt;
  const Cx c0 = {opq(bhi(k.Cr << sh) * cs), opq(-bhi(k.Ci << sh) * cs)};
  const float rh = ai * 0.15915494f, rl = fmaf(ai, 0.15915494f, -rh) + ai * 6.4206382e-09f;
  const Cx v1 = cexp_at(lg, -rh, -rl, 1.f), w1 = cexp_at(lg, rh, rl, 8.f), G = cexp_at(lg, -rh, -rl, 256.f);
  const unsigned wa = scr + lane * 4, ra = scr + (lane & 31) * 272 + (lane >> 5) * 16;
  {
    const Cx w2 = csqr(w1), w3 = cmul(w2, w1), w4 = csqr(w2), w8 = csqr(w4), w12 = cmul(w8, w4), w16 = csqr(w8);
    const Cx w20 = cmul(w16, w4), w24 = csqr(w12), w28 = cmul(w24, w4);
    const unsigned o[3] = {ch(w1), ch(w2), ch(w3)};
    const unsigned bs[7] = {ch(w4), ch(w8), ch(w12), ch(w16), ch(w20), ch(w24), ch(w28)};
    sts32(wa, 0x00003c00u);
#pragma unroll
    for (int j = 0; j < 3; ++j) sts32(wa + (1 + j) * 272, o[j]);
#pragma unroll
    for (int b = 0; b < 7; ++b) {
      sts32(wa + (4 + 4 * b) * 272, bs[b]);
#pragma unroll
      for (int j = 0; j < 3; ++j) sts32(wa + (5 + 4 * b + j) * 272, hmul(bs[b], o[j]));
    }
  }
  asm volatile("" ::: "memory");
  v4u y[8];
#pragma unroll
  for (int s = 0; s < 8; ++s) y[s] = lds128(ra + s * 32);
  asm volatile("" ::: "memory");
  {
    const Cx v2 = csqr(v1), v3 = cmul(v2, v1), v4 = csqr(v2), v6 = csqr(v3), x4 = cmul(c0, v4);
    const unsigned h1 = ch(v1), h2 = ch(v2), h3 = ch(v3), h4 = ch(v4), h6 = ch(v6), hx0 = ch(c0), hx4 = ch(x4);
    sts32(wa + 0 * 272, hx0); sts32(wa + 1 * 272, hmul(hx0, h1)); sts32(wa + 2 * 272, hmul(hx0, h2)); sts32(wa + 3 * 272, hmul(hx0, h3));
    sts32(wa + 4 * 272, hx4); sts32(wa + 5 * 272, hmul(hx4, h1)); sts32(wa + 6 * 272, hmul(hx4, h2)); sts32(wa + 7 * 272, hmul(hx4, h3));
    const Cx d1 = cmul(c0, G), d2 = cmul(d1, G), d3 = cmul(d2, G), d4 = cmul(d3, G);
    const Cx G4 = csqr(csqr(G));
    const Cx d5 = cmul(d1, G4), d6 = cmul(d2, G4), d7 = cmul(d3, G4), d8 = cmul(d4, G4);
    const unsigned e1 = ch(d1), e2 = ch(d2), e3 = ch(d3);
    sts32(wa + 8 * 272, e1); sts32(wa + 9 * 272, hmul(e1, h2)); sts32(wa + 10 * 272, hmul(e1, h4)); sts32(wa + 11 * 272, hmul(e1, h6));
    sts32(wa + 12 * 272, e2); sts32(wa + 13 * 272, hmul(e2, h4)); sts32(wa + 14 * 272, e3); sts32(wa + 15 * 272, hmul(e3, h4));
    sts32(wa + 16 * 272, ch(d4)); sts32(wa + 17 * 272, ch(d5)); sts32(wa + 18 * 272, ch(d6)); sts32(wa + 19 * 272, ch(d7));
    sts32(wa + 20 * 272, ch(d8));
  }
  asm volatile("" ::: "memory");
  v4u xx[8];
#pragma unroll
  for (int s = 0; s < 8; ++s) xx[s] = lds128(ra + s * 32);
  SCHED();
  f16v acc = {};
#pragma unroll
  for (int s = 0; s < 8; ++s) acc = mfma32h(xx[s], y[s], acc);
  const int j = lane & 31, h = lane >> 5;
  await(E_LNPUB, fp_free);
  sts128(fp + (8 * j + 4 * h) * 4, (v4u){f2u(acc[0]), f2u(acc[1]), f2u(acc[2]), f2u(acc[3])});
  sts64(fp + (h ? 384 + 2 * j : 256 + 4 * j) * 4, (v2u){f2u(acc[4]), f2u(acc[5])});
  sts64(fp + (h ? 448 + 2 * j : 258 + 4 * j) * 4, (v2u){f2u(acc[6]), f2u(acc[7])});
  if (lane <= 32) sts32(fp + (512 + 128 * h + j) * 4, f2u(acc[8]));
  if (h == 0) {
#pragma unroll
    for (int i = 1; i < 4; ++i) sts32(fp + (512 + j + 32 * i) * 4, f2u(acc[8 + i]));
  }
  return __builtin_amdgcn_ldexpf(1.f, ex);
}
DEV void kg_table0(unsigned cb, unsigned fp, int g, int lane) {
  auto f1 = [&](int i) { return opq(u2f(lds32(fp + i * 4)) + u2f(lds32(fp + FPOS + i * 4))); };
  if (g == 0) {
    const int j = lane & 31, h = lane >> 5;
    const unsigned a = fp + (h ? 256 + 4 * j : 8 * j) * 4;
    const v4u l0 = lds128(a), h0 = lds128(a + 16), l1 = lds128(a + FPOS), h1 = lds128(a + FPOS + 16);
    const float e0 = opq(u2f(l0.x) + u2f(l1.x)), e1 = opq(u2f(l0.y) + u2f(l1.y)), e2 = opq(u2f(l0.z) + u2f(l1.z)), e3 = opq(u2f(l0.w) + u2f(l1.w));
    const float e4 = opq(u2f(h0.x) + u2f(h1.x)), e5 = opq(u2f(h0.y) + u2f(h1.y)), e6 = opq(u2f(h0.z) + u2f(h1.z)), e7 = opq(u2f(h0.w) + u2f(h1.w));
    const v4u o0 = {pkh(e0, e1), pkh(e2, e3), pkh(e4, e5), pkh(e6, e7)};
    const v4u o1 = {pkh(e0, e0), pkh(e1, e2), pkh(e2, e2), pkh(e3, e4)};
    sts128(cb + 32 + lane * 16, h ? o1 : o0);
    const float fa = f1(512), fb = f1(513);
    const unsigned aa = pkh(fa, fa), bb = pkh(fb, fb);
    if (lane < 4) sts128(cb + 32 + (128 + lane) * 16, lane == 0 ? (v4u){aa, aa, pkh(fa, fb), bb} : lane == 1 ? (v4u){bb, bb, 0u, 0u} : (v4u){0u, 0u, 0u, 0u});
  } else {
    const float a = f1(384 + 2 * lane), b = f1(385 + 2 * lane), c = f1(386 + 2 * lane);
    sts128(cb + 32 + (64 + lane) * 16, (v4u){pkh(a, a), pkh(a, b), pkh(b, b), pkh(c, c)});
    const float lo = f1(640 - 2 * lane), hi = f1(lane ? 641 - 2 * lane : 640);
    sts32(cb + CH_R3 + lane * 4, pkh(lane ? hi : 0.f, lo));
    if (lane < 16) sts32(cb + CH_R3 + 256 + lane * 4, 0u);
    if (lane < 32) sts32(cb + (lane >> 3) * UCP + (lane & 7) * 4, 0u);
  }
}
DEV void kg_copies(unsigned cb, int g, int lane) {
  auto one = [&](int chunk) {
    const unsigned o = cb + 32 + chunk * 16;
    const v2u pv = lds64(o - 8);
    const v4u e = lds128(o);
    const unsigned a0 = __builtin_amdgcn_alignbit(pv.y, pv.x, 16), a1 = __builtin_amdgcn_alignbit(e.x, pv.y, 16), a2 = __builtin_amdgcn_alignbit(e.y, e.x, 16),
                   a3 = __builtin_amdgcn_alignbit(e.z, e.y, 16), a4 = __builtin_amdgcn_alignbit(e.w, e.z, 16);
    sts128(o + UCP, (v4u){a1, a2, a3, a4});
    sts128(o + 2 * UCP, (v4u){pv.y, e.x, e.y, e.z});
    sts128(o + 3 * UCP, (v4u){a0, a1, a2, a3});
  };
  one(64 * g + lane);
  if (lane < 2) one(128 + 2 * g + lane);
}
DEV void conv_load_u(rsrc_t ws, unsigned grp, int d0, int w, int lane) {
  v4u v[2];
#pragma unroll
  for (int i = 0; i < 2; ++i)
    v[i] = __builtin_amdgcn_raw_buffer_load_b128(ws, (lane >> 1) * 10240 + (lane & 1) * 16, WS_XNT + (64 * w + 32 * i) * 10240 + d0 * 16, SC1);
  if (lane < 16) sts128(grp + (w >> 1) * CH_BYTES + CH_U + (w & 1) * 256 + lane * 16, (v4u){0u, 0u, 0u, 0u});
#pragma unroll
  for (int i = 0; i < 2; ++i) sts128(grp + (lane & 1) * CH_BYTES + CH_U + 512 + (64 * w + 32 * i + (lane >> 1)) * 16, v[i]);
}
template <class F1>
DEV float ssm_prepare(rsrc_t ws, const KgIn& k, unsigned grp, unsigned fp, unsigned fp_free, unsigned cnt, int d0, int w, int lane, F1 f1) {
  const int ci = w >> 1, g = w & 1;
  const float isc = kg_wave(k, g, grp + w * KSCR, fp + w * FPOS, fp_free, lane);
  group_sync(cnt, 4, lane);
  kg_table0(grp + ci * CH_BYTES, fp + 2 * ci * FPOS, g, lane);
  group_sync(cnt + 4 + ci * 4, 2, lane);
  kg_copies(grp + ci * CH_BYTES, g, lane);
  f1();
  conv_load_u(ws, grp, d0, w, lane);
  group_sync(cnt + 12, 4, lane);
  return isc;
}

template <int B> DEV void conv_tail(f4v (&acc)[8], unsigned ua, unsigned r3, v4u w7, v4u w9) {
  const f4v b7 = mfma16h(lds128(ua + 512 * B), w7, (f4v){}), b9 = mfma16h(lds128(ua + 512 * B + 16), w9, (f4v){});
  const v4u bx = {pkh(b7[0], b9[0]), pkh(b7[1], b9[1]), pkh(b7[2], b9[2]), pkh(b7[3], b9[3])};
#pragma unroll
  for (int dl = 4; dl + B < 8; ++dl) {
    const unsigned a = r3 - 64 * dl;
    acc[dl + B] = mfma16h(bx, (v4u){lds32(a), lds32(a + 4), lds32(a + 8), lds32(a + 12)}, acc[dl + B]);
  }
}
DEV void conv_channel(rsrc_t ws, unsigned cb, unsigned cnt, int d, int eh, float isc, float dsk, int lane) {
  const int g = lane >> 4, c16 = lane & 15;
  const unsigned ga = cb + (c16 >> 2) * UCP + 64 - 16 * g + 8 * (lane & 3) + eh * 256;
  const unsigned ba = cb + CH_U + 480 + 32 * c16 + 16 * g - eh * 256;
  f4v acc[8] = {};
  v4u G[2][4], B[2][8];
#pragma unroll
  for (int i = 0; i < 8; ++i) { B[0][i] = lds128(ba + 512 * i); if (i < 4) G[0][i] = ldstr2(ga + 512 * i, ga + 512 * i - 8); }
  {
    unsigned w7[4], w9[4];
#pragma unroll
    for (int i = 0; i < 4; ++i) {
      const int k0 = 8 * g + 2 * i - c16;
      w7[i] = ((k0 >= 5 && k0 <= 11) ? 0x3c00u : 0u) | ((k0 >= 4 && k0 <= 10) ? 0x3c000000u : 0u);
      w9[i] = ((k0 >= 4 && k0 <= 12) ? 0x3c00u : 0u) | ((k0 >= 3 && k0 <= 11) ? 0x3c000000u : 0u);
    }
    const v4u W7 = {w7[0], w7[1], w7[2], w7[3]}, W9 = {w9[0], w9[1], w9[2], w9[3]};
    const unsigned ua = cb + CH_U + 448 + 32 * c16 + 16 * g, r3 = cb + CH_R3 + (127 - c16 + 4 * g) * 4;
    if (eh) {
      conv_tail<0>(acc, ua, r3, W7, W9);
      conv_tail<3>(acc, ua, r3, W7, W9);
    } else {
      conv_tail<1>(acc, ua, r3, W7, W9);
      conv_tail<2>(acc, ua, r3, W7, W9);
      const v4u g4 = ldstr2(ga + 2048, ga + 2048 - 8);
#pragma unroll
      for (int j = 4; j < 8; ++j) acc[j] = mfma16h(g4, B[0][j - 4], acc[j]);
    }
  }
  SCHED();
#pragma unroll
  for (int e = 0; e < 4; ++e) {
    int m = 0;
#pragma unroll
    for (int cc = 0; cc < 4; ++cc)
#pragma unroll
      for (int j = cc; j < 8; ++j) {
        acc[j] = mfma16h(G[e & 1][cc], B[e & 1][j - cc], acc[j]);
        if (e < 3 && (m & 1) == 0 && m < 24) {
          const int i = m >> 1;
          if (i == 1 || i > 8) { const int c = i == 1 ? 0 : i - 8; G[(e + 1) & 1][c] = ldstr2(ga + 512 * c + 64 * (e + 1), ga + 512 * c + 64 * (e + 1) - 8); }
          else { const int c = i == 0 ? 0 : i - 1; B[(e + 1) & 1][c] = lds128(ba + 512 * c - 64 * (e + 1)); }
        }
        ++m;
        SCHED();
      }
  }
  const unsigned to = 2 * (1024 * eh + 16 * c16 + 4 * g);
  v2u uu[4];
#pragma unroll
  for (int j = 0; j < 4; ++j) uu[j] = lds64(cb + CH_U + 512 + to + 512 * j);
  group_sync<1>(cnt, 2, lane);
  f4v mine[4];
  if (eh) {
#pragma unroll
    for (int j = 0; j < 4; ++j) { sts128(cb + 4096 + j * 1024 + lane * 16, __builtin_bit_cast(v4u, acc[j])); mine[j] = acc[4 + j]; }
  } else {
#pragma unroll
    for (int j = 0; j < 4; ++j) { sts128(cb + j * 1024 + lane * 16, __builtin_bit_cast(v4u, acc[4 + j])); mine[j] = acc[j]; }
  }
  group_sync<1>(cnt + 8, 2, lane);
#pragma unroll
  for (int j = 0; j < 4; ++j) {
    const f4v o = __builtin_bit_cast(f4v, lds128(cb + (eh ^ 1) * 4096 + j * 1024 + lane * 16));
    const h2v u0 = u2h(uu[j].x), u1 = u2h(uu[j].y);
    const float s0 = opq(fmaf(mine[j][0] + o[0], isc, dsk * (float)u0.x)), s1 = opq(fmaf(mine[j][1] + o[1], isc, dsk * (float)u0.y));
    const float s2 = opq(fmaf(mine[j][2] + o[2], isc, dsk * (float)u1.x)), s3 = opq(fmaf(mine[j][3] + o[3], isc, dsk * (float)u1.y));
    __builtin_amdgcn_raw_buffer_store_b64((v2u){pkbf(s0, s1), pkbf(s2, s3)}, ws, to + 512 * j, WS_ST + d * 4096, SC1);
  }
}

template <int PACE> DEV void w2_chunk(v4u wss, int ct, int lw, int c, int lane) {
  const unsigned po = (lw & 1) * 7168;
  const unsigned so = WS_W2T + (ct * 54 + 27 * (lw >> 1) + 3 * c) * 5120 + po, lb = (c % 5) * RING2 + (lw >> 1) * 15360 + po;
  dma4_lin_sc1(wss, lane * 16, so, lb);
  if (PACE) __builtin_amdgcn_s_sleep(PACE);
  dma4_lin_sc1(wss, lane * 16, so + 4096, lb + 4096);
  if (PACE) __builtin_amdgcn_s_sleep(PACE);
}
DEV void retile_load(const Params& p, int blk, unsigned stg, int lane) {
  const unsigned v0 = (lane >> 3) * 1280 + 16 * ((lane & 7) ^ (((lane >> 4) & 3) << 1));
  const unsigned v[4] = {v0, v0 + 9216, v0 + 2 * 9216, v0 + 3 * 9216};
  dma4(make_srd(p.w2, W_BYTES), v, (blk / 10) * 40960 + (blk % 10) * 128, stg);
}
struct Frag4 { v4u f[4]; };
DEV Frag4 retile_read(unsigned stg, int lane) {
  const int g = lane >> 4, pp = lane & 3, row = 16 * (g >> 1) + 4 * (g & 1) + ((lane & 15) >> 2);
  Frag4 r;
#pragma unroll
  for (int j = 0; j < 4; ++j) {
    const unsigned a = stg + row * 128 + 16 * ((2 * j + (pp >> 1)) ^ (((row >> 1) & 3) << 1)) + 8 * (pp & 1);
    r.f[j] = ldstr2(a, a + 1024);
  }
  asm volatile("s_waitcnt lgkmcnt(0)" ::: "memory");
  return r;
}
DEV void retile_write(rsrc_t ws, int blk, const Frag4& r, int lane) {
#pragma unroll
  for (int j = 0; j < 4; ++j) {
    const int ng = 4 * (blk % 10) + j;
    __builtin_amdgcn_raw_buffer_store_b128(r.f[j], ws, lane * 16 + WS_W2T + (((ng / 5) * 54 + blk / 10) * 5 + ng % 5) * 1024, 0, SC1);
  }
}

__global__ __launch_bounds__(768, 3) void fused_kernel(Params p_) {
  unsigned long long t_entry;
  const __attribute__((address_space(4))) Params* kp = (const __attribute__((address_space(4))) Params*)__builtin_amdgcn_kernarg_segment_ptr();
  asm volatile("s_memrealtime %0\n\ts_waitcnt lgkmcnt(0)" : "=s"(t_entry), "+s"(kp) : : "memory");
  const Params p = *kp;
  extern __shared__ __attribute__((aligned(16))) unsigned char lds_base[];
  unsigned long long pc;
  unsigned code_bytes;
  asm volatile("s_getpc_b64 %0\n.Lfused_begin:\n\ts_mov_b32 %1, .Lfused_end_a-.Lfused_begin\n\ts_max_i32 %1, %1, .Lfused_end_b-.Lfused_begin"
               : "=s"(pc), "=s"(code_bytes) : : "scc");
  const int lane = threadIdx.x & 63, wave = __builtin_amdgcn_readfirstlane(threadIdx.x >> 6), role = wave >> 2, w = wave & 3;
  const int xcd = blockIdx.x & 7, slot = blockIdx.x >> 3, tb = 32 * xcd + slot;
  const bool gemm_wg = slot < 28;
  const int rp = 4 * (xcd >> 1) + slot / 7, cp = 7 * (xcd & 1) + slot % 7, t0 = 128 * rp, f0 = 128 * cp;
#define HOLD(P) do { if (HOLD_AT == P) { for (int i_ = 0; i_ < 100000 && __builtin_amdgcn_s_memrealtime() - t_entry < HOLD_US * 100ull; ++i_) __builtin_amdgcn_s_sleep(1); } } while (0)
  STAMP_INIT();
  STAMP(0);
  asm volatile("" ::"v"((LAS unsigned char*)lds_base));

  const unsigned vx0 = (lane >> 3) * 1280 + 16 * ((lane & 7) ^ (lane >> 4));
  const unsigned vw0 = (lane >> 4) * 3456 + 16 * ((lane & 15) ^ ((lane >> 4) << 2));
  const unsigned vx[4] = {vx0, (vx0 ^ 64) + 9216, vx0 + 2 * 9216, (vx0 ^ 64) + 3 * 9216};
  const unsigned vw[4] = {vw0, vw0 + 12800, vw0 + 2 * 12800, vw0 + 3 * 12800};
  auto stage_x = [&](int i) { dma4(make_srd(p.x, X_BYTES), vx, (LT0 + 32 * w) * 1280 + 128 * ((i + ROTV) % 10), ((2 * i) % 7) * UNIT + w * 4096); };
  auto stage_w = [&](int i) { dma4(make_srd(p.w1, W_BYTES), vw, (64 * ((i + ROTV) % 10) + 16 * w) * 3456 + 2 * LF0, ((2 * i + 1) % 7) * UNIT + w * 4096); };
  const rsrc_t ws = make_rsrc(p.ws, WS_BYTES), xr = make_rsrc(p.x, X_BYTES);
  unsigned ev = 0;
  LnIn ln = {};
  if (role == 1) {
    PRIO(3);
    if (gemm_wg) { stage_x(0); stage_w(0); }
    ev = epoch_load(ws);
    ln = ln_load(xr, 8 * tb + 2 * w + (lane >> 5), lane);
    if (w < 2) retile_load(p, 2 * tb + w, gemm_wg ? RSTG + w * 4096 : 98304 + w * 8192, lane);
  }
  if (gemm_wg && role == 0) { stage_x(2); stage_w(2); }
  if (!gemm_wg) {
    const int tw = (slot - 28) * 12 + wave;
    const unsigned tx = (512 * (xcd >> 1) + (tw % 8) * 64 + lane) * 1280 + (3 + tw / 8) * 128;
    touch(make_srd(p.x, X_BYTES), tx);
    if (tw < 8) touch(make_srd(p.x, X_BYTES), tx + 6 * 128);
#pragma unroll
    for (int j = 0; j < 2; ++j) {
      const int id = (tw * 2 + j) * 64 + lane;
      touch(make_srd(p.w1, W_BYTES), id < 6272 ? (192 + id / 14) * 3456 + (xcd & 1) * 1792 + (id % 14) * 128 : 0u);
    }
    if (tw == 46) touch(make_srd(p.w1, W_BYTES), (192 + (6144 + lane) / 14) * 3456 + (xcd & 1) * 1792 + ((6144 + lane) % 14) * 128);
    if (tw == 47) touch(make_srd(p.w1, W_BYTES), (192 + (6208 + lane) / 14) * 3456 + (xcd & 1) * 1792 + ((6208 + lane) % 14) * 128);
    if (tw == 8) touch(make_srd(p.b1, F_ * 2), lane * 128);
    if (tw == 9) touch(make_srd(p.b2, D_ * 2), lane * 128);
    if (tw == 10) touch(make_srd(p.ws, WS_BYTES), lane * 262144);
    if (tw == 11) touch(make_srd(p.out, X_BYTES), lane * 65536);
  }
  if (wave < 2) dma1(make_srd((const void*)pc, code_bytes & ~15u), lane * 16, (2 * slot + wave) * 1024, JUNK);

  const v4u wss = make_srd(p.ws, WS_BYTES);
  const int dg = 80 * xcd + (gemm_wg ? 2 * slot : 56 + 6 * (slot - 28) + 2 * role);
  const int tt = 4 * xcd + (slot >> 3), ct = slot & 7;
  const unsigned fslot = (tb >> 2) * 128 + (tb & 3) * 4;
  if (threadIdx.x < N_ZERO) sts32(CNT + threadIdx.x * 4, 0u);

  if (role == 2) {
    const KgIn kin = kg_load(p, dg + (w >> 1), lane);
    BAR();
    const float isc = ssm_prepare(ws, kin, GRP, LNST, 2, C_GRP + 96, dg, w, lane, [&] { STAMP(2); await(E_F1OK, 1); STAMP(3); });
    STAMP(6);
    await(E_GELU, 8);
    STAMP(7);
    conv_channel(ws, GRP + (w >> 1) * CH_BYTES, C_GRP + 96 + 16 + (w >> 1) * 4, dg + (w >> 1), w & 1, isc, kin.dsk, lane);
    STAMP(8);
    signal(E_CONV, lane);
    const unsigned tag = lds_tag();
    publish(ws, C_GRP + 96 + 36, 4, WS_F3 + fslot, tag, lane);
    STAMP(9);
    if (w < 2) {
      await<8>(E_DONE + 14 * 4, 4);
      poll_words4(ws, WS_F3 + (ct * 8 + (lane & 7)) * 128, tag);
      v4u sv[5];
#pragma unroll
      for (int i = 0; i < 5; ++i)
        sv[i] = __builtin_amdgcn_raw_buffer_load_b128(ws, (lane >> 3) * 4096 + (lane & 7) * 16, WS_ST + (80 * ct + 40 * w + 8 * i) * 4096 + tt * 128, SC1);
#pragma unroll
      for (int i = 0; i < 5; ++i) sts128(STILE + (40 * w + 8 * i + (lane >> 3)) * 160 + (lane & 7) * 16, sv[i]);
      signal(E_SRDY, lane);
    }
    STAMP(10);
    STAMP_FLUSH();
    asm volatile(".Lfused_end_a:");
    return;
  }

  auto poll_f1 = [&](unsigned tag) {
    poll_words4(ws, WS_F1 + lane * 128, tag);
    signal(E_F1OK, lane);
    if (blockIdx.x == 0 && lane == 0) __builtin_amdgcn_raw_buffer_store_b32(lds32(W_TAG + 4) + 1u, ws, WS_EPOCH, 0, SC1);
  };
  if (role == 1) {
    const int h = lane >> 5, fi = 4 * xcd + slot - 28;
    const bool extra = !gemm_wg && w == 0 && fi < 28;
    const unsigned stg = gemm_wg ? RSTG + w * 4096 : 98304 + w * 8192;
    Frag4 rt = {};
    BAR();
    if (gemm_wg) {
      stage_x(1); stage_w(1);
      WAITV(8); signal(E_FULL, lane);
      STAMP(1);
      if (w < 2) rt = retile_read(stg, lane);
      WAITV(0); signal(E_FULL + 4, lane);
    }
    STAMP(2);
    PRIO(LPRIO);
    if (extra) retile_load(p, 512 + fi, stg + 4096, lane);
    if (!gemm_wg && w < 2) { WAITV(0); rt = retile_read(stg, lane); }
    const unsigned tag = epoch_tag(ev, w == 0, lane);
    ln_compute(ln, ws, 8 * tb + 2 * w + h, 2 * w + h, tag, lane);
    signal(E_LN, lane);
    if (w >= 2) {
      await(E_LN, 4);
      ln_publish(ws, tb, w - 2, lane);
      signal(E_LNPUB, lane);
      STAMP(3);
      publish(ws, C_F1, 2, WS_F1 + fslot, tag, lane);
      STAMP(4);
      PRIO(0);
      if (gemm_wg && w == 2) { await<4>(E_VMQ, 4); poll_f1(tag); }
    } else {
      PRIO(0);
      if (gemm_wg) await<4>(E_VMQ, 4);
      retile_write(ws, 2 * tb + w, rt, lane);
      if (extra) retile_write(ws, 512 + fi, retile_read(stg + 4096, lane), lane);
      STAMP(3);
      publish(ws, C_FW, 2, WS_FW + fslot, tag, lane);
      STAMP(4);
    }
  }

  if (!gemm_wg) {
    const unsigned grp = role * (4 * KSCR + 4 * FPOS), cnt = C_GRP + role * 48;
    const KgIn kin = kg_load(p, dg + (w >> 1), lane);
    if (role == 0) BAR();
    const float isc = ssm_prepare(ws, kin, grp, grp + 4 * KSCR, 0, cnt, dg, w, lane, [&] {
      if (wave == 0) { await(E_LN, 4); poll_f1(lds_tag()); } else { await<4>(E_F1OK, 1); }
    });
    STAMP(16);
    conv_channel(ws, grp + (w >> 1) * CH_BYTES, cnt + 16 + (w >> 1) * 4, dg + (w >> 1), w & 1, isc, kin.dsk, lane);
    WAITV(0);
    group_sync(C_H, 8, lane);
    if (role == 0) { signal(E_GELU, lane); signal(E_GELU, lane); }
    STAMP(5);
  } else if (role == 0) {
    const int wf = w & 1, wt = w >> 1, h = lane >> 5;
    BAR();
    PRIO(2);
    unsigned aa[2][2], ba[2][4];
    {
      const int q = (lane & 15) >> 2, pp = lane & 3, fb = (lane >> 4) & 1;
#pragma unroll
      for (int mi = 0; mi < 2; ++mi) {
        aa[0][mi] = (8 * h + q) * 256 + 16 * ((8 * wf + 4 * (mi ^ wt) + 2 * fb + (pp >> 1)) ^ (q << 2)) + 8 * (pp & 1);
        aa[1][mi] = aa[0][mi] + 4 * UNIT;
        asm volatile("" : "+v"(aa[1][mi]));
      }
#pragma unroll
      for (int s = 0; s < 4; ++s) {
        ba[0][s] = (64 * wt + (lane & 31)) * 128 + 16 * ((2 * s + h) ^ ((lane >> 1) & 7));
        ba[1][s] = ba[0][s] + 4 * UNIT;
        asm volatile("" : "+v"(ba[1][s]));
      }
    }
    f16v acc[2][2] = {};
    v4u A[3][2], B[3][2], st[2];
    unsigned b1v[2], fl = 0, dn = 0, cbase = CNT, sbase = sig_base(lane), one = 1u, vxo = vx0 ^ 64;
    asm volatile("" : "+v"(cbase), "+v"(sbase), "+v"(one), "+v"(vxo));
    f4v cs4 = {};
    const unsigned sel1 = (((lane >> 2) ^ (lane >> 4)) & 1) ? 0u : 0x3f803f80u;
    const v4u sel = {sel1, sel1, sel1, sel1};
    auto rdA = [&](int n, int mi) {
      const int u = (2 * (n >> 2) + 1) % 7;
      const unsigned a = aa[u >> 2][mi] + (u & 3) * UNIT + (n & 3) * 4096;
      A[n % 3][mi] = ldstr2(a, a + 1024);
    };
    auto rdB = [&](int n, int ni) {
      const int u = (2 * (n >> 2)) % 7;
      B[n % 3][ni] = lds128(ba[u >> 2][n & 3] + (u & 3) * UNIT + ni * 4096);
    };
    const v4u xs = make_srd(p.x, X_BYTES), w1s = make_srd(p.w1, W_BYTES);
    unsigned sx[4], sw[4], mb = w * 4096;
#pragma unroll
    for (int j = 0; j < 4; ++j) {
      sx[j] = (LT0 + 32 * w) * 1280 + ((3 + ROTV) % 10) * 128 + j * 9216;
      sw[j] = (((3 + ROTV) % 10) * 64 + 16 * w) * 3456 + 2 * LF0 + j * 12800;
      asm volatile("" : "+s"(sx[j]), "+s"(sw[j]));
    }
    auto piece = [&](auto qc) {
      constexpr int q = qc, i = (q + 8) / 8 + 2 + ((q >> 2) & 1), j = q & 3;
      if constexpr (q >= -4 && q < 52) {
        if constexpr (q & 4) {
          if constexpr (j == 0) dma1(xs, vx0, sx[0], mb + ((2 * i) % 7) * UNIT);
          else dma_next<j * 1024>(xs, (j & 1) ? vxo : vx0, sx[j]);
          sx[j] += (i + ROTV) % 10 == 9 ? -9 * 128 : 128;
          asm volatile("" : "+s"(sx[j]));
        } else {
          if constexpr (j == 0) dma1(w1s, vw0, sw[0], mb + ((2 * i + 1) % 7) * UNIT);
          else dma_next<j * 1024>(w1s, vw0, sw[j]);
          sw[j] += (i + ROTV) % 10 == 9 ? -9 * 64 * 3456 : 64 * 3456;
          asm volatile("" : "+s"(sw[j]));
        }
      }
    };
    auto load_stats = [&] {
#pragma unroll
      for (int i = 0; i < 2; ++i) st[i] = __builtin_amdgcn_raw_buffer_load_b128(ws, (lane & 31) * 16, WS_STAT + (t0 + 64 * wt + 32 * i) * 16, SC1);
    };
    await(E_FULL, 4);
    HOLD(1);
    rdA(0, 0); rdB(0, 0); rdB(0, 1); rdA(0, 1); rdA(1, 0); rdB(1, 0); rdB(1, 1); rdA(1, 1);
    SCHED();
    unsigned tag = 0, tf = 0, td = 0, tv = 0;
    v4u eb[2];
    static_for<40>([&](auto nc) {
      constexpr int n = nc, sg = n >> 2, r = n & 3;
      acc[0][0] = mfma32b(A[n % 3][0], B[n % 3][0], acc[0][0]);
      SCHED();
      if (n < 38) { rdA(n + 2, 0); rdB(n + 2, 0); }
      SCHED();
      acc[0][1] = mfma32b(A[n % 3][0], B[n % 3][1], acc[0][1]);
      SCHED();
      if constexpr (n == 2) { piece(IC<-4>{}); piece(IC<-3>{}); } else if constexpr (n > 2) piece(IC<2 * (n - 3)>{});
      if (r == 1 && sg < 9) fl = *(volatile LAS unsigned*)lp(cbase + (E_FULL - CNT) + (sg + 1) * 4);
      if (r == 2 && sg < 7) dn = *(volatile LAS unsigned*)lp(cbase + (E_DONE - CNT) + sg * 4);
      if (n == 34) { dn = *(volatile LAS unsigned*)lp(cbase + (E_LN - CNT)); tag = *(volatile LAS unsigned*)lp(cbase + (W_TAG - CNT)); }
      SCHED();
      cs4 = mfma16b(sel, A[n % 3][0], cs4);
      SCHED();
      acc[1][0] = mfma32b(A[n % 3][1], B[n % 3][0], acc[1][0]);
      SCHED();
      if (n < 38) { rdB(n + 2, 1); rdA(n + 2, 1); }
      if (r == 1) signal_at<E_DONE - CNT + sg * 4>(sbase, one);
      if (n == 39) { sts32(SEXC + w * 128 + (lane & 31) * 4, f2u(cs4[0])); signal(E_SX + w * 4, lane); }
      SCHED();
      acc[1][1] = mfma32b(A[n % 3][1], B[n % 3][1], acc[1][1]);
      SCHED();
      if constexpr (n == 2) { piece(IC<-2>{}); piece(IC<-1>{}); } else if constexpr (n > 2) piece(IC<2 * (n - 3) + 1>{});
      if (r == 1 && sg < 9) { CHK(fl, E_FULL + (sg + 1) * 4, 4, tf); if (sg & 1) STAMPL(16 + (sg >> 1)); }
      if (r == 2 && sg < 7) CHK(dn, E_DONE + sg * 4, 4, td);
      if (r == 0 && sg >= 1 && sg < 9) {
        if (sg < 8) TACC(tv, WAITV(8)); else TACC(tv, WAITV(4));
        signal_at<E_FULL - CNT + (sg + 1) * 4>(sbase, one);
      }
      if (n == 29) load_stats();
      if (n == 30) {
#pragma unroll
        for (int i = 0; i < 2; ++i)
          b1v[i] = __builtin_amdgcn_raw_buffer_load_b16(make_rsrc(p.b1, F_ * 2), (lane & 31) * 2, (f0 + 64 * wf + 32 * (i ^ wt)) * 2, 0);
        signal_at<E_VMQ - CNT>(sbase, one);
      }
      if (n == 35) {
        if (__builtin_expect(__builtin_amdgcn_readfirstlane(dn) < 4, 0)) { await(E_LN, 4); tag = peek(W_TAG); }
        tag = __builtin_amdgcn_readfirstlane(tag);
      }
      if (n == 37) {
        if (__builtin_expect(!__all(st[0].y == tag && st[0].w == tag && st[1].y == tag && st[1].w == tag), 0)) {
#pragma unroll 1
          for (unsigned s = 0; s < SPIN_CAP; ++s) {
            asm volatile("" ::: "memory");
            load_stats();
            if (__all(st[0].y == tag && st[0].w == tag && st[1].y == tag && st[1].w == tag)) break;
          }
        }
      }
      if (n == 37 || n == 38) {
        constexpr int i = n == 38;
        const float mu = -u2f(st[i].x), ir = u2f(st[i].z), mh = bfr(mu), ih = bfr(ir);
        eb[i] = h ? (v4u){0u, 0u, 0u, 0u} : (v4u){pkbf(mh, opq(mu - mh)), pkbf(mh, 0.f), pkbf(ih, opq(ir - ih)), 0u};
        asm volatile("" : "+v"(eb[i]));
      }
      SCHED();
    });
    HOLD(2);
    auto ext = [&](int mi, float S) {
      const float sh = bfr(S);
      const v4u ea = h ? (v4u){0u, 0u, 0u, 0u} : (v4u){pkbf(sh, sh), pkbf(S - sh, 0.f), b1v[mi] | (b1v[mi] << 16), 0u};
      acc[mi][0] = mfma32b(ea, eb[0], acc[mi][0]);
      acc[mi][1] = mfma32b(ea, eb[1], acc[mi][1]);
    };
    fl = peek(E_SX + (w ^ 2) * 4);
    float so = u2f(peek(SEXC + (w ^ 2) * 128 + (lane & 31) * 4));
    ext(0, cs4[0]);
    SCHED();
    if (__builtin_expect(__builtin_amdgcn_readfirstlane(fl) < 1, 0)) {
      await(E_SX + (w ^ 2) * 4, 1);
      so = u2f(peek(SEXC + (w ^ 2) * 128 + (lane & 31) * 4));
    }
    ext(1, so);
    STAMP(1);
    if (f0 + 64 * wf < F_) {
      const unsigned hv = ((lane >> 4) & 1) * 55296 + h * 256 + (lane & 15) * 16;
#pragma unroll
      for (int mi = 0; mi < 2; ++mi) {
#pragma unroll
        for (int ni = 0; ni < 2; ++ni) {
          const float ir = u2f(st[ni].z), rho = __builtin_amdgcn_rcpf(ir);
          const float k1 = -2.302208198f * rho, k2 = k1 * 0.044715f * rho * rho;
          const unsigned hs = WS_H + ((t0 + 64 * wt + 32 * ni) / 16) * 55296 + ((f0 + 64 * wf) / 32 + (mi ^ wt)) * 1024;
#pragma unroll
          for (int hf = 0; hf < 2; ++hf) {
            unsigned o[4];
#pragma unroll
            for (int r = 0; r < 4; ++r) {
              const f2v a = {acc[mi][ni][8 * hf + 2 * r], acc[mi][ni][8 * hf + 2 * r + 1]};
              const f2v x = a * (a * a * k2 + k1);
              const f2v e = {__builtin_amdgcn_exp2f(x.x), __builtin_amdgcn_exp2f(x.y)};
              const f2v dd = e * ir + ir;
              const f2v rr = {__builtin_amdgcn_rcpf(dd.x), __builtin_amdgcn_rcpf(dd.y)};
              const f2v gl = a * rr;
              o[r] = pkbf(gl.x, gl.y);
            }
            __builtin_amdgcn_raw_buffer_store_b128((v4u){o[0], o[1], o[2], o[3]}, ws, HADDR, 0, SC1);
            SCHED();
          }
        }
        if (mi == 0) signal(E_GELU, lane);
      }
    } else {
      signal(E_GELU, lane);
    }
    signal(E_GELU, lane);
    STAMP(4); STAMPC(13);
    publish(ws, C_H, 4, WS_F2 + (rp * 16 + cp) * 128, tag, lane);
    STAMP(5);
  }

  if (role == 0) {
    const unsigned tag = lds_tag();
    const int mh = w & 1, kh = w >> 1, g = lane >> 4;
    const int tok = 64 * tt + 32 * mh + 16 * kh + (lane & 15);
    poll_words(ws, WS_F2 + ((tt >> 1) * 16 + min(lane, 13)) * 128, tag);
    HOLD(3);
    PRIO(2);
    constexpr int W = 6;
    v4u H[W][2], A[2][5];
    f4v acc[2][5] = {};
    const unsigned hs = WS_H + (4 * tt + 2 * mh) * 55296 + 27 * kh * 1024;
    const unsigned hv[2] = {lane * 16 + kh * 55296u, lane * 16 + (kh ^ 1) * 55296u};
#pragma unroll
    for (int k = 0; k < W; ++k)
#pragma unroll
      for (int ti = 0; ti < 2; ++ti) H[k][ti] = __builtin_amdgcn_raw_buffer_load_b128(ws, hv[ti], hs + k * 1024, SC1);
    SCHED();
    v2u xv[5], bv[5];
    unsigned ab[3];
#pragma unroll
    for (int i = 0; i < 3; ++i) { ab[i] = kh * 15360 + lane * 16 + 2 * i * RING2; asm volatile("" : "+v"(ab[i])); }
    auto rd = [&](int k, int n) { A[k & 1][n] = lds128(ab[((k / 3) % 5) >> 1] + (((k / 3) % 5) & 1) * RING2 + (k % 3) * 5120 + n * 1024); };
    unsigned fl = 0, cbase = CNT, sbase = sig_base(lane), one = 1u;
    asm volatile("" : "+v"(cbase), "+v"(sbase), "+v"(one));
    await<1>(E_FULL + 10 * 4, 4);
#pragma unroll
    for (int n = 0; n < 5; ++n) rd(0, n);
    STAMP(7); STAMPC(14);
    SCHED();
    static_for<27>([&](auto kc) {
      constexpr int k = kc;
#pragma unroll
      for (int ti = 0; ti < 2; ++ti) {
#pragma unroll
        for (int n = 0; n < 5; ++n) {
          acc[ti][n] = mfma16b(A[k & 1][n], H[k % W][ti], acc[ti][n]);
          SCHED();
          if (ti == 0 && k < 26) {
            rd(k + 1, n);
            if (n == 4 && k % 3 == 1) signal_at<E_DONE - CNT + (10 + k / 3) * 4>(sbase, one);
            SCHED();
          }
          if (ti == 1 && n == 0 && k % 3 == 1 && k < 24) { fl = *(volatile LAS unsigned*)lp(cbase + (E_FULL - CNT) + (11 + k / 3) * 4); SCHED(); }
        }
        if (k + W < 27) H[k % W][ti] = __builtin_amdgcn_raw_buffer_load_b128(ws, hv[ti], hs + (k + W) * 1024, SC1);
        if constexpr (k >= 21 && k < 26) {
          if (ti == 0) xv[k - 21] = __builtin_amdgcn_raw_buffer_load_b64(xr, tok * 1280 + 8 * g + 32 * (k - 21), 160 * ct, 0);
          else bv[k - 21] = __builtin_amdgcn_raw_buffer_load_b64(make_rsrc(p.b2, D_ * 2), 8 * g + 32 * (k - 21), 160 * ct, 0);
        }
        SCHED();
      }
      if (k % 3 == 1 && k < 24) { check(fl, E_FULL + (11 + k / 3) * 4, 4); STAMPL(22 + k / 3); SCHED(); }
    });
    HOLD(4);
    await<1>(E_DONE + 14 * 4, 4);
#pragma unroll
    for (int n = 0; n < 5; ++n) sts128(RED2 + w * 5120 + n * 1024 + lane * 16, __builtin_bit_cast(v4u, acc[1][n]));
    signal(E_RED + w * 4, lane);
    await<1>(E_SRDY, 2);
    await<1>(E_RED + (w ^ 2) * 4, 1);
    STAMP(9);
    const unsigned sa = STILE + (4 * g + ((lane & 15) >> 2)) * 160 + (32 * mh + 16 * kh + 4 * (lane & 3)) * 2;
#pragma unroll
    for (int n = 0; n < 5; ++n) {
      const f4v o = __builtin_bit_cast(f4v, lds128(RED2 + (w ^ 2) * 5120 + n * 1024 + lane * 16));
      const v2u sv = ldstr(sa + n * 2560);
      const float bz[4] = {blo(bv[n].x), bhi(bv[n].x), blo(bv[n].y), bhi(bv[n].y)};
      const float sz[4] = {blo(sv.x), bhi(sv.x), blo(sv.y), bhi(sv.y)};
      const float xz[4] = {blo(xv[n].x), bhi(xv[n].x), blo(xv[n].y), bhi(xv[n].y)};
      float r[4];
#pragma unroll
      for (int i = 0; i < 4; ++i) {
        const float z = acc[0][n][i] + o[i] + bz[i];
        const float gt = z * __builtin_amdgcn_rcpf(1.f + __builtin_amdgcn_exp2f(z * -1.44269504088896f));
        r[i] = fmaf(sz[i], gt, xz[i]);
      }
      *(GAS v2u*)(p.out + tok * 640 + 80 * ct + 16 * n + 4 * g) = (v2u){pkbf(r[0], r[1]), pkbf(r[2], r[3])};
    }

    asm volatile(".Lfused_end_b:");
  } else {
    if (gemm_wg) await<4>(E_GELU, FWPOLL_AT);
    if (w == 0) { poll_words4(ws, WS_FW + lane * 128, lds_tag()); signal(E_FWOK, lane); } else { await<2>(E_FWOK, 1); }
    if (gemm_wg) await<2>(E_GELU, 8);
    STAMP(5);
    PRIO(3);
#pragma unroll 1
    for (int c = 0; c < 3; ++c) w2_chunk<PACE0>(wss, ct, w, c, lane);
    WAITV(16); signal(E_FULL + 10 * 4, lane);
    WAITV(8); signal(E_FULL + 11 * 4, lane);
    WAITV(0); signal(E_FULL + 12 * 4, lane);
    STAMP(6);
    await<4>(E_CONV, 4);
    STAMP(7);
    w2_chunk<0>(wss, ct, w, 3, lane);
    w2_chunk<0>(wss, ct, w, 4, lane);
    WAITV(8);
    signal(E_FULL + 13 * 4, lane);
#pragma unroll 1
    for (int c = 5; c < 9; ++c) {
      await(E_DONE + (5 + c) * 4, 4);
      w2_chunk<0>(wss, ct, w, c, lane);
      WAITV(8);
      signal(E_FULL + (9 + c) * 4, lane);
      STAMP(19 + c);
    }
    WAITV(0);
    signal(E_FULL + 18 * 4, lane);
    STAMP(10);
  }
  STAMP_FLUSH();
}

__global__ void t_mark(unsigned long long* p) { if (threadIdx.x == 0) *p = __builtin_amdgcn_s_memrealtime(); }
__global__ void t_hold(const unsigned long long* p) {
  const unsigned long long t0 = *p;
  for (int i = 0; i < 200000 && __builtin_amdgcn_s_memrealtime() - t0 < 6000ull; ++i) __builtin_amdgcn_s_sleep(1);
}
}

extern "C" void kernel_launch(void* const* d_in, const int* in_sizes, int n_in, void* d_out, int out_size, void* d_ws, size_t ws_size,
                              hipStream_t stream) {
  Params p;
  p.x = (const unsigned short*)d_in[0];
  p.lA = (const unsigned short*)d_in[1];
  p.Ai = (const unsigned short*)d_in[2];
  p.Cr = (const unsigned short*)d_in[3];
  p.Ci = (const unsigned short*)d_in[4];
  p.ldt = (const unsigned short*)d_in[5];
  p.Dsk = (const unsigned short*)d_in[6];
  p.w1 = (const unsigned short*)d_in[8];
  p.b1 = (const unsigned short*)d_in[9];
  p.w2 = (const unsigned short*)d_in[10];
  p.b2 = (const unsigned short*)d_in[11];
  p.out = (unsigned short*)d_out;
  p.ws = (unsigned char*)d_ws;
  (void)hipFuncSetAttribute((const void*)fused_kernel, hipFuncAttributeMaxDynamicSharedMemorySize, LDS_BYTES);
  t_mark<<<1, 64, 0, stream>>>((unsigned long long*)((char*)d_ws + (20u << 20)));
  fused_kernel<<<256, 768, LDS_BYTES, stream>>>(p);
}
```

```cpp
#include <hip/hip_runtime.h>
#include <hip/hip_bf16.h>

namespace {

typedef unsigned v4u __attribute__((ext_vector_type(4)));
typedef unsigned v2u __attribute__((ext_vector_type(2)));
typedef short s4v __attribute__((ext_vector_type(4)));
typedef float f2v __attribute__((ext_vector_type(2)));
typedef float f4v __attribute__((ext_vector_type(4)));
typedef float f16v __attribute__((ext_vector_type(16)));
typedef _Float16 h2v __attribute__((ext_vector_type(2)));
typedef _Float16 h8v __attribute__((ext_vector_type(8)));
typedef __bf16 b2v __attribute__((ext_vector_type(2)));
typedef __bf16 b8v __attribute__((ext_vector_type(8)));
#define GAS __attribute__((address_space(1)))
#define LAS __attribute__((address_space(3)))
#define DEV __device__ __forceinline__
typedef __amdgpu_buffer_rsrc_t rsrc_t;

constexpr int L_ = 2048, D_ = 640, F_ = 1728;
constexpr unsigned X_BYTES = L_ * D_ * 2, W_BYTES = D_ * F_ * 2;
constexpr int SC1 = 16;
constexpr unsigned SPIN_CAP = 1u << 16;

constexpr unsigned WS_EPOCH = 0;
constexpr unsigned WS_F1 = 4096;
constexpr unsigned WS_F2 = 65536;
constexpr unsigned WS_F3 = 16384;
constexpr unsigned WS_STAT = 32768;
constexpr unsigned WS_XNT = 131072;
constexpr unsigned WS_W2T = 2752512;
constexpr unsigned WS_H = 5242880;
constexpr unsigned WS_ST = 12582912;
constexpr unsigned WS_STAMP = 15204352;
constexpr unsigned WS_BYTES = WS_STAMP + 256 * 12 * 64 * 8;

constexpr unsigned LDS_BYTES = 163840;
constexpr unsigned UNIT = 16384;
constexpr unsigned KSCR = 8704;
constexpr unsigned FPOS = 2576;
constexpr unsigned GRP = 7 * UNIT;
constexpr unsigned LNST = GRP + 4 * KSCR;
constexpr unsigned CNT = LNST + 10752;
constexpr unsigned SEXC = CNT + 512;
constexpr unsigned JUNK = CNT + 1024;
constexpr unsigned RSTG = 6 * UNIT;
constexpr unsigned UCP = 2368;
constexpr unsigned CH_R3 = 4 * UCP, CH_U = 9856, CH_BYTES = 2 * KSCR;
constexpr unsigned RING2 = 30720;
constexpr unsigned STILE = 4 * RING2, RED2 = STILE + 12800;
constexpr unsigned E_FULL = CNT, E_DONE = CNT + 80;
constexpr unsigned E_GELU = CNT + 160, E_LN = CNT + 164, E_LNPUB = CNT + 168, E_SRDY = CNT + 172, E_RED = CNT + 176, E_SX = CNT + 192;
constexpr unsigned C_H = CNT + 208, C_F1 = CNT + 212, E_F1OK = CNT + 216, E_CONV = CNT + 220, E_VMQ = CNT + 76, E_RT = CNT + 364;
constexpr unsigned C_GRP = CNT + 224;
constexpr unsigned W_TAG = CNT + 368;
constexpr int N_ZERO = 92;

struct Params {
  const unsigned short *x, *lA, *Ai, *Cr, *Ci, *ldt, *Dsk, *w1, *b1, *w2, *b2;
  unsigned short* out;
  unsigned char* ws;
};

#define STAMP(i) do { } while (0)
#define STAMPC(i) do { } while (0)
#define STAMPV(i, v) do { } while (0)
#define STAMP_INIT() do { } while (0)
#define STAMP_FLUSH() do { } while (0)

#define PRIO(x) __builtin_amdgcn_s_setprio(x)
#define PACE0 0
#define APRIO 0
#define LT0 t0
#define LF0 f0
#undef TACC
#define TACC(acc, stmt) do { stmt; } while (0)
#define STAMPL(i) do { } while (0)
#define CHK(e, a, n, t) check(e, a, n)
#define HADDR (hv + hs + hf * 512)
#define ROTV 0
#define F2_SLEEP 2
#define OUT_AUX 0
#define SLA 1
#define SLL 2
#define W2_AT (E_DONE + 36)
#define BAR() asm volatile("s_waitcnt lgkmcnt(0)\n\ts_barrier" ::: "memory")
#define WAITV(n) asm volatile("s_waitcnt vmcnt(" #n ")" ::: "memory")
#define SCHED() __builtin_amdgcn_sched_barrier(0)

template <int I> struct IC { constexpr operator int() const { return I; } };
template <int N, int I = 0, class F> DEV void static_for(F&& f) {
  if constexpr (I < N) { f(IC<I>{}); static_for<N, I + 1>(f); }
}
DEV float u2f(unsigned u) { return __builtin_bit_cast(float, u); }
DEV unsigned f2u(float f) { return __builtin_bit_cast(unsigned, f); }
DEV h2v u2h(unsigned u) { return __builtin_bit_cast(h2v, u); }
DEV float blo(unsigned u) { return u2f(u << 16); }
DEV float bhi(unsigned u) { return u2f(u & 0xffff0000u); }
DEV unsigned pkbf(float a, float b) { return __builtin_bit_cast(unsigned, __builtin_convertvector((f2v){a, b}, b2v)); }
DEV unsigned pkh(float a, float b) { return __builtin_bit_cast(unsigned, __builtin_convertvector((f2v){a, b}, h2v)); }
DEV float opq(float x) { asm("" : "+v"(x)); return x; }
DEV float bfr(float a) { return blo(pkbf(a, a)); }
DEV f16v mfma32b(v4u a, v4u b, f16v c) { return __builtin_amdgcn_mfma_f32_32x32x16_bf16(__builtin_bit_cast(b8v, a), __builtin_bit_cast(b8v, b), c, 0, 0, 0); }
DEV f16v mfma32h(v4u a, v4u b, f16v c) { return __builtin_amdgcn_mfma_f32_32x32x16_f16(__builtin_bit_cast(h8v, a), __builtin_bit_cast(h8v, b), c, 0, 0, 0); }
DEV f4v mfma16b(v4u a, v4u b, f4v c) { return __builtin_amdgcn_mfma_f32_16x16x32_bf16(__builtin_bit_cast(b8v, a), __builtin_bit_cast(b8v, b), c, 0, 0, 0); }
DEV f4v mfma16h(v4u a, v4u b, f4v c) { return __builtin_amdgcn_mfma_f32_16x16x32_f16(__builtin_bit_cast(h8v, a), __builtin_bit_cast(h8v, b), c, 0, 0, 0); }

DEV LAS unsigned char* lp(unsigned a) { return (LAS unsigned char*)(size_t)a; }
DEV v4u lds128(unsigned a) { return *(LAS v4u*)lp(a); }
DEV v2u lds64(unsigned a) { return *(LAS v2u*)lp(a); }
DEV unsigned lds32(unsigned a) { return *(LAS unsigned*)lp(a); }
DEV void sts128(unsigned a, v4u v) { *(LAS v4u*)lp(a) = v; }
DEV void sts64(unsigned a, v2u v) { *(LAS v2u*)lp(a) = v; }
DEV void sts32(unsigned a, unsigned v) { *(LAS unsigned*)lp(a) = v; }
DEV v2u ldstr(unsigned a) { return __builtin_bit_cast(v2u, __builtin_amdgcn_ds_read_tr16_b64_v4i16((LAS s4v*)lp(a))); }
DEV v4u ldstr2(unsigned a0, unsigned a1) { const v2u x = ldstr(a0), y = ldstr(a1); return (v4u){x.x, x.y, y.x, y.y}; }

DEV v4u make_srd(const void* ptr, unsigned bytes) {
  return (v4u){(unsigned)(size_t)ptr, (unsigned)((size_t)ptr >> 32) & 0xffffu, bytes, 0x00020000u};
}
DEV rsrc_t make_rsrc(const void* ptr, unsigned bytes) { return __builtin_amdgcn_make_buffer_rsrc((void*)ptr, 0, bytes, 0x00020000); }
#define DMA_HEAD "s_mov_b32 m0, %0\n\ts_nop 0\n\t"
DEV void dma1(v4u srd, unsigned v, unsigned soff, unsigned lds) {
  asm volatile(DMA_HEAD "buffer_load_dwordx4 %1, %2, %3 offen lds" ::"s"(lds), "v"(v), "s"(srd), "s"(soff) : "memory");
}
template <int OFF> DEV void dma_next(v4u srd, unsigned v, unsigned soff) {
  asm volatile("buffer_load_dwordx4 %0, %1, %2 offen offset:%3 lds" ::"v"(v), "s"(srd), "s"(soff), "i"(OFF) : "memory");
}
DEV void dma4(v4u srd, const unsigned (&v)[4], unsigned soff, unsigned lds) {
  asm volatile(DMA_HEAD "buffer_load_dwordx4 %1, %5, %6 offen lds\n\tbuffer_load_dwordx4 %2, %5, %6 offen offset:1024 lds\n\t"
               "buffer_load_dwordx4 %3, %5, %6 offen offset:2048 lds\n\tbuffer_load_dwordx4 %4, %5, %6 offen offset:3072 lds"
               ::"s"(lds), "v"(v[0]), "v"(v[1]), "v"(v[2]), "v"(v[3]), "s"(srd), "s"(soff) : "memory");
}
DEV void dma4_lin_sc1(v4u srd, unsigned v, unsigned soff, unsigned lds) {
  asm volatile(DMA_HEAD "buffer_load_dwordx4 %1, %2, %3 offen sc1 lds\n\tbuffer_load_dwordx4 %1, %2, %3 offen offset:1024 sc1 lds\n\t"
               "buffer_load_dwordx4 %1, %2, %3 offen offset:2048 sc1 lds\n\tbuffer_load_dwordx4 %1, %2, %3 offen offset:3072 sc1 lds"
               ::"s"(lds), "v"(v), "s"(srd), "s"(soff) : "memory");
}

DEV void poll_words(rsrc_t ws, unsigned off, unsigned tag) {
#pragma unroll 1
  for (unsigned s = 0; s < SPIN_CAP; ++s) {
    asm volatile("" ::: "memory");
    if (__all(__builtin_amdgcn_raw_buffer_load_b32(ws, off, 0, SC1) == tag)) break;
    __builtin_amdgcn_s_sleep(F2_SLEEP);
  }
  asm volatile("" ::: "memory");
}
DEV void poll_words4(rsrc_t ws, unsigned off, unsigned tag) {
#pragma unroll 1
  for (unsigned s = 0; s < SPIN_CAP; ++s) {
    asm volatile("" ::: "memory");
    const v4u v = __builtin_amdgcn_raw_buffer_load_b128(ws, off, 0, SC1);
    if (__all(v.x == tag && v.y == tag && v.z == tag && v.w == tag)) break;
    __builtin_amdgcn_s_sleep(8);
  }
  asm volatile("" ::: "memory");
}
DEV unsigned sig_base(int lane) { return lane == 0 ? CNT : JUNK + lane * 4; }
template <int OFF> DEV void signal_at(unsigned base, unsigned one) {
  asm volatile("ds_add_u32 %0, %1 offset:%2" ::"v"(base), "v"(one), "i"(OFF) : "memory");
}
DEV void signal(unsigned a, int lane) {
  asm volatile("ds_add_u32 %0, %1" ::"v"(sig_base(lane) + (a - CNT)), "v"(1u) : "memory");
}
DEV unsigned peek(unsigned a) { return *(volatile LAS unsigned*)lp(a); }
template <int SLEEP = 1> DEV void await(unsigned a, unsigned need) {
#pragma unroll 1
  for (unsigned s = 0; s < (SPIN_CAP << 2); ++s) {
    asm volatile("" : "+s"(s));
    if (__builtin_amdgcn_readfirstlane(peek(a)) >= need) break;
    __builtin_amdgcn_s_sleep(SLEEP);
  }
  asm volatile("" ::: "memory");
}
DEV void check(unsigned early, unsigned a, unsigned need) {
  if (__builtin_expect(__builtin_amdgcn_readfirstlane(early) < need, 0)) await<1>(a, need);
  asm volatile("" ::: "memory");
}
DEV unsigned lds_arrive(unsigned cnt, int lane) {
  unsigned old;
  asm volatile("ds_add_rtn_u32 %0, %1, %2\n\ts_waitcnt lgkmcnt(0)" : "=&v"(old) : "v"(sig_base(lane) + (cnt - CNT)), "v"(1u) : "memory");
  return __builtin_amdgcn_readfirstlane(old);
}
template <int SLEEP = 1> DEV void group_sync(unsigned cnt, unsigned target, int lane) {
  if (lds_arrive(cnt, lane) + 1 < target) await<SLEEP>(cnt, target);
}
DEV void publish(rsrc_t ws, unsigned cnt, unsigned nwaves, unsigned flag_off, unsigned tag, int lane) {
  WAITV(0);
  if (lds_arrive(cnt, lane) + 1 == nwaves && lane == 0) __builtin_amdgcn_raw_buffer_store_b32(tag, ws, flag_off, 0, SC1);
}
DEV unsigned epoch_load(rsrc_t ws) { return __builtin_amdgcn_raw_buffer_load_b32(ws, WS_EPOCH, 0, SC1); }
DEV unsigned epoch_tag(unsigned ev, bool share, int lane) {
  const unsigned e = __builtin_amdgcn_readfirstlane(ev);
  const unsigned tag = e * 0x9E3779B1u + 0x7F4A7C15u;
  if (share && lane == 0) sts64(W_TAG, (v2u){tag, e});
  return tag;
}
DEV unsigned lds_tag() { return __builtin_amdgcn_readfirstlane(lds32(W_TAG)); }

template <int CTRL> DEV float dpp_add(float v) {
  return v + u2f(__builtin_amdgcn_update_dpp(0u, f2u(v), CTRL, 0xf, 0xf, false));
}
template <int CTRL> DEV float dpp_max(float v) {
  return fmaxf(v, u2f(__builtin_amdgcn_update_dpp(0u, f2u(v), CTRL, 0xf, 0xf, false)));
}
DEV float half_sum(float v) {
  v = dpp_add<0x128>(v); v = dpp_add<0x124>(v); v = dpp_add<0x122>(v); v = dpp_add<0x121>(v);
  const auto r = __builtin_amdgcn_permlane16_swap(f2u(v), f2u(v), false, false);
  return u2f(r[0]) + u2f(r[1]);
}
DEV float wave_max(float v) {
  v = dpp_max<0x128>(v); v = dpp_max<0x124>(v); v = dpp_max<0x122>(v); v = dpp_max<0x121>(v);
  const auto r = __builtin_amdgcn_permlane16_swap(f2u(v), f2u(v), false, false);
  v = fmaxf(u2f(r[0]), u2f(r[1]));
  const auto q = __builtin_amdgcn_permlane32_swap(f2u(v), f2u(v), false, false);
  return fmaxf(u2f(q[0]), u2f(q[1]));
}

struct LnIn { v2u v[5]; };
DEV LnIn ln_load(rsrc_t xr, int token, int lane) {
  LnIn r;
#pragma unroll
  for (int m = 0; m < 5; ++m) r.v[m] = __builtin_amdgcn_raw_buffer_load_b64(xr, token * 1280 + (lane & 31) * 8 + m * 256, 0, 0);
  return r;
}
DEV void ln_compute(const LnIn& in, rsrc_t ws, int token, int row, unsigned tag, int lane) {
  float xv[20], s = 0.f, q = 0.f;
#pragma unroll
  for (int m = 0; m < 5; ++m) { xv[4 * m] = blo(in.v[m].x); xv[4 * m + 1] = bhi(in.v[m].x); xv[4 * m + 2] = blo(in.v[m].y); xv[4 * m + 3] = bhi(in.v[m].y); }
#pragma unroll
  for (int m = 0; m < 20; ++m) { s = opq(s + xv[m]); q = opq(fmaf(xv[m], xv[m], q)); }
  s = half_sum(s); q = half_sum(q);
  const float mean = s * (1.f / 640.f);
  const float var = fmaxf(fmaf(-mean, mean, q * (1.f / 640.f)), 0.f);
  const float m1 = bfr(mean), s1 = bfr(__builtin_amdgcn_sqrtf(bfr(bfr(var) + 1e-5f)));
  const float r1 = __builtin_amdgcn_rcpf(s1);
  const float m2 = bfr((mean - m1) * r1), s2 = bfr(__builtin_amdgcn_sqrtf(bfr(bfr(var * r1 * r1) + 1e-5f)));
  if ((lane & 31) == 0)
    __builtin_amdgcn_raw_buffer_store_b128((v4u){f2u(fmaf(m2, s1, m1)), tag, f2u(s1 * s2), tag}, ws, WS_STAT + token * 16, 0, SC1);
  const float c = -m1 * r1;
#pragma unroll
  for (int m = 0; m < 5; ++m) {
    const v2u o = {pkh(opq(fmaf(xv[4 * m], r1, c)), opq(fmaf(xv[4 * m + 1], r1, c))), pkh(opq(fmaf(xv[4 * m + 2], r1, c)), opq(fmaf(xv[4 * m + 3], r1, c)))};
    sts64(LNST + row * 1344 + (lane & 31) * 8 + m * 256, o);
  }
}
DEV void ln_publish(rsrc_t ws, int tb, int aw, int lane) {
  const unsigned a = LNST + ((lane & 15) >> 2) * 1344 + (lane >> 4) * 32 + (lane & 3) * 8 + aw * 640;
  v4u v[5];
#pragma unroll
  for (int i = 0; i < 5; ++i) v[i] = ldstr2(a + i * 128, a + i * 128 + 4 * 1344);
  signal(E_LNPUB, lane);
#pragma unroll
  for (int i = 0; i < 5; ++i) {
    __builtin_amdgcn_raw_buffer_store_b128(v[i], ws, WS_XNT + tb * 10240 + (aw * 320 + i * 64 + lane) * 16, 0, SC1);
  }
}

struct Cx { float r, i; };
DEV Cx cmul(Cx a, Cx b) { return {opq(fmaf(a.r, b.r, -a.i * b.i)), opq(fmaf(a.r, b.i, a.i * b.r))}; }
DEV Cx csqr(Cx a) { return {opq(fmaf(a.r, a.r, -a.i * a.i)), opq((a.r + a.r) * a.i)}; }
DEV unsigned ch(Cx a) { return pkh(a.r, a.i); }
DEV unsigned hmul(unsigned a, unsigned b) {
  unsigned t, r;
  asm("v_pk_mul_f16 %0, %1, %2 op_sel:[0,0] op_sel_hi:[0,1]" : "=v"(t) : "v"(a), "v"(b));
  asm("v_pk_fma_f16 %0, %1, %2, %3 op_sel:[1,1,0] op_sel_hi:[1,0,1] neg_lo:[0,1,0]" : "=v"(r) : "v"(a), "v"(b), "v"(t));
  return r;
}
DEV float exp_bf(float x) {
  const float t = x * 1.44268798828125f, lo = x * 7.0526077138765e-06f;
  const float n = __builtin_rintf(t);
  return bfr(__builtin_amdgcn_ldexpf(__builtin_amdgcn_exp2f((t - n) + lo), (int)fminf(fmaxf(n, -200.f), 200.f)));
}
DEV Cx cexp_at(float lg, float rh, float rl, float P) {
  const float m = __builtin_amdgcn_exp2f(lg * P), ph = fmaf(rl, P, __builtin_amdgcn_fractf(rh * P));
  return {opq(m * __builtin_amdgcn_cosf(ph)), opq(m * __builtin_amdgcn_sinf(ph))};
}
struct KgIn { unsigned lA, Ai, Cr, Ci; float dt, dsk; };
DEV KgIn kg_load(const Params& p, int d, int lane) {
  KgIn k;
  k.lA = ((const GAS unsigned*)p.lA)[d * 64 + lane]; k.Ai = ((const GAS unsigned*)p.Ai)[d * 64 + lane];
  k.Cr = ((const GAS unsigned*)p.Cr)[d * 64 + lane]; k.Ci = ((const GAS unsigned*)p.Ci)[d * 64 + lane];
  k.dt = u2f((unsigned)((const GAS unsigned short*)p.ldt)[d] << 16);
  k.dsk = u2f((unsigned)((const GAS unsigned short*)p.Dsk)[d] << 16);
  return k;
}
DEV float kg_wave(const KgIn& k, int g, unsigned scr, unsigned fp, unsigned fp_free, int lane) {
  const float dt = exp_bf(k.dt), c2 = dt + dt;
  const float mx = wave_max(fmaxf(fmaxf(fabsf(blo(k.Cr)), fabsf(bhi(k.Cr))), fmaxf(fabsf(blo(k.Ci)), fabsf(bhi(k.Ci))))) * c2;
  int ex = __builtin_amdgcn_frexp_expf(mx);
  ex = ex < -120 ? -120 : (ex > 120 ? 120 : ex);
  const float cs = c2 * __builtin_amdgcn_ldexpf(1.f, -ex);
  const int sh = g ? 0 : 16;
  const float lg = -(exp_bf(bhi(k.lA << sh)) * dt) * 1.44269504088896f, ai = bhi(k.Ai << sh) * dt;
  const Cx c0 = {opq(bhi(k.Cr << sh) * cs), opq(-bhi(k.Ci << sh) * cs)};
  const float rh = ai * 0.15915494f, rl = fmaf(ai, 0.15915494f, -rh) + ai * 6.4206382e-09f;
  const Cx v1 = cexp_at(lg, -rh, -rl, 1.f), w1 = cexp_at(lg, rh, rl, 8.f), G = cexp_at(lg, -rh, -rl, 256.f);
  const unsigned wa = scr + lane * 4, ra = scr + (lane & 31) * 272 + (lane >> 5) * 16;
  {
    const Cx w2 = csqr(w1), w3 = cmul(w2, w1), w4 = csqr(w2), w8 = csqr(w4), w12 = cmul(w8, w4), w16 = csqr(w8);
    const Cx w20 = cmul(w16, w4), w24 = csqr(w12), w28 = cmul(w24, w4);
    const unsigned o[3] = {ch(w1), ch(w2), ch(w3)};
    const unsigned bs[7] = {ch(w4), ch(w8), ch(w12), ch(w16), ch(w20), ch(w24), ch(w28)};
    sts32(wa, 0x00003c00u);
#pragma unroll
    for (int j = 0; j < 3; ++j) sts32(wa + (1 + j) * 272, o[j]);
#pragma unroll
    for (int b = 0; b < 7; ++b) {
      sts32(wa + (4 + 4 * b) * 272, bs[b]);
#pragma unroll
      for (int j = 0; j < 3; ++j) sts32(wa + (5 + 4 * b + j) * 272, hmul(bs[b], o[j]));
    }
  }
  asm volatile("" ::: "memory");
  v4u y[8];
#pragma unroll
  for (int s = 0; s < 8; ++s) y[s] = lds128(ra + s * 32);
  asm volatile("" ::: "memory");
  {
    const Cx v2 = csqr(v1), v3 = cmul(v2, v1), v4 = csqr(v2), v6 = csqr(v3), x4 = cmul(c0, v4);
    const unsigned h1 = ch(v1), h2 = ch(v2), h3 = ch(v3), h4 = ch(v4), h6 = ch(v6), hx0 = ch(c0), hx4 = ch(x4);
    sts32(wa + 0 * 272, hx0); sts32(wa + 1 * 272, hmul(hx0, h1)); sts32(wa + 2 * 272, hmul(hx0, h2)); sts32(wa + 3 * 272, hmul(hx0, h3));
    sts32(wa + 4 * 272, hx4); sts32(wa + 5 * 272, hmul(hx4, h1)); sts32(wa + 6 * 272, hmul(hx4, h2)); sts32(wa + 7 * 272, hmul(hx4, h3));
    const Cx d1 = cmul(c0, G), d2 = cmul(d1, G), d3 = cmul(d2, G), d4 = cmul(d3, G);
    const Cx G4 = csqr(csqr(G));
    const Cx d5 = cmul(d1, G4), d6 = cmul(d2, G4), d7 = cmul(d3, G4), d8 = cmul(d4, G4);
    const unsigned e1 = ch(d1), e2 = ch(d2), e3 = ch(d3);
    sts32(wa + 8 * 272, e1); sts32(wa + 9 * 272, hmul(e1, h2)); sts32(wa + 10 * 272, hmul(e1, h4)); sts32(wa + 11 * 272, hmul(e1, h6));
    sts32(wa + 12 * 272, e2); sts32(wa + 13 * 272, hmul(e2, h4)); sts32(wa + 14 * 272, e3); sts32(wa + 15 * 272, hmul(e3, h4));
    sts32(wa + 16 * 272, ch(d4)); sts32(wa + 17 * 272, ch(d5)); sts32(wa + 18 * 272, ch(d6)); sts32(wa + 19 * 272, ch(d7));
    sts32(wa + 20 * 272, ch(d8));
  }
  asm volatile("" ::: "memory");
  v4u xx[8];
#pragma unroll
  for (int s = 0; s < 8; ++s) xx[s] = lds128(ra + s * 32);
  SCHED();
  f16v acc = {};
#pragma unroll
  for (int s = 0; s < 8; ++s) acc = mfma32h(xx[s], y[s], acc);
  const int j = lane & 31, h = lane >> 5;
  await(E_LNPUB, fp_free);
  sts128(fp + (8 * j + 4 * h) * 4, (v4u){f2u(acc[0]), f2u(acc[1]), f2u(acc[2]), f2u(acc[3])});
  sts64(fp + (h ? 384 + 2 * j : 256 + 4 * j) * 4, (v2u){f2u(acc[4]), f2u(acc[5])});
  sts64(fp + (h ? 448 + 2 * j : 258 + 4 * j) * 4, (v2u){f2u(acc[6]), f2u(acc[7])});
  if (lane <= 32) sts32(fp + (512 + 128 * h + j) * 4, f2u(acc[8]));
  if (h == 0) {
#pragma unroll
    for (int i = 1; i < 4; ++i) sts32(fp + (512 + j + 32 * i) * 4, f2u(acc[8 + i]));
  }
  return __builtin_amdgcn_ldexpf(1.f, ex);
}
DEV void kg_table0(unsigned cb, unsigned fp, int g, int lane) {
  auto f1 = [&](int i) { return opq(u2f(lds32(fp + i * 4)) + u2f(lds32(fp + FPOS + i * 4))); };
  if (g == 0) {
    const int j = lane & 31, h = lane >> 5;
    const unsigned a = fp + (h ? 256 + 4 * j : 8 * j) * 4;
    const v4u l0 = lds128(a), h0 = lds128(a + 16), l1 = lds128(a + FPOS), h1 = lds128(a + FPOS + 16);
    const float e0 = opq(u2f(l0.x) + u2f(l1.x)), e1 = opq(u2f(l0.y) + u2f(l1.y)), e2 = opq(u2f(l0.z) + u2f(l1.z)), e3 = opq(u2f(l0.w) + u2f(l1.w));
    const float e4 = opq(u2f(h0.x) + u2f(h1.x)), e5 = opq(u2f(h0.y) + u2f(h1.y)), e6 = opq(u2f(h0.z) + u2f(h1.z)), e7 = opq(u2f(h0.w) + u2f(h1.w));
    const v4u o0 = {pkh(e0, e1), pkh(e2, e3), pkh(e4, e5), pkh(e6, e7)};
    const v4u o1 = {pkh(e0, e0), pkh(e1, e2), pkh(e2, e2), pkh(e3, e4)};
    sts128(cb + 32 + lane * 16, h ? o1 : o0);
    const float fa = f1(512), fb = f1(513);
    const unsigned aa = pkh(fa, fa), bb = pkh(fb, fb);
    if (lane < 4) sts128(cb + 32 + (128 + lane) * 16, lane == 0 ? (v4u){aa, aa, pkh(fa, fb), bb} : lane == 1 ? (v4u){bb, bb, 0u, 0u} : (v4u){0u, 0u, 0u, 0u});
  } else {
    const float a = f1(384 + 2 * lane), b = f1(385 + 2 * lane), c = f1(386 + 2 * lane);
    sts128(cb + 32 + (64 + lane) * 16, (v4u){pkh(a, a), pkh(a, b), pkh(b, b), pkh(c, c)});
    const float lo = f1(640 - 2 * lane), hi = f1(lane ? 641 - 2 * lane : 640);
    sts32(cb + CH_R3 + lane * 4, pkh(lane ? hi : 0.f, lo));
    if (lane < 16) sts32(cb + CH_R3 + 256 + lane * 4, 0u);
    if (lane < 32) sts32(cb + (lane >> 3) * UCP + (lane & 7) * 4, 0u);
  }
}
DEV void kg_copies(unsigned cb, int g, int lane) {
  auto one = [&](int chunk) {
    const unsigned o = cb + 32 + chunk * 16;
    const v2u pv = lds64(o - 8);
    const v4u e = lds128(o);
    const unsigned a0 = __builtin_amdgcn_alignbit(pv.y, pv.x, 16), a1 = __builtin_amdgcn_alignbit(e.x, pv.y, 16), a2 = __builtin_amdgcn_alignbit(e.y, e.x, 16),
                   a3 = __builtin_amdgcn_alignbit(e.z, e.y, 16), a4 = __builtin_amdgcn_alignbit(e.w, e.z, 16);
    sts128(o + UCP, (v4u){a1, a2, a3, a4});
    sts128(o + 2 * UCP, (v4u){pv.y, e.x, e.y, e.z});
    sts128(o + 3 * UCP, (v4u){a0, a1, a2, a3});
  };
  one(64 * g + lane);
  if (lane < 2) one(128 + 2 * g + lane);
}
DEV void conv_load_u(rsrc_t ws, unsigned grp, int d0, int w, int lane) {
  v4u v[2];
#pragma unroll
  for (int i = 0; i < 2; ++i)
    v[i] = __builtin_amdgcn_raw_buffer_load_b128(ws, (lane >> 1) * 10240 + (lane & 1) * 16, WS_XNT + (64 * w + 32 * i) * 10240 + d0 * 16, SC1);
  if (lane < 16) sts128(grp + (w >> 1) * CH_BYTES + CH_U + (w & 1) * 256 + lane * 16, (v4u){0u, 0u, 0u, 0u});
#pragma unroll
  for (int i = 0; i < 2; ++i) sts128(grp + (lane & 1) * CH_BYTES + CH_U + 512 + (64 * w + 32 * i + (lane >> 1)) * 16, v[i]);
}
template <class F1>
DEV float ssm_prepare(rsrc_t ws, const KgIn& k, unsigned grp, unsigned fp, unsigned fp_free, unsigned cnt, int d0, int w, int lane, F1 f1) {
  const int ci = w >> 1, g = w & 1;
  const float isc = kg_wave(k, g, grp + w * KSCR, fp + w * FPOS, fp_free, lane);
  group_sync(cnt, 4, lane);
  kg_table0(grp + ci * CH_BYTES, fp + 2 * ci * FPOS, g, lane);
  group_sync(cnt + 4 + ci * 4, 2, lane);
  kg_copies(grp + ci * CH_BYTES, g, lane);
  f1();
  conv_load_u(ws, grp, d0, w, lane);
  group_sync(cnt + 12, 4, lane);
  return isc;
}

template <int B> DEV void conv_tail(f4v (&acc)[8], unsigned ua, unsigned r3, v4u w7, v4u w9) {
  const f4v b7 = mfma16h(lds128(ua + 512 * B), w7, (f4v){}), b9 = mfma16h(lds128(ua + 512 * B + 16), w9, (f4v){});
  const v4u bx = {pkh(b7[0], b9[0]), pkh(b7[1], b9[1]), pkh(b7[2], b9[2]), pkh(b7[3], b9[3])};
#pragma unroll
  for (int dl = 4; dl + B < 8; ++dl) {
    const unsigned a = r3 - 64 * dl;
    acc[dl + B] = mfma16h(bx, (v4u){lds32(a), lds32(a + 4), lds32(a + 8), lds32(a + 12)}, acc[dl + B]);
  }
}
DEV void conv_channel(rsrc_t ws, unsigned cb, unsigned cnt, int d, int eh, float isc, float dsk, int lane) {
  const int g = lane >> 4, c16 = lane & 15;
  const unsigned ga = cb + (c16 >> 2) * UCP + 64 - 16 * g + 8 * (lane & 3) + eh * 256;
  const unsigned ba = cb + CH_U + 480 + 32 * c16 + 16 * g - eh * 256;
  f4v acc[8] = {};
  v4u G[2][4], B[2][8];
#pragma unroll
  for (int i = 0; i < 8; ++i) { B[0][i] = lds128(ba + 512 * i); if (i < 4) G[0][i] = ldstr2(ga + 512 * i, ga + 512 * i - 8); }
  {
    unsigned w7[4], w9[4];
#pragma unroll
    for (int i = 0; i < 4; ++i) {
      const int k0 = 8 * g + 2 * i - c16;
      w7[i] = ((k0 >= 5 && k0 <= 11) ? 0x3c00u : 0u) | ((k0 >= 4 && k0 <= 10) ? 0x3c000000u : 0u);
      w9[i] = ((k0 >= 4 && k0 <= 12) ? 0x3c00u : 0u) | ((k0 >= 3 && k0 <= 11) ? 0x3c000000u : 0u);
    }
    const v4u W7 = {w7[0], w7[1], w7[2], w7[3]}, W9 = {w9[0], w9[1], w9[2], w9[3]};
    const unsigned ua = cb + CH_U + 448 + 32 * c16 + 16 * g, r3 = cb + CH_R3 + (127 - c16 + 4 * g) * 4;
    if (eh) {
      conv_tail<0>(acc, ua, r3, W7, W9);
      conv_tail<3>(acc, ua, r3, W7, W9);
    } else {
      conv_tail<1>(acc, ua, r3, W7, W9);
      conv_tail<2>(acc, ua, r3, W7, W9);
      const v4u g4 = ldstr2(ga + 2048, ga + 2048 - 8);
#pragma unroll
      for (int j = 4; j < 8; ++j) acc[j] = mfma16h(g4, B[0][j - 4], acc[j]);
    }
  }
  SCHED();
#pragma unroll
  for (int e = 0; e < 4; ++e) {
    int m = 0;
#pragma unroll
    for (int cc = 0; cc < 4; ++cc)
#pragma unroll
      for (int j = cc; j < 8; ++j) {
        acc[j] = mfma16h(G[e & 1][cc], B[e & 1][j - cc], acc[j]);
        if (e < 3 && (m & 1) == 0 && m < 24) {
          const int i = m >> 1;
          if (i == 1 || i > 8) { const int c = i == 1 ? 0 : i - 8; G[(e + 1) & 1][c] = ldstr2(ga + 512 * c + 64 * (e + 1), ga + 512 * c + 64 * (e + 1) - 8); }
          else { const int c = i == 0 ? 0 : i - 1; B[(e + 1) & 1][c] = lds128(ba + 512 * c - 64 * (e + 1)); }
        }
        ++m;
        SCHED();
      }
  }
  const unsigned to = 2 * (1024 * eh + 16 * c16 + 4 * g);
  v2u uu[4];
#pragma unroll
  for (int j = 0; j < 4; ++j) uu[j] = lds64(cb + CH_U + 512 + to + 512 * j);
  group_sync<1>(cnt, 2, lane);
  f4v mine[4];
  if (eh) {
#pragma unroll
    for (int j = 0; j < 4; ++j) { sts128(cb + 4096 + j * 1024 + lane * 16, __builtin_bit_cast(v4u, acc[j])); mine[j] = acc[4 + j]; }
  } else {
#pragma unroll
    for (int j = 0; j < 4; ++j) { sts128(cb + j * 1024 + lane * 16, __builtin_bit_cast(v4u, acc[4 + j])); mine[j] = acc[j]; }
  }
  group_sync<1>(cnt + 8, 2, lane);
#pragma unroll
  for (int j = 0; j < 4; ++j) {
    const f4v o = __builtin_bit_cast(f4v, lds128(cb + (eh ^ 1) * 4096 + j * 1024 + lane * 16));
    const h2v u0 = u2h(uu[j].x), u1 = u2h(uu[j].y);
    const float s0 = opq(fmaf(mine[j][0] + o[0], isc, dsk * (float)u0.x)), s1 = opq(fmaf(mine[j][1] + o[1], isc, dsk * (float)u0.y));
    const float s2 = opq(fmaf(mine[j][2] + o[2], isc, dsk * (float)u1.x)), s3 = opq(fmaf(mine[j][3] + o[3], isc, dsk * (float)u1.y));
    __builtin_amdgcn_raw_buffer_store_b64((v2u){pkbf(s0, s1), pkbf(s2, s3)}, ws, to + 512 * j, WS_ST + d * 4096, SC1);
  }
}

template <int PACE> DEV void w2_chunk(v4u wss, int ct, int lw, int c, int lane) {
  const unsigned po = (lw & 1) * 7168;
  const unsigned so = WS_W2T + (ct * 54 + 27 * (lw >> 1) + 3 * c) * 5120 + po, lb = (c % 5) * RING2 + (lw >> 1) * 15360 + po;
  dma4_lin_sc1(wss, lane * 16, so, lb);
  if (PACE) __builtin_amdgcn_s_sleep(PACE);
  dma4_lin_sc1(wss, lane * 16, so + 4096, lb + 4096);
  if (PACE) __builtin_amdgcn_s_sleep(PACE);
}
DEV void retile_load(const Params& p, int blk, unsigned stg, int lane) {
  const unsigned v0 = (lane >> 3) * 1280 + 16 * ((lane & 7) ^ (((lane >> 4) & 3) << 1));
  const unsigned v[4] = {v0, v0 + 9216, v0 + 2 * 9216, v0 + 3 * 9216};
  dma4(make_srd(p.w2, W_BYTES), v, (blk / 10) * 40960 + (blk % 10) * 128, stg);
}
struct Frag4 { v4u f[4]; };
DEV Frag4 retile_read(unsigned stg, int lane) {
  const int g = lane >> 4, pp = lane & 3, row = 16 * (g >> 1) + 4 * (g & 1) + ((lane & 15) >> 2);
  Frag4 r;
#pragma unroll
  for (int j = 0; j < 4; ++j) {
    const unsigned a = stg + row * 128 + 16 * ((2 * j + (pp >> 1)) ^ (((row >> 1) & 3) << 1)) + 8 * (pp & 1);
    r.f[j] = ldstr2(a, a + 1024);
  }
  asm volatile("s_waitcnt lgkmcnt(0)" ::: "memory");
  return r;
}
DEV void retile_write(rsrc_t ws, int blk, const Frag4& r, int lane) {
#pragma unroll
  for (int j = 0; j < 4; ++j) {
    const int ng = 4 * (blk % 10) + j;
    __builtin_amdgcn_raw_buffer_store_b128(r.f[j], ws, lane * 16 + WS_W2T + (((ng / 5) * 54 + blk / 10) * 5 + ng % 5) * 1024, 0, SC1);
  }
}

__global__ __launch_bounds__(768, 3) void fused_kernel(Params p) {
  extern __shared__ __attribute__((aligned(16))) unsigned char lds_base[];
  unsigned long long pc;
  unsigned code_bytes;
  asm volatile("s_getpc_b64 %0\n.Lfused_begin:\n\ts_mov_b32 %1, .Lfused_end_a-.Lfused_begin\n\ts_max_i32 %1, %1, .Lfused_end_b-.Lfused_begin"
               : "=s"(pc), "=s"(code_bytes) : : "scc");
  const int lane = threadIdx.x & 63, wave = __builtin_amdgcn_readfirstlane(threadIdx.x >> 6), role = wave >> 2, w = wave & 3;
  const int xcd = blockIdx.x & 7, slot = blockIdx.x >> 3, tb = 32 * xcd + slot;
  const bool gemm_wg = slot < 28;
  const int rp = 4 * (xcd >> 1) + slot / 7, cp = 7 * (xcd & 1) + slot % 7, t0 = 128 * rp, f0 = 128 * cp;
  STAMP_INIT();
  STAMP(0);
  asm volatile("" ::"v"((LAS unsigned char*)lds_base));

  const unsigned vx0 = (lane >> 3) * 1280 + 16 * ((lane & 7) ^ (lane >> 4));
  const unsigned vw0 = (lane >> 4) * 3456 + 16 * ((lane & 15) ^ ((lane >> 4) << 2));
  const unsigned vx[4] = {vx0, (vx0 ^ 64) + 9216, vx0 + 2 * 9216, (vx0 ^ 64) + 3 * 9216};
  const unsigned vw[4] = {vw0, vw0 + 12800, vw0 + 2 * 12800, vw0 + 3 * 12800};
  auto stage_x = [&](int i) { dma4(make_srd(p.x, X_BYTES), vx, (LT0 + 32 * w) * 1280 + 128 * ((i + ROTV) % 10), ((2 * i) % 7) * UNIT + w * 4096); };
  auto stage_w = [&](int i) { dma4(make_srd(p.w1, W_BYTES), vw, (64 * ((i + ROTV) % 10) + 16 * w) * 3456 + 2 * LF0, ((2 * i + 1) % 7) * UNIT + w * 4096); };
  const rsrc_t ws = make_rsrc(p.ws, WS_BYTES), xr = make_rsrc(p.x, X_BYTES);
  unsigned ev = 0;
  LnIn ln = {};
  if (role == 0) {
    if (gemm_wg) { stage_x(0); stage_w(0); }
  } else if (role == 1) {
    ln = ln_load(xr, 8 * tb + 2 * w + (lane >> 5), lane);
    ev = epoch_load(ws);
    if (w < 2) retile_load(p, 2 * tb + w, gemm_wg ? RSTG + w * 4096 : 98304 + w * 8192, lane);
  }
  if (wave >= 10) dma1(make_srd((const void*)pc, code_bytes & ~15u), lane * 16, (2 * slot + wave - 10) * 1024, JUNK);

  const v4u wss = make_srd(p.ws, WS_BYTES);
  const int dg = 80 * xcd + (gemm_wg ? 2 * slot : 56 + 6 * (slot - 28) + 2 * role);
  const int tt = 4 * xcd + (slot >> 3), ct = slot & 7;
  const unsigned fslot = (tb >> 2) * 128 + (tb & 3) * 4;
  if (threadIdx.x < N_ZERO) sts32(CNT + threadIdx.x * 4, 0u);

  if (role == 2) {
    const KgIn kin = kg_load(p, dg + (w >> 1), lane);
    BAR();
    const float isc = ssm_prepare(ws, kin, GRP, LNST, 2, C_GRP + 96, dg, w, lane, [&] { STAMP(2); await<SLA>(E_F1OK, 1); STAMP(3); });
    STAMP(6);
    await(E_GELU, 4);
    STAMP(7);
    conv_channel(ws, GRP + (w >> 1) * CH_BYTES, C_GRP + 96 + 16 + (w >> 1) * 4, dg + (w >> 1), w & 1, isc, kin.dsk, lane);
    STAMP(8);
    signal(E_CONV, lane);
    const unsigned tag = lds_tag();
    publish(ws, C_GRP + 96 + 36, 4, WS_F3 + fslot, tag, lane);
    STAMP(9);
    if (w < 2) {
      await<8>(E_DONE + 14 * 4, 4);
      poll_words4(ws, WS_F3 + (ct * 8 + (lane & 7)) * 128, tag);
      v4u sv[5];
#pragma unroll
      for (int i = 0; i < 5; ++i)
        sv[i] = __builtin_amdgcn_raw_buffer_load_b128(ws, (lane >> 3) * 4096 + (lane & 7) * 16, WS_ST + (80 * ct + 40 * w + 8 * i) * 4096 + tt * 128, SC1);
#pragma unroll
      for (int i = 0; i < 5; ++i) sts128(STILE + (40 * w + 8 * i + (lane >> 3)) * 160 + (lane & 7) * 16, sv[i]);
      signal(E_SRDY, lane);
    }
    STAMP(10);
    STAMP_FLUSH();
    asm volatile(".Lfused_end_a:");
    return;
  }

  auto poll_f1 = [&](unsigned tag) {
    poll_words4(ws, WS_F1 + lane * 128, tag);
    signal(E_F1OK, lane);
    if (blockIdx.x == 0 && lane == 0) __builtin_amdgcn_raw_buffer_store_b32(lds32(W_TAG + 4) + 1u, ws, WS_EPOCH, 0, SC1);
  };
  if (role == 1) {
    const int h = lane >> 5, fi = 4 * xcd + slot - 28;
    const bool extra = !gemm_wg && w == 0 && fi < 28;
    const unsigned stg = gemm_wg ? RSTG + w * 4096 : 98304 + w * 8192;
    PRIO(3);
    BAR();
    const unsigned tag = epoch_tag(ev, w == 0, lane);
    Frag4 rt = {};
    if (w < 2) {
      WAITV(0);
      rt = retile_read(stg, lane);
      signal(E_RT, lane);
      if (extra) retile_load(p, 512 + fi, stg, lane);
    }
    STAMP(1);
    ln_compute(ln, ws, 8 * tb + 2 * w + h, 2 * w + h, tag, lane);
    signal(E_LN, lane);
    STAMP(2);
    if (w >= 2) {
      await(E_LN, 4);
      ln_publish(ws, tb, w - 2, lane);
    } else {
      retile_write(ws, 2 * tb + w, rt, lane);
      if (extra) { WAITV(0); retile_write(ws, 512 + fi, retile_read(stg, lane), lane); }
    }
    STAMP(3);
    PRIO(0);
    publish(ws, C_F1, 4, WS_F1 + fslot, tag, lane);
    STAMP(4);
    if (gemm_wg) {
      if (w == 2) { await<SLL>(E_VMQ, 4); poll_f1(tag); } else { await<SLL>(E_F1OK, 1); }
    }
  }

  if (!gemm_wg) {
    const unsigned grp = role * (4 * KSCR + 4 * FPOS), cnt = C_GRP + role * 48;
    const KgIn kin = kg_load(p, dg + (w >> 1), lane);
    if (role == 0) BAR();
    const float isc = ssm_prepare(ws, kin, grp, grp + 4 * KSCR, 0, cnt, dg, w, lane, [&] {
      if (wave == 0) { await(E_LN, 4); poll_f1(lds_tag()); } else { await<4>(E_F1OK, 1); }
    });
    STAMP(16);
    conv_channel(ws, grp + (w >> 1) * CH_BYTES, cnt + 16 + (w >> 1) * 4, dg + (w >> 1), w & 1, isc, kin.dsk, lane);
    WAITV(0);
    group_sync(C_H, 8, lane);
    if (role == 0) signal(E_GELU, lane);
    STAMP(5);
  } else if (role == 0) {
    const int wf = w & 1, wt = w >> 1, h = lane >> 5;
    BAR();
    stage_x(1); stage_w(1); stage_x(2); stage_w(2);
    PRIO(2);
    unsigned aa[2][2], ba[2][4];
    {
      const int q = (lane & 15) >> 2, pp = lane & 3, fb = (lane >> 4) & 1;
#pragma unroll
      for (int mi = 0; mi < 2; ++mi) {
        aa[0][mi] = (8 * h + q) * 256 + 16 * ((8 * wf + 4 * (mi ^ wt) + 2 * fb + (pp >> 1)) ^ (q << 2)) + 8 * (pp & 1);
        aa[1][mi] = aa[0][mi] + 4 * UNIT;
        asm volatile("" : "+v"(aa[1][mi]));
      }
#pragma unroll
      for (int s = 0; s < 4; ++s) {
        ba[0][s] = (64 * wt + (lane & 31)) * 128 + 16 * ((2 * s + h) ^ ((lane >> 1) & 7));
        ba[1][s] = ba[0][s] + 4 * UNIT;
        asm volatile("" : "+v"(ba[1][s]));
      }
    }
    f16v acc[2][2] = {};
    v4u A[3][2], B[3][2], st[2];
    unsigned b1v[2], fl = 0, dn = 0, cbase = CNT, sbase = sig_base(lane), one = 1u, vxo = vx0 ^ 64;
    asm volatile("" : "+v"(cbase), "+v"(sbase), "+v"(one), "+v"(vxo));
    f4v cs4 = {};
    const unsigned sel1 = (((lane >> 2) ^ (lane >> 4)) & 1) ? 0u : 0x3f803f80u;
    const v4u sel = {sel1, sel1, sel1, sel1};
    auto rdA = [&](int n, int mi) {
      const int u = (2 * (n >> 2) + 1) % 7;
      const unsigned a = aa[u >> 2][mi] + (u & 3) * UNIT + (n & 3) * 4096;
      A[n % 3][mi] = ldstr2(a, a + 1024);
    };
    auto rdB = [&](int n, int ni) {
      const int u = (2 * (n >> 2)) % 7;
      B[n % 3][ni] = lds128(ba[u >> 2][n & 3] + (u & 3) * UNIT + ni * 4096);
    };
    const v4u xs = make_srd(p.x, X_BYTES), w1s = make_srd(p.w1, W_BYTES);
    unsigned sx[4], sw[4], mb = w * 4096;
#pragma unroll
    for (int j = 0; j < 4; ++j) {
      sx[j] = (LT0 + 32 * w) * 1280 + ((3 + ROTV) % 10) * 128 + j * 9216;
      sw[j] = (((3 + ROTV) % 10) * 64 + 16 * w) * 3456 + 2 * LF0 + j * 12800;
      asm volatile("" : "+s"(sx[j]), "+s"(sw[j]));
    }
    auto piece = [&](auto qc) {
      constexpr int q = qc, i = (q + 8) / 8 + 2 + ((q >> 2) & 1), j = q & 3;
      if constexpr (q >= -4 && q < 52) {
        if constexpr (q & 4) {
          if constexpr (j == 0) dma1(xs, vx0, sx[0], mb + ((2 * i) % 7) * UNIT);
          else dma_next<j * 1024>(xs, (j & 1) ? vxo : vx0, sx[j]);
          sx[j] += (i + ROTV) % 10 == 9 ? -9 * 128 : 128;
          asm volatile("" : "+s"(sx[j]));
        } else {
          if constexpr (j == 0) dma1(w1s, vw0, sw[0], mb + ((2 * i + 1) % 7) * UNIT);
          else dma_next<j * 1024>(w1s, vw0, sw[j]);
          sw[j] += (i + ROTV) % 10 == 9 ? -9 * 64 * 3456 : 64 * 3456;
          asm volatile("" : "+s"(sw[j]));
        }
      }
    };
    auto load_stats = [&] {
#pragma unroll
      for (int i = 0; i < 2; ++i) st[i] = __builtin_amdgcn_raw_buffer_load_b128(ws, (lane & 31) * 16, WS_STAT + (t0 + 64 * wt + 32 * i) * 16, SC1);
    };
    WAITV(16);
    group_sync(E_FULL, 4, lane);
    STAMP(2); STAMPC(11);
    rdA(0, 0); rdB(0, 0); rdB(0, 1); rdA(0, 1); rdA(1, 0); rdB(1, 0); rdB(1, 1); rdA(1, 1);
    SCHED();
    unsigned tag = 0, tf = 0, td = 0, tv = 0;
    v4u eb[2];
    static_for<40>([&](auto nc) {
      constexpr int n = nc, sg = n >> 2, r = n & 3;
      acc[0][0] = mfma32b(A[n % 3][0], B[n % 3][0], acc[0][0]);
      SCHED();
      if (n < 38) { rdA(n + 2, 0); rdB(n + 2, 0); }
      SCHED();
      acc[0][1] = mfma32b(A[n % 3][0], B[n % 3][1], acc[0][1]);
      SCHED();
      if constexpr (n == 2) { piece(IC<-4>{}); piece(IC<-3>{}); } else if constexpr (n > 2) piece(IC<2 * (n - 3)>{});
      if (r == 1 && sg < 9) fl = *(volatile LAS unsigned*)lp(cbase + (E_FULL - CNT) + (sg + 1) * 4);
      if (r == 2 && sg < 7) dn = *(volatile LAS unsigned*)lp(cbase + (E_DONE - CNT) + sg * 4);
      if (n == 1) dn = *(volatile LAS unsigned*)lp(cbase + (E_RT - CNT));
      if (n == 34) { dn = *(volatile LAS unsigned*)lp(cbase + (E_LN - CNT)); tag = *(volatile LAS unsigned*)lp(cbase + (W_TAG - CNT)); }
      SCHED();
      cs4 = mfma16b(sel, A[n % 3][0], cs4);
      SCHED();
      acc[1][0] = mfma32b(A[n % 3][1], B[n % 3][0], acc[1][0]);
      SCHED();
      if (n < 38) { rdB(n + 2, 1); rdA(n + 2, 1); }
      if (r == 1) signal_at<E_DONE - CNT + sg * 4>(sbase, one);
      if (n == 39) { sts32(SEXC + w * 128 + (lane & 31) * 4, f2u(cs4[0])); signal(E_SX + w * 4, lane); }
      SCHED();
      acc[1][1] = mfma32b(A[n % 3][1], B[n % 3][1], acc[1][1]);
      SCHED();
      if constexpr (n == 2) { piece(IC<-2>{}); piece(IC<-1>{}); } else if constexpr (n > 2) piece(IC<2 * (n - 3) + 1>{});
      if (r == 1 && sg < 9) { CHK(fl, E_FULL + (sg + 1) * 4, 4, tf); if (sg & 1) STAMPL(16 + (sg >> 1)); }
      if (n == 1) check(dn, E_RT, 2);
      if (r == 2 && sg < 7) CHK(dn, E_DONE + sg * 4, 4, td);
      if (r == 0 && sg < 9) {
        if (sg < 8) TACC(tv, WAITV(8)); else TACC(tv, WAITV(4));
        signal_at<E_FULL - CNT + (sg + 1) * 4>(sbase, one);
      }
      if (n == 29) load_stats();
      if (n == 30) {
#pragma unroll
        for (int i = 0; i < 2; ++i)
          b1v[i] = __builtin_amdgcn_raw_buffer_load_b16(make_rsrc(p.b1, F_ * 2), (lane & 31) * 2, (f0 + 64 * wf + 32 * (i ^ wt)) * 2, 0);
        signal_at<E_VMQ - CNT>(sbase, one);
      }
      if (n == 35) {
        if (__builtin_expect(__builtin_amdgcn_readfirstlane(dn) < 4, 0)) { await(E_LN, 4); tag = peek(W_TAG); }
        tag = __builtin_amdgcn_readfirstlane(tag);
      }
      if (n == 37) {
        if (__builtin_expect(!__all(st[0].y == tag && st[0].w == tag && st[1].y == tag && st[1].w == tag), 0)) {
#pragma unroll 1
          for (unsigned s = 0; s < SPIN_CAP; ++s) {
            asm volatile("" ::: "memory");
            load_stats();
            if (__all(st[0].y == tag && st[0].w == tag && st[1].y == tag && st[1].w == tag)) break;
          }
        }
      }
      if (n == 37 || n == 38) {
        constexpr int i = n == 38;
        const float mu = -u2f(st[i].x), ir = u2f(st[i].z), mh = bfr(mu), ih = bfr(ir);
        eb[i] = h ? (v4u){0u, 0u, 0u, 0u} : (v4u){pkbf(mh, opq(mu - mh)), pkbf(mh, 0.f), pkbf(ih, opq(ir - ih)), 0u};
        asm volatile("" : "+v"(eb[i]));
      }
      SCHED();
    });
    STAMP(3); STAMPC(12); STAMPV(29, tf); STAMPV(30, td); STAMPV(31, tv);
    auto ext = [&](int mi, float S) {
      const float sh = bfr(S);
      const v4u ea = h ? (v4u){0u, 0u, 0u, 0u} : (v4u){pkbf(sh, sh), pkbf(S - sh, 0.f), b1v[mi] | (b1v[mi] << 16), 0u};
      acc[mi][0] = mfma32b(ea, eb[0], acc[mi][0]);
      acc[mi][1] = mfma32b(ea, eb[1], acc[mi][1]);
    };
    fl = peek(E_SX + (w ^ 2) * 4);
    float so = u2f(peek(SEXC + (w ^ 2) * 128 + (lane & 31) * 4));
    ext(0, cs4[0]);
    SCHED();
    if (__builtin_expect(__builtin_amdgcn_readfirstlane(fl) < 1, 0)) {
      await(E_SX + (w ^ 2) * 4, 1);
      so = u2f(peek(SEXC + (w ^ 2) * 128 + (lane & 31) * 4));
    }
    ext(1, so);
    STAMP(1);
    if (f0 + 64 * wf < F_) {
      const unsigned hv = ((lane >> 4) & 1) * 55296 + h * 256 + (lane & 15) * 16;
#pragma unroll
      for (int mi = 0; mi < 2; ++mi) {
#pragma unroll
        for (int ni = 0; ni < 2; ++ni) {
          const float ir = u2f(st[ni].z), rho = __builtin_amdgcn_rcpf(ir);
          const float k1 = -2.302208198f * rho, k2 = k1 * 0.044715f * rho * rho;
          const unsigned hs = WS_H + ((t0 + 64 * wt + 32 * ni) / 16) * 55296 + ((f0 + 64 * wf) / 32 + (mi ^ wt)) * 1024;
#pragma unroll
          for (int hf = 0; hf < 2; ++hf) {
            unsigned o[4];
#pragma unroll
            for (int r = 0; r < 4; ++r) {
              const f2v a = {acc[mi][ni][8 * hf + 2 * r], acc[mi][ni][8 * hf + 2 * r + 1]};
              const f2v x = a * (a * a * k2 + k1);
              const f2v e = {__builtin_amdgcn_exp2f(x.x), __builtin_amdgcn_exp2f(x.y)};
              const f2v dd = e * ir + ir;
              const f2v rr = {__builtin_amdgcn_rcpf(dd.x), __builtin_amdgcn_rcpf(dd.y)};
              const f2v gl = a * rr;
              o[r] = pkbf(gl.x, gl.y);
            }
            __builtin_amdgcn_raw_buffer_store_b128((v4u){o[0], o[1], o[2], o[3]}, ws, HADDR, 0, SC1);
            SCHED();
          }
        }
      }
    }
    signal(E_GELU, lane);
    STAMP(4); STAMPC(13);
    publish(ws, C_H, 4, WS_F2 + (rp * 16 + cp) * 128, tag, lane);
    STAMP(5);
  }

  if (role == 0) {
    const unsigned tag = lds_tag();
    const int mh = w & 1, kh = w >> 1, g = lane >> 4;
    const int tok = 64 * tt + 32 * mh + 16 * kh + (lane & 15);
    PRIO(0);
    poll_words(ws, WS_F2 + ((tt >> 1) * 16 + min(lane, 13)) * 128, tag);
    STAMP(6);
    PRIO(2);
    constexpr int W = 6;
    v4u H[W][2], A[2][5];
    f4v acc[2][5] = {};
    const unsigned hs = WS_H + (4 * tt + 2 * mh) * 55296 + 27 * kh * 1024;
    const unsigned hv[2] = {lane * 16 + kh * 55296u, lane * 16 + (kh ^ 1) * 55296u};
#pragma unroll
    for (int k = 0; k < W; ++k)
#pragma unroll
      for (int ti = 0; ti < 2; ++ti) H[k][ti] = __builtin_amdgcn_raw_buffer_load_b128(ws, hv[ti], hs + k * 1024, SC1);
    SCHED();
    v2u xv[5], bv[5];
    unsigned ab[3];
#pragma unroll
    for (int i = 0; i < 3; ++i) { ab[i] = kh * 15360 + lane * 16 + 2 * i * RING2; asm volatile("" : "+v"(ab[i])); }
    auto rd = [&](int k, int n) { A[k & 1][n] = lds128(ab[((k / 3) % 5) >> 1] + (((k / 3) % 5) & 1) * RING2 + (k % 3) * 5120 + n * 1024); };
    unsigned fl = 0, cbase = CNT, sbase = sig_base(lane), one = 1u;
    asm volatile("" : "+v"(cbase), "+v"(sbase), "+v"(one));
    await<1>(E_FULL + 10 * 4, 4);
#pragma unroll
    for (int n = 0; n < 5; ++n) rd(0, n);
    STAMP(7); STAMPC(14);
    SCHED();
    static_for<27>([&](auto kc) {
      constexpr int k = kc;
#pragma unroll
      for (int ti = 0; ti < 2; ++ti) {
#pragma unroll
        for (int n = 0; n < 5; ++n) {
          acc[ti][n] = mfma16b(A[k & 1][n], H[k % W][ti], acc[ti][n]);
          SCHED();
          if (ti == 0 && k < 26) {
            rd(k + 1, n);
            if (n == 4 && k % 3 == 1) signal_at<E_DONE - CNT + (10 + k / 3) * 4>(sbase, one);
            SCHED();
          }
          if (ti == 1 && n == 0 && k % 3 == 1 && k < 24) { fl = *(volatile LAS unsigned*)lp(cbase + (E_FULL - CNT) + (11 + k / 3) * 4); SCHED(); }
        }
        if (k + W < 27) H[k % W][ti] = __builtin_amdgcn_raw_buffer_load_b128(ws, hv[ti], hs + (k + W) * 1024, SC1);
        if constexpr (k >= 21 && k < 26) {
          if (ti == 0) xv[k - 21] = __builtin_amdgcn_raw_buffer_load_b64(xr, tok * 1280 + 8 * g + 32 * (k - 21), 160 * ct, 0);
          else bv[k - 21] = __builtin_amdgcn_raw_buffer_load_b64(make_rsrc(p.b2, D_ * 2), 8 * g + 32 * (k - 21), 160 * ct, 0);
        }
        SCHED();
      }
      if (k % 3 == 1 && k < 24) { check(fl, E_FULL + (11 + k / 3) * 4, 4); STAMPL(22 + k / 3); SCHED(); }
    });
    STAMP(8); STAMPC(15);
    await<1>(E_DONE + 14 * 4, 4);
#pragma unroll
    for (int n = 0; n < 5; ++n) sts128(RED2 + w * 5120 + n * 1024 + lane * 16, __builtin_bit_cast(v4u, acc[1][n]));
    signal(E_RED + w * 4, lane);
    await<1>(E_SRDY, 2);
    await<1>(E_RED + (w ^ 2) * 4, 1);
    STAMP(9);
    const unsigned sa = STILE + (4 * g + ((lane & 15) >> 2)) * 160 + (32 * mh + 16 * kh + 4 * (lane & 3)) * 2;
#pragma unroll
    for (int n = 0; n < 5; ++n) {
      const f4v o = __builtin_bit_cast(f4v, lds128(RED2 + (w ^ 2) * 5120 + n * 1024 + lane * 16));
      const v2u sv = ldstr(sa + n * 2560);
      const float bz[4] = {blo(bv[n].x), bhi(bv[n].x), blo(bv[n].y), bhi(bv[n].y)};
      const float sz[4] = {blo(sv.x), bhi(sv.x), blo(sv.y), bhi(sv.y)};
      const float xz[4] = {blo(xv[n].x), bhi(xv[n].x), blo(xv[n].y), bhi(xv[n].y)};
      float r[4];
#pragma unroll
      for (int i = 0; i < 4; ++i) {
        const float z = acc[0][n][i] + o[i] + bz[i];
        const float gt = z * __builtin_amdgcn_rcpf(1.f + __builtin_amdgcn_exp2f(z * -1.44269504088896f));
        r[i] = fmaf(sz[i], gt, xz[i]);
      }
      __builtin_amdgcn_raw_buffer_store_b64((v2u){pkbf(r[0], r[1]), pkbf(r[2], r[3])}, make_rsrc(p.out, X_BYTES), tok * 1280 + 8 * g + 32 * n, 160 * ct, OUT_AUX);
    }
    STAMP(10);
    asm volatile(".Lfused_end_b:");
  } else {
    if (gemm_wg) await<2>(W2_AT, 4);
    STAMP(5);
    PRIO(3);
#pragma unroll 1
    for (int c = 0; c < 3; ++c) w2_chunk<PACE0>(wss, ct, w, c, lane);
    WAITV(16); signal(E_FULL + 10 * 4, lane);
    WAITV(8); signal(E_FULL + 11 * 4, lane);
    WAITV(0); signal(E_FULL + 12 * 4, lane);
    STAMP(6);
    await<4>(E_CONV, 4);
    STAMP(7);
    w2_chunk<0>(wss, ct, w, 3, lane);
    w2_chunk<0>(wss, ct, w, 4, lane);
    WAITV(8);
    signal(E_FULL + 13 * 4, lane);
#pragma unroll 1
    for (int c = 5; c < 9; ++c) {
      await(E_DONE + (5 + c) * 4, 4);
      w2_chunk<0>(wss, ct, w, c, lane);
      WAITV(8);
      signal(E_FULL + (9 + c) * 4, lane);
      STAMP(19 + c);
    }
    WAITV(0);
    signal(E_FULL + 18 * 4, lane);
    STAMP(10);
  }
  STAMP_FLUSH();
}

}

extern "C" void kernel_launch(void* const* d_in, const int* in_sizes, int n_in, void* d_out, int out_size, void* d_ws, size_t ws_size,
                              hipStream_t stream) {
  Params p;
  p.x = (const unsigned short*)d_in[0];
  p.lA = (const unsigned short*)d_in[1];
  p.Ai = (const unsigned short*)d_in[2];
  p.Cr = (const unsigned short*)d_in[3];
  p.Ci = (const unsigned short*)d_in[4];
  p.ldt = (const unsigned short*)d_in[5];
  p.Dsk = (const unsigned short*)d_in[6];
  p.w1 = (const unsigned short*)d_in[8];
  p.b1 = (const unsigned short*)d_in[9];
  p.w2 = (const unsigned short*)d_in[10];
  p.b2 = (const unsigned short*)d_in[11];
  p.out = (unsigned short*)d_out;
  p.ws = (unsigned char*)d_ws;
  (void)hipFuncSetAttribute((const void*)fused_kernel, hipFuncAttributeMaxDynamicSharedMemorySize, LDS_BYTES);
  fused_kernel<<<256, 768, LDS_BYTES, stream>>>(p);
}
```

```cpp
#include <hip/hip_runtime.h>
#include <hip/hip_bf16.h>

namespace {

typedef unsigned v4u __attribute__((ext_vector_type(4)));
typedef unsigned v2u __attribute__((ext_vector_type(2)));
typedef short s4v __attribute__((ext_vector_type(4)));
typedef float f2v __attribute__((ext_vector_type(2)));
typedef float f4v __attribute__((ext_vector_type(4)));
typedef float f16v __attribute__((ext_vector_type(16)));
typedef _Float16 h2v __attribute__((ext_vector_type(2)));
typedef _Float16 h8v __attribute__((ext_vector_type(8)));
typedef __bf16 b2v __attribute__((ext_vector_type(2)));
typedef __bf16 b8v __attribute__((ext_vector_type(8)));
#define GAS __attribute__((address_space(1)))
#define LAS __attribute__((address_space(3)))
#define DEV __device__ __forceinline__
typedef __amdgpu_buffer_rsrc_t rsrc_t;

constexpr int L_ = 2048, D_ = 640, F_ = 1728;
constexpr unsigned X_BYTES = L_ * D_ * 2, W_BYTES = D_ * F_ * 2;
constexpr int SC1 = 16;
constexpr unsigned SPIN_CAP = 1u << 16;

constexpr unsigned WS_EPOCH = 0;
constexpr unsigned WS_FALL = 4096;
constexpr unsigned WS_STAT = 32768;
constexpr unsigned WS_F2 = 65536;
constexpr unsigned WS_F1 = 5013504;
constexpr unsigned WS_FW = WS_F1 + 32768;
constexpr unsigned WS_F3 = WS_FW + 32768;
constexpr unsigned WS_XNT = 131072;
constexpr unsigned WS_W2T = 2752512;
constexpr unsigned WS_H = 5242880;
constexpr unsigned WS_ST = 12582912;
constexpr unsigned WS_STAMP = 15204352;
constexpr unsigned WS_BYTES = WS_STAMP + 256 * 12 * 64 * 8;

constexpr unsigned LDS_BYTES = 163840;
constexpr unsigned UNIT = 16384;
constexpr unsigned KSCR = 8704;
constexpr unsigned FPOS = 2576;
constexpr unsigned GRP = 7 * UNIT;
constexpr unsigned LNST = GRP + 4 * KSCR;
constexpr unsigned CNT = LNST + 10752;
constexpr unsigned SEXC = CNT + 512;
constexpr unsigned JUNK = CNT + 1024;
constexpr unsigned UCP = 2368;
constexpr unsigned CH_R3 = 4 * UCP, CH_U = 9856, CH_BYTES = 2 * KSCR;
constexpr unsigned SLOT_HI = 102400, STILE = 143360;
constexpr unsigned E_FULL = CNT, E_DONE = CNT + 40;
constexpr unsigned E_W0 = CNT + 80, E_W1 = CNT + 84, E_F2OK = CNT + 88, E_F3OK = CNT + 92, E_RED = CNT + 96;
constexpr unsigned E_QD = CNT + 384, E_QF = CNT + 104;
constexpr unsigned E_GELU = CNT + 184, E_LN = CNT + 188, E_LNPUB = CNT + 192, E_SRDY = CNT + 196, E_VMQ = CNT + 200, E_FWOK = CNT + 204, C_FW = CNT + 364, C_AW = CNT + 152, C_AX = CNT + 156, E_R1 = CNT + 160, E_SDR = CNT + 164, W_ISC = CNT + 472;
constexpr unsigned C_H = CNT + 208, C_F1 = CNT + 212, E_F1OK = CNT + 216, E_CONV = CNT + 220, E_SX = CNT + 368, E_RT = CNT + 360;
constexpr unsigned C_GRP = CNT + 224;
constexpr unsigned W_TAG = CNT + 464;
constexpr int N_ZERO = 116;

struct Params {
  const unsigned short *x, *lA, *Ai, *Cr, *Ci, *ldt, *Dsk, *w1, *b1, *w2, *b2;
  unsigned short* out;
  unsigned char* ws;
};

#define STAMP(i) do { } while (0)
#define STAMPC(i) do { } while (0)
#define STAMPV(i, v) do { } while (0)
#define STAMP_INIT() do { } while (0)
#define STAMP_FLUSH() do { } while (0)

#define PRIO(x) __builtin_amdgcn_s_setprio(x)
#define APRIO 0
#define LT0 t0
#define LF0 f0
#undef TACC
#define TACC(acc, stmt) do { stmt; } while (0)
#define STAMPL(i) do { } while (0)
#define CHK(e, a, n, t) check(e, a, n)
#define HADDR (hv + hs + hf * 512)
#define ROTV 0
#define F2_SLEEP 2
#define OUT_AUX 0
#define SLA 1
#define SLL 2
#define FALL_AT 5
#define W2_DEPTH 11
#define BAR() asm volatile("s_waitcnt lgkmcnt(0)\n\ts_barrier" ::: "memory")
#define WAITV(n) asm volatile("s_waitcnt vmcnt(" #n ")" ::: "memory")
#define SCHED() __builtin_amdgcn_sched_barrier(0)

template <int I> struct IC { constexpr operator int() const { return I; } };
template <int N, int I = 0, class F> DEV void static_for(F&& f) {
  if constexpr (I < N) { f(IC<I>{}); static_for<N, I + 1>(f); }
}
DEV int fresh_lane() {
  int l;
  asm volatile("v_mbcnt_lo_u32_b32 %0, -1, 0\n\tv_mbcnt_hi_u32_b32 %0, -1, %0" : "=v"(l));
  return l;
}
DEV float u2f(unsigned u) { return __builtin_bit_cast(float, u); }
DEV unsigned f2u(float f) { return __builtin_bit_cast(unsigned, f); }
DEV h2v u2h(unsigned u) { return __builtin_bit_cast(h2v, u); }
DEV float blo(unsigned u) { return u2f(u << 16); }
DEV float bhi(unsigned u) { return u2f(u & 0xffff0000u); }
DEV unsigned pkbf(float a, float b) { return __builtin_bit_cast(unsigned, __builtin_convertvector((f2v){a, b}, b2v)); }
DEV unsigned pkh(float a, float b) { return __builtin_bit_cast(unsigned, __builtin_convertvector((f2v){a, b}, h2v)); }
DEV float opq(float x) { asm("" : "+v"(x)); return x; }
DEV float bfr(float a) { return blo(pkbf(a, a)); }
DEV f16v mfma32b(v4u a, v4u b, f16v c) { return __builtin_amdgcn_mfma_f32_32x32x16_bf16(__builtin_bit_cast(b8v, a), __builtin_bit_cast(b8v, b), c, 0, 0, 0); }
DEV f16v mfma32h(v4u a, v4u b, f16v c) { return __builtin_amdgcn_mfma_f32_32x32x16_f16(__builtin_bit_cast(h8v, a), __builtin_bit_cast(h8v, b), c, 0, 0, 0); }
DEV f4v mfma16b(v4u a, v4u b, f4v c) { return __builtin_amdgcn_mfma_f32_16x16x32_bf16(__builtin_bit_cast(b8v, a), __builtin_bit_cast(b8v, b), c, 0, 0, 0); }
DEV f4v mfma16h(v4u a, v4u b, f4v c) { return __builtin_amdgcn_mfma_f32_16x16x32_f16(__builtin_bit_cast(h8v, a), __builtin_bit_cast(h8v, b), c, 0, 0, 0); }

DEV LAS unsigned char* lp(unsigned a) { return (LAS unsigned char*)(size_t)a; }
DEV v4u lds128(unsigned a) { return *(LAS v4u*)lp(a); }
DEV v2u lds64(unsigned a) { return *(LAS v2u*)lp(a); }
DEV unsigned lds32(unsigned a) { return *(LAS unsigned*)lp(a); }
DEV void sts128(unsigned a, v4u v) { *(LAS v4u*)lp(a) = v; }
DEV void sts64(unsigned a, v2u v) { *(LAS v2u*)lp(a) = v; }
DEV void sts32(unsigned a, unsigned v) { *(LAS unsigned*)lp(a) = v; }
DEV v2u ldstr(unsigned a) { return __builtin_bit_cast(v2u, __builtin_amdgcn_ds_read_tr16_b64_v4i16((LAS s4v*)lp(a))); }
DEV v4u ldstr2(unsigned a0, unsigned a1) { const v2u x = ldstr(a0), y = ldstr(a1); return (v4u){x.x, x.y, y.x, y.y}; }

DEV v4u make_srd(const void* ptr, unsigned bytes) {
  return (v4u){(unsigned)(size_t)ptr, (unsigned)((size_t)ptr >> 32) & 0xffffu, bytes, 0x00020000u};
}
DEV rsrc_t make_rsrc(const void* ptr, unsigned bytes) { return __builtin_amdgcn_make_buffer_rsrc((void*)ptr, 0, bytes, 0x00020000); }
#define DMA_HEAD "s_mov_b32 m0, %0\n\ts_nop 0\n\t"
DEV void dma1(v4u srd, unsigned v, unsigned soff, unsigned lds) {
  asm volatile(DMA_HEAD "buffer_load_dwordx4 %1, %2, %3 offen lds" ::"s"(lds), "v"(v), "s"(srd), "s"(soff) : "memory");
}
template <int OFF> DEV void dma_next(v4u srd, unsigned v, unsigned soff) {
  asm volatile("buffer_load_dwordx4 %0, %1, %2 offen offset:%3 lds" ::"v"(v), "s"(srd), "s"(soff), "i"(OFF) : "memory");
}
DEV void dma4(v4u srd, const unsigned (&v)[4], unsigned soff, unsigned lds) {
  asm volatile(DMA_HEAD "buffer_load_dwordx4 %1, %5, %6 offen lds\n\tbuffer_load_dwordx4 %2, %5, %6 offen offset:1024 lds\n\t"
               "buffer_load_dwordx4 %3, %5, %6 offen offset:2048 lds\n\tbuffer_load_dwordx4 %4, %5, %6 offen offset:3072 lds"
               ::"s"(lds), "v"(v[0]), "v"(v[1]), "v"(v[2]), "v"(v[3]), "s"(srd), "s"(soff) : "memory");
}
DEV void dma5_lin_sc1(v4u srd, unsigned v, unsigned soff, unsigned lds) {
  asm volatile(DMA_HEAD "buffer_load_dwordx4 %1, %2, %3 offen sc1 lds\n\tbuffer_load_dwordx4 %1, %2, %3 offen offset:1024 sc1 lds\n\t"
               "buffer_load_dwordx4 %1, %2, %3 offen offset:2048 sc1 lds\n\tbuffer_load_dwordx4 %1, %2, %3 offen offset:3072 sc1 lds"
               ::"s"(lds), "v"(v), "s"(srd), "s"(soff) : "memory");
  asm volatile(DMA_HEAD "buffer_load_dwordx4 %1, %2, %3 offen sc1 lds" ::"s"(lds + 4096), "v"(v), "s"(srd), "s"(soff + 4096) : "memory");
}

DEV void poll_words(rsrc_t ws, unsigned off, unsigned tag) {
#pragma unroll 1
  for (unsigned s = 0; s < SPIN_CAP; ++s) {
    asm volatile("" ::: "memory");
    if (__all(__builtin_amdgcn_raw_buffer_load_b32(ws, off, 0, SC1) == tag)) break;
    __builtin_amdgcn_s_sleep(F2_SLEEP);
  }
  asm volatile("" ::: "memory");
}
DEV unsigned sig_base(int lane) { return lane == 0 ? CNT : JUNK + lane * 4; }
template <int OFF> DEV void signal_at(unsigned base, unsigned one) {
  asm volatile("ds_add_u32 %0, %1 offset:%2" ::"v"(base), "v"(one), "i"(OFF) : "memory");
}
DEV void signal(unsigned a, int lane) {
  asm volatile("ds_add_u32 %0, %1" ::"v"(sig_base(lane) + (a - CNT)), "v"(1u) : "memory");
}
DEV unsigned peek(unsigned a) { return *(volatile LAS unsigned*)lp(a); }
template <int SLEEP = 1> DEV void await(unsigned a, unsigned need) {
#pragma unroll 1
  for (unsigned s = 0; s < (SPIN_CAP << 2); ++s) {
    asm volatile("" : "+s"(s));
    if (__builtin_amdgcn_readfirstlane(peek(a)) >= need) break;
    if (SLEEP) __builtin_amdgcn_s_sleep(SLEEP);
  }
  asm volatile("" ::: "memory");
}
DEV void check(unsigned early, unsigned a, unsigned need) {
  if (__builtin_expect(__builtin_amdgcn_readfirstlane(early) < need, 0)) await<1>(a, need);
  asm volatile("" ::: "memory");
}
DEV unsigned lds_arrive(unsigned cnt, int lane) {
  unsigned old;
  asm volatile("ds_add_rtn_u32 %0, %1, %2\n\ts_waitcnt lgkmcnt(0)" : "=&v"(old) : "v"(sig_base(lane) + (cnt - CNT)), "v"(1u) : "memory");
  return __builtin_amdgcn_readfirstlane(old);
}
template <int SLEEP = 1> DEV void group_sync(unsigned cnt, unsigned target, int lane) {
  if (lds_arrive(cnt, lane) + 1 < target) await<SLEEP>(cnt, target);
}
DEV void publish(rsrc_t ws, unsigned cnt, unsigned nwaves, unsigned flag_off, unsigned tag, int lane) {
  WAITV(0);
  if (lds_arrive(cnt, lane) + 1 == nwaves && lane == 0) __builtin_amdgcn_raw_buffer_store_b32(tag, ws, flag_off, 0, SC1);
}
DEV unsigned epoch_tag(unsigned ev, bool share, int lane) {
  const unsigned e = __builtin_amdgcn_readfirstlane(ev);
  const unsigned tag = e * 0x9E3779B1u + 0x7F4A7C15u;
  if (share && lane == 0) sts64(W_TAG, (v2u){tag, e});
  return tag;
}
DEV unsigned lds_tag() { return __builtin_amdgcn_readfirstlane(lds32(W_TAG)); }

template <int CTRL> DEV float dpp_add(float v) {
  return v + u2f(__builtin_amdgcn_update_dpp(0u, f2u(v), CTRL, 0xf, 0xf, false));
}
template <int CTRL> DEV float dpp_max(float v) {
  return fmaxf(v, u2f(__builtin_amdgcn_update_dpp(0u, f2u(v), CTRL, 0xf, 0xf, false)));
}
DEV float half_sum(float v) {
  v = dpp_add<0x128>(v); v = dpp_add<0x124>(v); v = dpp_add<0x122>(v); v = dpp_add<0x121>(v);
  const auto r = __builtin_amdgcn_permlane16_swap(f2u(v), f2u(v), false, false);
  return u2f(r[0]) + u2f(r[1]);
}
DEV float wave_max(float v) {
  v = dpp_max<0x128>(v); v = dpp_max<0x124>(v); v = dpp_max<0x122>(v); v = dpp_max<0x121>(v);
  const auto r = __builtin_amdgcn_permlane16_swap(f2u(v), f2u(v), false, false);
  v = fmaxf(u2f(r[0]), u2f(r[1]));
  const auto q = __builtin_amdgcn_permlane32_swap(f2u(v), f2u(v), false, false);
  return fmaxf(u2f(q[0]), u2f(q[1]));
}

struct LnIn { v2u v[5]; unsigned e; v4u b[4]; };
DEV LnIn ln_load(v4u xs, v4u wss, v4u w2s, int token, int blk, int lane) {
  LnIn r;
  const unsigned a = token * 1280 + (lane & 31) * 8, b = (blk / 10) * 40960 + (blk % 10) * 128 + (lane >> 3) * 1280 + (lane & 7) * 16;
  asm volatile("buffer_load_dwordx2 %0, %6, %7, 0 offen\n\tbuffer_load_dwordx2 %1, %6, %7, 0 offen offset:256\n\t"
               "buffer_load_dwordx2 %2, %6, %7, 0 offen offset:512\n\tbuffer_load_dwordx2 %3, %6, %7, 0 offen offset:768\n\t"
               "buffer_load_dwordx2 %4, %6, %7, 0 offen offset:1024\n\tbuffer_load_dword %5, off, %8, 0 sc1"
               : "=&v"(r.v[0]), "=&v"(r.v[1]), "=&v"(r.v[2]), "=&v"(r.v[3]), "=&v"(r.v[4]), "=&v"(r.e) : "v"(a), "s"(xs), "s"(wss) : "memory");
  asm volatile("buffer_load_dwordx4 %0, %4, %8, 0 offen\n\tbuffer_load_dwordx4 %1, %5, %8, 0 offen\n\t"
               "buffer_load_dwordx4 %2, %6, %8, 0 offen\n\tbuffer_load_dwordx4 %3, %7, %8, 0 offen"
               : "=&v"(r.b[0]), "=&v"(r.b[1]), "=&v"(r.b[2]), "=&v"(r.b[3]) : "v"(b), "v"(b + 10240), "v"(b + 20480), "v"(b + 30720), "s"(w2s) : "memory");
  return r;
}
template <int N> DEV void ln_wait(LnIn& r) {
  asm volatile("s_waitcnt vmcnt(%10)" : "+v"(r.v[0]), "+v"(r.v[1]), "+v"(r.v[2]), "+v"(r.v[3]), "+v"(r.v[4]), "+v"(r.e), "+v"(r.b[0]), "+v"(r.b[1]),
               "+v"(r.b[2]), "+v"(r.b[3]) : "i"(N) : "memory");
}
DEV f2v ln_compute(const LnIn& in, int row, int lane) {
  float xv[20], s = 0.f, q = 0.f;
#pragma unroll
  for (int m = 0; m < 5; ++m) { xv[4 * m] = blo(in.v[m].x); xv[4 * m + 1] = bhi(in.v[m].x); xv[4 * m + 2] = blo(in.v[m].y); xv[4 * m + 3] = bhi(in.v[m].y); }
#pragma unroll
  for (int m = 0; m < 20; ++m) { s = opq(s + xv[m]); q = opq(fmaf(xv[m], xv[m], q)); }
  s = half_sum(s); q = half_sum(q);
  const float mean = s * (1.f / 640.f);
  const float var = fmaxf(fmaf(-mean, mean, q * (1.f / 640.f)), 0.f);
  const float m1 = bfr(mean), s1 = bfr(__builtin_amdgcn_sqrtf(bfr(bfr(var) + 1e-5f)));
  const float r1 = __builtin_amdgcn_rcpf(s1), c = -m1 * r1;
#pragma unroll
  for (int m = 0; m < 5; ++m) {
    const v2u o = {pkh(opq(fmaf(xv[4 * m], r1, c)), opq(fmaf(xv[4 * m + 1], r1, c))), pkh(opq(fmaf(xv[4 * m + 2], r1, c)), opq(fmaf(xv[4 * m + 3], r1, c)))};
    sts64(LNST + row * 1344 + (lane & 31) * 8 + m * 256, o);
  }
  const float m2 = bfr((mean - m1) * r1), s2 = bfr(__builtin_amdgcn_sqrtf(bfr(bfr(var * r1 * r1) + 1e-5f)));
  return (f2v){opq(fmaf(m2, s1, m1)), opq(s1 * s2)};
}
DEV void ln_publish(rsrc_t ws, int tb, int w, int lane) {
  const unsigned a = LNST + ((lane & 15) >> 2) * 1344 + (lane >> 4) * 32 + (lane & 3) * 8 + w * 128;
  const unsigned o = WS_XNT + tb * 10240 + (w * 64 + lane) * 16;
  const v4u v0 = ldstr2(a, a + 4 * 1344), v1 = ldstr2(a + 512, a + 512 + 4 * 1344), v2 = ldstr2(a + (w < 2 ? 1024 : 0), a + (w < 2 ? 1024 : 0) + 4 * 1344);
  signal(E_LNPUB, lane);
  __builtin_amdgcn_raw_buffer_store_b128(v0, ws, o, 0, SC1);
  __builtin_amdgcn_raw_buffer_store_b128(v1, ws, o + 4096, 0, SC1);
  if (w < 2) __builtin_amdgcn_raw_buffer_store_b128(v2, ws, o + 8192, 0, SC1);
}

struct Cx { float r, i; };
DEV Cx cmul(Cx a, Cx b) { return {opq(fmaf(a.r, b.r, -a.i * b.i)), opq(fmaf(a.r, b.i, a.i * b.r))}; }
DEV Cx csqr(Cx a) { return {opq(fmaf(a.r, a.r, -a.i * a.i)), opq((a.r + a.r) * a.i)}; }
DEV unsigned ch(Cx a) { return pkh(a.r, a.i); }
DEV unsigned hmul(unsigned a, unsigned b) {
  unsigned t, r;
  asm("v_pk_mul_f16 %0, %1, %2 op_sel:[0,0] op_sel_hi:[0,1]" : "=v"(t) : "v"(a), "v"(b));
  asm("v_pk_fma_f16 %0, %1, %2, %3 op_sel:[1,1,0] op_sel_hi:[1,0,1] neg_lo:[0,1,0]" : "=v"(r) : "v"(a), "v"(b), "v"(t));
  return r;
}
DEV float exp_bf(float x) {
  const float t = x * 1.44268798828125f, lo = x * 7.0526077138765e-06f;
  const float n = __builtin_rintf(t);
  return bfr(__builtin_amdgcn_ldexpf(__builtin_amdgcn_exp2f((t - n) + lo), (int)fminf(fmaxf(n, -200.f), 200.f)));
}
DEV Cx cexp_at(float lg, float rh, float rl, float P) {
  const float m = __builtin_amdgcn_exp2f(lg * P), ph = fmaf(rl, P, __builtin_amdgcn_fractf(rh * P));
  return {opq(m * __builtin_amdgcn_cosf(ph)), opq(m * __builtin_amdgcn_sinf(ph))};
}
struct KgIn { unsigned lA, Ai, Cr, Ci; float dt, dsk; };
DEV KgIn kg_load(const Params& p, int d, int lane) {
  KgIn k;
  k.lA = ((const GAS unsigned*)p.lA)[d * 64 + lane]; k.Ai = ((const GAS unsigned*)p.Ai)[d * 64 + lane];
  k.Cr = ((const GAS unsigned*)p.Cr)[d * 64 + lane]; k.Ci = ((const GAS unsigned*)p.Ci)[d * 64 + lane];
  k.dt = u2f((unsigned)((const GAS unsigned short*)p.ldt)[d] << 16);
  k.dsk = u2f((unsigned)((const GAS unsigned short*)p.Dsk)[d] << 16);
  return k;
}
DEV float kg_wave(const KgIn& k, int g, unsigned scr, unsigned fp, unsigned busy, int lane) {
  const float dt = exp_bf(k.dt), c2 = dt + dt;
  const float mx = wave_max(fmaxf(fmaxf(fabsf(blo(k.Cr)), fabsf(bhi(k.Cr))), fmaxf(fabsf(blo(k.Ci)), fabsf(bhi(k.Ci))))) * c2;
  int ex = __builtin_amdgcn_frexp_expf(fmaxf(mx, fabsf(k.dsk) * 0x1p-10f));
  ex = ex < -120 ? -120 : (ex > 120 ? 120 : ex);
  const float cs = c2 * __builtin_amdgcn_ldexpf(1.f, -ex);
  const int sh = g ? 0 : 16;
  const float lg = -(exp_bf(bhi(k.lA << sh)) * dt) * 1.44269504088896f, ai = bhi(k.Ai << sh) * dt;
  const Cx c0 = {opq(bhi(k.Cr << sh) * cs), opq(-bhi(k.Ci << sh) * cs)};
  const float rh = ai * 0.15915494f, rl = fmaf(ai, 0.15915494f, -rh) + ai * 6.4206382e-09f;
  const Cx v1 = cexp_at(lg, -rh, -rl, 1.f), w1 = cexp_at(lg, rh, rl, 8.f), G = cexp_at(lg, -rh, -rl, 256.f);
  const unsigned wa = scr + lane * 4, ra = scr + (lane & 31) * 272 + (lane >> 5) * 16;
  {
    const Cx w2 = csqr(w1), w3 = cmul(w2, w1), w4 = csqr(w2), w8 = csqr(w4), w12 = cmul(w8, w4), w16 = csqr(w8);
    const Cx w20 = cmul(w16, w4), w24 = csqr(w12), w28 = cmul(w24, w4);
    const unsigned o[3] = {ch(w1), ch(w2), ch(w3)};
    const unsigned bs[7] = {ch(w4), ch(w8), ch(w12), ch(w16), ch(w20), ch(w24), ch(w28)};
    await(E_RT, busy);
    sts32(wa, 0x00003c00u);
#pragma unroll
    for (int j = 0; j < 3; ++j) sts32(wa + (1 + j) * 272, o[j]);
#pragma unroll
    for (int b = 0; b < 7; ++b) {
      sts32(wa + (4 + 4 * b) * 272, bs[b]);
#pragma unroll
      for (int j = 0; j < 3; ++j) sts32(wa + (5 + 4 * b + j) * 272, hmul(bs[b], o[j]));
    }
  }
  asm volatile("" ::: "memory");
  v4u y[8];
#pragma unroll
  for (int s = 0; s < 8; ++s) y[s] = lds128(ra + s * 32);
  asm volatile("" ::: "memory");
  {
    const Cx v2 = csqr(v1), v3 = cmul(v2, v1), v4 = csqr(v2), v6 = csqr(v3), x4 = cmul(c0, v4);
    const unsigned h1 = ch(v1), h2 = ch(v2), h3 = ch(v3), h4 = ch(v4), h6 = ch(v6), hx0 = ch(c0), hx4 = ch(x4);
    sts32(wa + 0 * 272, hx0); sts32(wa + 1 * 272, hmul(hx0, h1)); sts32(wa + 2 * 272, hmul(hx0, h2)); sts32(wa + 3 * 272, hmul(hx0, h3));
    sts32(wa + 4 * 272, hx4); sts32(wa + 5 * 272, hmul(hx4, h1)); sts32(wa + 6 * 272, hmul(hx4, h2)); sts32(wa + 7 * 272, hmul(hx4, h3));
    const Cx d1 = cmul(c0, G), d2 = cmul(d1, G), d3 = cmul(d2, G), d4 = cmul(d3, G);
    const Cx G4 = csqr(csqr(G));
    const Cx d5 = cmul(d1, G4), d6 = cmul(d2, G4), d7 = cmul(d3, G4), d8 = cmul(d4, G4);
    const unsigned e1 = ch(d1), e2 = ch(d2), e3 = ch(d3);
    sts32(wa + 8 * 272, e1); sts32(wa + 9 * 272, hmul(e1, h2)); sts32(wa + 10 * 272, hmul(e1, h4)); sts32(wa + 11 * 272, hmul(e1, h6));
    sts32(wa + 12 * 272, e2); sts32(wa + 13 * 272, hmul(e2, h4)); sts32(wa + 14 * 272, e3); sts32(wa + 15 * 272, hmul(e3, h4));
    sts32(wa + 16 * 272, ch(d4)); sts32(wa + 17 * 272, ch(d5)); sts32(wa + 18 * 272, ch(d6)); sts32(wa + 19 * 272, ch(d7));
    sts32(wa + 20 * 272, ch(d8));
  }
  asm volatile("" ::: "memory");
  v4u xx[8];
#pragma unroll
  for (int s = 0; s < 8; ++s) xx[s] = lds128(ra + s * 32);
  SCHED();
  f16v acc = {};
#pragma unroll
  for (int s = 0; s < 8; ++s) acc = mfma32h(xx[s], y[s], acc);
  const int j = lane & 31, h = lane >> 5;
  await(E_LNPUB, busy);
  if (g == 0 && lane == 0) acc[0] += __builtin_amdgcn_ldexpf(k.dsk, -ex);
  sts128(fp + (8 * j + 4 * h) * 4, (v4u){f2u(acc[0]), f2u(acc[1]), f2u(acc[2]), f2u(acc[3])});
  sts64(fp + (h ? 384 + 2 * j : 256 + 4 * j) * 4, (v2u){f2u(acc[4]), f2u(acc[5])});
  sts64(fp + (h ? 448 + 2 * j : 258 + 4 * j) * 4, (v2u){f2u(acc[6]), f2u(acc[7])});
  if (lane <= 32) sts32(fp + (512 + 128 * h + j) * 4, f2u(acc[8]));
  if (h == 0) {
#pragma unroll
    for (int i = 1; i < 4; ++i) sts32(fp + (512 + j + 32 * i) * 4, f2u(acc[8 + i]));
  }
  return __builtin_amdgcn_ldexpf(1.f, ex);
}
DEV void kg_table0(unsigned cb, unsigned fp, int g, int lane) {
  auto f1 = [&](int i) { return opq(u2f(lds32(fp + i * 4)) + u2f(lds32(fp + FPOS + i * 4))); };
  if (g == 0) {
    const int j = lane & 31, h = lane >> 5;
    const unsigned a = fp + (h ? 256 + 4 * j : 8 * j) * 4;
    const v4u l0 = lds128(a), h0 = lds128(a + 16), l1 = lds128(a + FPOS), h1 = lds128(a + FPOS + 16);
    const float e0 = opq(u2f(l0.x) + u2f(l1.x)), e1 = opq(u2f(l0.y) + u2f(l1.y)), e2 = opq(u2f(l0.z) + u2f(l1.z)), e3 = opq(u2f(l0.w) + u2f(l1.w));
    const float e4 = opq(u2f(h0.x) + u2f(h1.x)), e5 = opq(u2f(h0.y) + u2f(h1.y)), e6 = opq(u2f(h0.z) + u2f(h1.z)), e7 = opq(u2f(h0.w) + u2f(h1.w));
    const v4u o0 = {pkh(e0, e1), pkh(e2, e3), pkh(e4, e5), pkh(e6, e7)};
    const v4u o1 = {pkh(e0, e0), pkh(e1, e2), pkh(e2, e2), pkh(e3, e4)};
    sts128(cb + 32 + lane * 16, h ? o1 : o0);
    const float fa = f1(512), fb = f1(513);
    const unsigned aa = pkh(fa, fa), bb = pkh(fb, fb);
    if (lane < 4) sts128(cb + 32 + (128 + lane) * 16, lane == 0 ? (v4u){aa, aa, pkh(fa, fb), bb} : lane == 1 ? (v4u){bb, bb, 0u, 0u} : (v4u){0u, 0u, 0u, 0u});
  } else {
    const float a = f1(384 + 2 * lane), b = f1(385 + 2 * lane), c = f1(386 + 2 * lane);
    sts128(cb + 32 + (64 + lane) * 16, (v4u){pkh(a, a), pkh(a, b), pkh(b, b), pkh(c, c)});
    const float lo = f1(640 - 2 * lane), hi = f1(lane ? 641 - 2 * lane : 640);
    sts32(cb + CH_R3 + lane * 4, pkh(lane ? hi : 0.f, lo));
    if (lane < 16) sts32(cb + CH_R3 + 256 + lane * 4, 0u);
    if (lane < 32) sts32(cb + (lane >> 3) * UCP + (lane & 7) * 4, 0u);
  }
}
DEV void kg_copies(unsigned cb, int g, int lane) {
  auto one = [&](int chunk) {
    const unsigned o = cb + 32 + chunk * 16;
    const v2u pv = lds64(o - 8);
    const v4u e = lds128(o);
    const unsigned a0 = __builtin_amdgcn_alignbit(pv.y, pv.x, 16), a1 = __builtin_amdgcn_alignbit(e.x, pv.y, 16), a2 = __builtin_amdgcn_alignbit(e.y, e.x, 16),
                   a3 = __builtin_amdgcn_alignbit(e.z, e.y, 16), a4 = __builtin_amdgcn_alignbit(e.w, e.z, 16);
    sts128(o + UCP, (v4u){a1, a2, a3, a4});
    sts128(o + 2 * UCP, (v4u){pv.y, e.x, e.y, e.z});
    sts128(o + 3 * UCP, (v4u){a0, a1, a2, a3});
  };
  one(64 * g + lane);
  if (lane < 2) one(128 + 2 * g + lane);
}
DEV void conv_load_u(rsrc_t ws, unsigned grp, int d0, int w, int lane) {
  v4u v[2];
#pragma unroll
  for (int i = 0; i < 2; ++i)
    v[i] = __builtin_amdgcn_raw_buffer_load_b128(ws, (lane >> 1) * 10240 + (lane & 1) * 16, WS_XNT + (64 * w + 32 * i) * 10240 + d0 * 16, SC1);
  if (lane < 16) sts128(grp + (w >> 1) * CH_BYTES + CH_U + (w & 1) * 256 + lane * 16, (v4u){0u, 0u, 0u, 0u});
#pragma unroll
  for (int i = 0; i < 2; ++i) sts128(grp + (lane & 1) * CH_BYTES + CH_U + 512 + (64 * w + 32 * i + (lane >> 1)) * 16, v[i]);
}
template <class F1>
DEV float ssm_prepare(rsrc_t ws, const KgIn& k, unsigned grp, unsigned fp, unsigned busy, unsigned cnt, int d0, int w, int lane, F1 f1, unsigned isc_out = 0) {
  const int ci = w >> 1, g = w & 1;
  const float isc = kg_wave(k, g, grp + w * KSCR, fp + w * FPOS, busy, lane);
  if (isc_out && grp == GRP) sts32(isc_out + ci * 4, f2u(isc));
  group_sync(cnt, 4, lane);
  kg_table0(grp + ci * CH_BYTES, fp + 2 * ci * FPOS, g, lane);
  group_sync(cnt + 4 + ci * 4, 2, lane);
  kg_copies(grp + ci * CH_BYTES, g, lane);
  f1();
  conv_load_u(ws, grp, d0, w, lane);
  group_sync(cnt + 12, 4, lane);
  return isc;
}

template <int J0, int NJ, class Go, class Done>
DEV void conv_tiles(rsrc_t ws, unsigned cb, int d, float isc, int lane, Go go, Done done) {
  constexpr int B0 = J0 > 3 ? J0 - 3 : 0, NB = J0 + NJ - B0;
  const int g = lane >> 4, c16 = lane & 15;
  const unsigned ga = cb + (c16 >> 2) * UCP + 64 - 16 * g + 8 * (lane & 3);
  const unsigned ba = cb + CH_U + 480 + 32 * c16 + 16 * g;
  f4v acc[NJ] = {};
  {
    unsigned w7[4], w9[4];
#pragma unroll
    for (int i = 0; i < 4; ++i) {
      const int k0 = 8 * g + 2 * i - c16;
      w7[i] = ((k0 >= 5 && k0 <= 11) ? 0x3c00u : 0u) | ((k0 >= 4 && k0 <= 10) ? 0x3c000000u : 0u);
      w9[i] = ((k0 >= 4 && k0 <= 12) ? 0x3c00u : 0u) | ((k0 >= 3 && k0 <= 11) ? 0x3c000000u : 0u);
    }
    const v4u W7 = {w7[0], w7[1], w7[2], w7[3]}, W9 = {w9[0], w9[1], w9[2], w9[3]};
    const unsigned r3 = cb + CH_R3 + (127 - c16 + 4 * g) * 4;
    const v4u g4 = ldstr2(ga + 2048, ga + 2048 - 8);
#pragma unroll
    for (int b = 0; b + 4 < J0 + NJ; ++b) {
      const f4v b7 = mfma16h(lds128(ba - 32 + 512 * b), W7, (f4v){}), b9 = mfma16h(lds128(ba - 16 + 512 * b), W9, (f4v){});
      const v4u bx = {pkh(b7[0], b9[0]), pkh(b7[1], b9[1]), pkh(b7[2], b9[2]), pkh(b7[3], b9[3])};
#pragma unroll
      for (int j = (b + 4 > J0 ? b + 4 : J0); j < J0 + NJ; ++j) {
        const unsigned a = r3 - 64 * (j - b);
        acc[j - J0] = mfma16h(bx, (v4u){lds32(a), lds32(a + 4), lds32(a + 8), lds32(a + 12)}, acc[j - J0]);
      }
      if (b + 4 >= J0) acc[b + 4 - J0] = mfma16h(g4, lds128(ba + 512 * b), acc[b + 4 - J0]);
    }
  }
  v4u G[2][4], B[2][NB];
  auto rd = [&](int e, int i) {
    constexpr int top = NJ;
    if (i == 0) G[e & 1][0] = ldstr2(ga + 64 * e, ga + 64 * e - 8);
    else if (i <= top) B[e & 1][NB - top + i - 1] = lds128(ba + 512 * (B0 + NB - top + i - 1) - 64 * e);
    else {
      const int k = i - top - 1;
      if (NB == top || (k & 1) == 0) { const int c = NB == top ? k + 1 : k / 2 + 1; G[e & 1][c] = ldstr2(ga + 512 * c + 64 * e, ga + 512 * c + 64 * e - 8); }
      else { const int bi = NB - top - 1 - k / 2; B[e & 1][bi] = lds128(ba + 512 * (B0 + bi) - 64 * e); }
    }
  };
#pragma unroll
  for (int i = 0; i < 4 + NB; ++i) rd(0, i);
  SCHED();
  go();
#pragma unroll
  for (int e = 0; e < 8; ++e) {
    int m = 0;
#pragma unroll
    for (int c = 0; c < 4; ++c)
#pragma unroll
      for (int j = (c > J0 ? c : J0); j < J0 + NJ; ++j) {
        acc[j - J0] = mfma16h(G[e & 1][c], B[e & 1][j - c - B0], acc[j - J0]);
        if (e < 7 && m < 4 + NB) rd(e + 1, m);
        if (e == 6 && m == 3 + NB) done();
        ++m;
        SCHED();
      }
  }
  unsigned pk[NJ][2];
#pragma unroll
  for (int j = 0; j < NJ; ++j) { pk[j][0] = pkbf(opq(acc[j][0] * isc), opq(acc[j][1] * isc)); pk[j][1] = pkbf(opq(acc[j][2] * isc), opq(acc[j][3] * isc)); }
  const unsigned so = WS_ST + d * 4096 + J0 * 512;
#pragma unroll
  for (int j = 0; j + 1 < NJ; j += 2) {
    const auto x = __builtin_amdgcn_permlane16_swap(pk[j][0], pk[j + 1][0], false, false);
    const auto y = __builtin_amdgcn_permlane16_swap(pk[j][1], pk[j + 1][1], false, false);
    __builtin_amdgcn_raw_buffer_store_b128((v4u){x[0], y[0], x[1], y[1]}, ws, so + (j + (g & 1)) * 512 + 32 * c16 + 16 * (g >> 1), 0, SC1);
  }
  __builtin_amdgcn_raw_buffer_store_b64((v2u){pk[NJ - 1][0], pk[NJ - 1][1]}, ws, so + (NJ - 1) * 512 + 32 * c16 + 8 * g, 0, SC1);
}
template <class Go, class Done>
DEV void conv_channel(rsrc_t ws, unsigned cb, int d, int half, float isc, int lane, Go go, Done done) {
  if (half) conv_tiles<5, 3>(ws, cb, d, isc, lane, go, done);
  else conv_tiles<0, 5>(ws, cb, d, isc, lane, go, done);
}

DEV void retile(rsrc_t ws, int blk, const v4u (&rows)[4], unsigned stg, int lane) {
#pragma unroll
  for (int j = 0; j < 4; ++j) sts128(stg + j * 1024 + (lane >> 3) * 128 + 16 * ((lane & 7) ^ (((lane >> 4) & 3) << 1)), rows[j]);
  asm volatile("" ::: "memory");
  const int g = lane >> 4, pp = lane & 3, row = 16 * (g >> 1) + 4 * (g & 1) + ((lane & 15) >> 2);
  v4u f[4];
#pragma unroll
  for (int j = 0; j < 4; ++j) {
    const unsigned a = stg + row * 128 + 16 * ((2 * j + (pp >> 1)) ^ (((row >> 1) & 3) << 1)) + 8 * (pp & 1);
    f[j] = ldstr2(a, a + 1024);
  }
  signal(E_RT, lane);
#pragma unroll
  for (int j = 0; j < 4; ++j) {
    const int ng = 4 * (blk % 10) + j;
    __builtin_amdgcn_raw_buffer_store_b128(f[j], ws, lane * 16 + WS_W2T + (((ng / 5) * 54 + blk / 10) * 5 + ng % 5) * 1024, 0, SC1);
  }
}

__global__ __launch_bounds__(768, 3) void fused_kernel(Params p) {
  extern __shared__ __attribute__((aligned(16))) unsigned char lds_base[];
  unsigned long long pc;
  unsigned code_bytes;
  asm volatile("s_getpc_b64 %0\n.Lfused_begin:\n\ts_mov_b32 %1, .Lfused_end_a-.Lfused_begin\n\ts_max_i32 %1, %1, .Lfused_end_b-.Lfused_begin"
               : "=s"(pc), "=s"(code_bytes) : : "scc");
  const int lane = threadIdx.x & 63, wave = __builtin_amdgcn_readfirstlane(threadIdx.x >> 6), role = wave >> 2, w = wave & 3;
  const int xcd = blockIdx.x & 7, slot = blockIdx.x >> 3, tb = 32 * xcd + slot;
  const bool gemm_wg = slot < 28;
  const int rp = 4 * (xcd >> 1) + slot / 7, cp = 7 * (xcd & 1) + slot % 7, t0 = 128 * rp, f0 = 128 * cp;
  STAMP_INIT();
  STAMP(0);
  asm volatile("" ::"v"((LAS unsigned char*)lds_base));

  const unsigned vx0 = (lane >> 3) * 1280 + 16 * ((lane & 7) ^ (lane >> 4));
  const unsigned wc = (lane & 15) ^ ((lane >> 4) << 2);
  const unsigned vw0 = (cp == 13 && wc >= 8) ? 0x40000000u : (lane >> 4) * 3456 + 16 * wc;
  const unsigned vx[4] = {vx0, (vx0 ^ 64) + 9216, vx0 + 2 * 9216, (vx0 ^ 64) + 3 * 9216};
  const unsigned vw[4] = {vw0, vw0 + 12800, vw0 + 2 * 12800, vw0 + 3 * 12800};
  auto stage_x = [&](int i) { dma4(make_srd(p.x, X_BYTES), vx, (LT0 + 32 * w) * 1280 + 128 * ((i + ROTV) % 10), ((2 * i) % 7) * UNIT + w * 4096); };
  auto stage_w = [&](int i) { dma4(make_srd(p.w1, W_BYTES), vw, (64 * ((i + ROTV) % 10) + 16 * w) * 3456 + 2 * LF0, ((2 * i + 1) % 7) * UNIT + w * 4096); };
  const rsrc_t ws = make_rsrc(p.ws, WS_BYTES), xr = make_rsrc(p.x, X_BYTES);
  const v4u wss = make_srd(p.ws, WS_BYTES);
  const bool last_pair = role == (gemm_wg ? 2 : 1);
  const unsigned grp = last_pair ? GRP : (role >> 1) * (4 * KSCR + 4 * FPOS), fpos = last_pair ? LNST : grp + 4 * KSCR, cnt = C_GRP + role * 48;
  auto stage2_or_skip = [&] {
    dma4(make_srd(p.x, gemm_wg ? X_BYTES : 0u), vx, (LT0 + 32 * w) * 1280 + 128 * 2, gemm_wg ? 4 * UNIT + w * 4096 : 98304u);
    dma4(make_srd(p.w1, gemm_wg ? W_BYTES : 0u), vw, (64 * 2 + 16 * w) * 3456 + 2 * LF0, gemm_wg ? 5 * UNIT + w * 4096 : 98304u);
  };
  if (role == 0 && gemm_wg) { stage_x(0); stage_w(0); }
  if (wave >= 10) dma1(make_srd((const void*)pc, code_bytes & ~15u), lane * 16, (2 * slot + wave - 10) * 1024, JUNK);

  const int dg = 80 * xcd + (gemm_wg ? 2 * slot : 56 + 6 * (slot - 28) + 2 * role);
  const int tt = 4 * xcd + (slot >> 3), ct = slot & 7;
  if (threadIdx.x < N_ZERO) sts32(CNT + threadIdx.x * 4, 0u);

  if (role == 2) {
    const KgIn kin = kg_load(p, dg + (w >> 1), lane);
    BAR();
    float isc = ssm_prepare(ws, kin, grp, fpos, gemm_wg ? 4 : 0, cnt, dg, w, lane, [&] {
      STAMP(2);
      if (gemm_wg && w == 0) {
        await<SLA>(E_VMQ, 4);
        poll_words(ws, WS_FALL + xcd * 256 + 128, lds_tag());
        signal(E_F1OK, lane);
        if (blockIdx.x == 0 && lane == 0) __builtin_amdgcn_raw_buffer_store_b32(lds32(W_TAG + 4) + 1u, ws, WS_EPOCH, 0, SC1);
      } else {
        await<SLA>(E_F1OK, 1);
      }
      STAMP(3);
    });
    STAMP(6);
    unsigned cb = grp + (w >> 1) * CH_BYTES;
    int d = dg + (w >> 1);
#pragma unroll 1
    for (int last = gemm_wg; last < 2; ++last) {
      conv_channel(ws, cb, d, w & 1, isc, lane, [&] { if (gemm_wg) await(E_GELU, 4); STAMP(7); }, [&] { signal(last ? E_CONV : E_R1, lane); });
      if (!last) {
        await(C_GRP + 48 + 12, 4);
        cb = GRP + (w >> 1) * CH_BYTES;
        d -= 2;
        isc = u2f(peek(W_ISC + (w >> 1) * 4));
        STAMP(5);
      }
    }
    STAMP(8);
  }

  if (role == 1) {
    const int token = 8 * tb + 2 * w + (lane >> 5);
    PRIO(3);
    const int fi = 4 * xcd + slot - 28, blk = w < 2 ? 2 * tb + w : (!gemm_wg && w == 2 && fi < 28) ? 512 + fi : -1;
    LnIn ln = ln_load(make_srd(p.x, X_BYTES), wss, make_srd(p.w2, blk < 0 ? 0u : W_BYTES), token, max(blk, 0), lane);
    stage2_or_skip();
    BAR();
    ln_wait<8>(ln);
    const unsigned tag = epoch_tag(ln.e, w == 0, lane);
    if (blk >= 0) retile(ws, blk, ln.b, GRP + w * KSCR, lane); else signal(E_RT, lane);
    STAMP(1);
    const f2v stat = ln_compute(ln, 2 * w + (lane >> 5), lane);
    signal(E_LN, lane);
    publish(ws, C_FW, 4, WS_FW + tb * 128, tag, lane);
    signal(E_FULL + 8, lane);
    STAMP(2);
    if ((lane & 31) == 0) __builtin_amdgcn_raw_buffer_store_b128((v4u){f2u(stat.x), tag, f2u(stat.y), tag}, ws, WS_STAT + token * 16, 0, SC1);
    await(E_LN, 4);
    ln_publish(ws, tb, w, lane);
    STAMP(3);
    PRIO(0);
    publish(ws, C_F1, 4, WS_F1 + tb * 128, tag, lane);
    STAMP(4);
    if (gemm_wg && cp == 13 && (rp & 1) == 0) {
      poll_words(ws, WS_FW + (64 * w + lane) * 128, tag);
      if (lds_arrive(C_AW, lane) == 3 && lane == 0) __builtin_amdgcn_raw_buffer_store_b32(tag, ws, WS_FALL + (rp >> 1) * 256, 0, SC1);
      poll_words(ws, WS_F1 + (64 * w + lane) * 128, tag);
      if (lds_arrive(C_AX, lane) == 3 && lane == 0) __builtin_amdgcn_raw_buffer_store_b32(tag, ws, WS_FALL + (rp >> 1) * 256 + 128, 0, SC1);
    }
    if (gemm_wg) await<SLL>(E_DONE + FALL_AT * 4, 4);
  }

  if (role == 2) {
  } else if (!gemm_wg) {
    const KgIn kin = kg_load(p, dg + (w >> 1), lane);
    if (role == 0) BAR();
    const float isc = ssm_prepare(ws, kin, grp, fpos, role ? 4 : 0, cnt, dg, w, lane, [&] {
      STAMP(17);
      if (wave == 0) { await(E_LN, 4); poll_words(ws, WS_FALL + xcd * 256 + 128, lds_tag()); signal(E_F1OK, lane); } else { await<2>(E_F1OK, 1); }
      STAMP(18);
    }, W_ISC);
    STAMP(16);
    if (role == 0) {
      conv_channel(ws, grp + (w >> 1) * CH_BYTES, dg + (w >> 1), w & 1, isc, lane, [&] {}, [&] { signal(E_R1, lane); });
      WAITV(0);
      signal(E_SDR, lane);
      STAMP(5);
    }
  } else if (role == 0) {
    const int wf = w & 1, wt = w >> 1, h = lane >> 5;
    BAR();
    stage_x(1); stage_w(1);
    PRIO(2);
    unsigned aa[2][2], ba[2][4];
    {
      const int q = (lane & 15) >> 2, pp = lane & 3, fb = (lane >> 4) & 1;
#pragma unroll
      for (int mi = 0; mi < 2; ++mi) {
        aa[0][mi] = (8 * h + q) * 256 + 16 * ((8 * wf + 4 * (mi ^ wt) + 2 * fb + (pp >> 1)) ^ (q << 2)) + 8 * (pp & 1);
        aa[1][mi] = aa[0][mi] + 4 * UNIT;
        asm volatile("" : "+v"(aa[1][mi]));
      }
#pragma unroll
      for (int s = 0; s < 4; ++s) {
        ba[0][s] = (64 * wt + (lane & 31)) * 128 + 16 * ((2 * s + h) ^ ((lane >> 1) & 7));
        ba[1][s] = ba[0][s] + 4 * UNIT;
        asm volatile("" : "+v"(ba[1][s]));
      }
    }
    f16v acc[2][2] = {};
    v4u A[3][2], B[3][2], st[2];
    unsigned b1v[2], fl = 0, dn = 0, sbase = sig_base(lane), one = 1u, vxo = vx0 ^ 64;
    asm volatile("" : "+v"(sbase), "+v"(one), "+v"(vxo));
    f4v cs4 = {};
    const unsigned sel1 = (((lane >> 2) ^ (lane >> 4)) & 1) ? 0u : 0x3f803f80u;
    const v4u sel = {sel1, sel1, sel1, sel1};
    auto rdA = [&](int n, int mi) {
      const int u = (2 * (n >> 2) + 1) % 7;
      const unsigned a = aa[u >> 2][mi] + (u & 3) * UNIT + (n & 3) * 4096;
      A[n % 3][mi] = ldstr2(a, a + 1024);
    };
    auto rdB = [&](int n, int ni) {
      const int u = (2 * (n >> 2)) % 7;
      B[n % 3][ni] = lds128(ba[u >> 2][n & 3] + (u & 3) * UNIT + ni * 4096);
    };
    const v4u xs = make_srd(p.x, X_BYTES), w1s = make_srd(p.w1, W_BYTES);
    unsigned sx[4], sw[4], mb = w * 4096;
#pragma unroll
    for (int j = 0; j < 4; ++j) {
      sx[j] = (LT0 + 32 * w) * 1280 + ((3 + ROTV) % 10) * 128 + j * 9216;
      sw[j] = (((3 + ROTV) % 10) * 64 + 16 * w) * 3456 + 2 * LF0 + j * 12800;
      asm volatile("" : "+s"(sx[j]), "+s"(sw[j]));
    }
    auto piece = [&](auto qc) {
      constexpr int q = qc, i = (q + 8) / 8 + 2 + ((q >> 2) & 1), j = q & 3;
      if constexpr (q >= -4 && q < 52) {
        if constexpr (q & 4) {
          if constexpr (j == 0) dma1(xs, vx0, sx[0], mb + ((2 * i) % 7) * UNIT);
          else dma_next<j * 1024>(xs, (j & 1) ? vxo : vx0, sx[j]);
          sx[j] += (i + ROTV) % 10 == 9 ? -9 * 128 : 128;
          asm volatile("" : "+s"(sx[j]));
        } else {
          if constexpr (j == 0) dma1(w1s, vw0, sw[0], mb + ((2 * i + 1) % 7) * UNIT);
          else dma_next<j * 1024>(w1s, vw0, sw[j]);
          sw[j] += (i + ROTV) % 10 == 9 ? -9 * 64 * 3456 : 64 * 3456;
          asm volatile("" : "+s"(sw[j]));
        }
      }
    };
    auto load_stats = [&] {
#pragma unroll
      for (int i = 0; i < 2; ++i) st[i] = __builtin_amdgcn_raw_buffer_load_b128(ws, (lane & 31) * 16, WS_STAT + (t0 + 64 * wt + 32 * i) * 16, SC1);
    };
    WAITV(8);
    group_sync(E_FULL, 4, lane);
    STAMP(2); STAMPC(11);
    rdA(0, 0); rdB(0, 0); rdB(0, 1); rdA(0, 1); rdA(1, 0); rdB(1, 0); rdB(1, 1); rdA(1, 1);
    SCHED();
    unsigned tag = 0, tf = 0, td = 0, tv = 0;
    v4u eb[2];
    static_for<40>([&](auto nc) {
      constexpr int n = nc, sg = n >> 2, r = n & 3;
      acc[0][0] = mfma32b(A[n % 3][0], B[n % 3][0], acc[0][0]);
      SCHED();
      if (n < 38) { rdA(n + 2, 0); rdB(n + 2, 0); }
      SCHED();
      acc[0][1] = mfma32b(A[n % 3][0], B[n % 3][1], acc[0][1]);
      SCHED();
      piece(IC<2 * (n - 3)>{});
      if (r == 1 && sg < 9) fl = *(volatile LAS unsigned*)lp(sbase + (E_FULL - CNT) + (sg + 1) * 4);
      if (r == 2 && sg < 7) dn = *(volatile LAS unsigned*)lp(sbase + (E_DONE - CNT) + sg * 4);
      if (n == 34) { dn = *(volatile LAS unsigned*)lp(sbase + (E_LN - CNT)); tag = *(volatile LAS unsigned*)lp(sbase + (W_TAG - CNT)); }
      SCHED();
      cs4 = mfma16b(sel, A[n % 3][0], cs4);
      SCHED();
      acc[1][0] = mfma32b(A[n % 3][1], B[n % 3][0], acc[1][0]);
      SCHED();
      if (n < 38) { rdB(n + 2, 1); rdA(n + 2, 1); }
      if (r == 1) signal_at<E_DONE - CNT + sg * 4>(sbase, one);
      if (n == 39) { sts32(SEXC + w * 128 + (lane & 31) * 4, f2u(cs4[0])); signal(E_SX + w * 4, lane); }
      SCHED();
      acc[1][1] = mfma32b(A[n % 3][1], B[n % 3][1], acc[1][1]);
      SCHED();
      piece(IC<2 * (n - 3) + 1>{});
      if (r == 1 && sg < 9) { CHK(fl, E_FULL + (sg + 1) * 4, 4, tf); if (sg & 1) STAMPL(16 + (sg >> 1)); }
      if (r == 2 && sg < 7) CHK(dn, E_DONE + sg * 4, 4, td);
      if (r == 0 && sg < 9 && sg != 1) {
        if (sg == 0) WAITV(0); else if (sg < 8) TACC(tv, WAITV(8)); else TACC(tv, WAITV(4));
        signal_at<E_FULL - CNT + (sg + 1) * 4>(sbase, one);
      }
      if (n == 29) load_stats();
      if (n == 30) {
#pragma unroll
        for (int i = 0; i < 2; ++i)
          b1v[i] = __builtin_amdgcn_raw_buffer_load_b16(make_rsrc(p.b1, F_ * 2), (lane & 31) * 2, (f0 + 64 * wf + 32 * (i ^ wt)) * 2, 0);
        signal_at<E_VMQ - CNT>(sbase, one);
      }
      if (n == 35) {
        if (__builtin_expect(__builtin_amdgcn_readfirstlane(dn) < 4, 0)) { await(E_LN, 4); tag = peek(W_TAG); }
        tag = __builtin_amdgcn_readfirstlane(tag);
      }
      if (n == 37) {
        if (__builtin_expect(!__all(st[0].y == tag && st[0].w == tag && st[1].y == tag && st[1].w == tag), 0)) {
#pragma unroll 1
          for (unsigned s = 0; s < SPIN_CAP; ++s) {
            asm volatile("" ::: "memory");
            load_stats();
            if (__all(st[0].y == tag && st[0].w == tag && st[1].y == tag && st[1].w == tag)) break;
          }
        }
      }
      if (n == 37 || n == 38) {
        constexpr int i = n == 38;
        const float mu = -u2f(st[i].x), ir = u2f(st[i].z), mh = bfr(mu), ih = bfr(ir);
        eb[i] = h ? (v4u){0u, 0u, 0u, 0u} : (v4u){pkbf(mh, opq(mu - mh)), pkbf(mh, 0.f), pkbf(ih, opq(ir - ih)), 0u};
        asm volatile("" : "+v"(eb[i]));
      }
      SCHED();
    });
    STAMP(3); STAMPC(12); STAMPV(29, tf); STAMPV(30, td); STAMPV(31, tv);
    auto ext = [&](int mi, float S) {
      const float sh = bfr(S);
      const v4u ea = h ? (v4u){0u, 0u, 0u, 0u} : (v4u){pkbf(sh, sh), pkbf(S - sh, 0.f), b1v[mi] | (b1v[mi] << 16), 0u};
      acc[mi][0] = mfma32b(ea, eb[0], acc[mi][0]);
      acc[mi][1] = mfma32b(ea, eb[1], acc[mi][1]);
    };
    fl = peek(E_SX + (w ^ 2) * 4);
    float so = u2f(peek(SEXC + (w ^ 2) * 128 + (lane & 31) * 4));
    ext(0, cs4[0]);
    SCHED();
    if (__builtin_expect(__builtin_amdgcn_readfirstlane(fl) < 1, 0)) {
      await(E_SX + (w ^ 2) * 4, 1);
      so = u2f(peek(SEXC + (w ^ 2) * 128 + (lane & 31) * 4));
    }
    ext(1, so);
    STAMP(1);
    if (f0 + 64 * wf < F_) {
      const unsigned hv = ((lane >> 4) & 1) * 55296 + h * 256 + (lane & 15) * 16;
#pragma unroll
      for (int mi = 0; mi < 2; ++mi) {
#pragma unroll
        for (int ni = 0; ni < 2; ++ni) {
          const float ir = u2f(st[ni].z), rho = __builtin_amdgcn_rcpf(ir);
          const float k1 = -2.302208198f * rho, k2 = k1 * 0.044715f * rho * rho;
          const unsigned hs = WS_H + ((t0 + 64 * wt + 32 * ni) / 16) * 55296 + ((f0 + 64 * wf) / 32 + (mi ^ wt)) * 1024;
#pragma unroll
          for (int hf = 0; hf < 2; ++hf) {
            unsigned o[4];
#pragma unroll
            for (int r = 0; r < 4; ++r) {
              const f2v a = {acc[mi][ni][8 * hf + 2 * r], acc[mi][ni][8 * hf + 2 * r + 1]};
              const f2v x = a * (a * a * k2 + k1);
              const f2v e = {__builtin_amdgcn_exp2f(x.x), __builtin_amdgcn_exp2f(x.y)};
              const f2v dd = e * ir + ir;
              const f2v rr = {__builtin_amdgcn_rcpf(dd.x), __builtin_amdgcn_rcpf(dd.y)};
              const f2v gl = a * rr;
              o[r] = pkbf(gl.x, gl.y);
            }
            __builtin_amdgcn_raw_buffer_store_b128((v4u){o[0], o[1], o[2], o[3]}, ws, HADDR, 0, SC1);
            SCHED();
          }
        }
      }
    }
    signal(E_GELU, lane);
    STAMP(4); STAMPC(13);
    publish(ws, C_H, 4, WS_F2 + (rp * 16 + cp) * 128, tag, lane);
    STAMP(5);
  }

  if (role != 1) {
    const int mh = w & 1, q = role + (w >> 1);
    if (wave == 0) {
      PRIO(0);
      poll_words(ws, WS_F2 + ((tt >> 1) * 16 + min(lane, 13)) * 128, lds_tag());
      signal(E_F2OK, lane);
    } else {
      await<1>(E_F2OK, 1);
    }
    if (role == 0) STAMP(6);
    PRIO(3);
    v4u H[7][2], A[2][5];
    f4v acc[2][5] = {};
    const unsigned hs = WS_H + (4 * tt + 2 * mh) * 55296 + (14 * q - (q == 3)) * 1024;
    const unsigned hv = lane * 16;
    auto ldh = [&](int k, int ti) { H[k % 7][ti] = __builtin_amdgcn_raw_buffer_load_b128(ws, hv, hs + ti * 55296 + k * 1024, SC1); SCHED(); };
#pragma unroll
    for (int k = 0; k < 3; ++k) { ldh(k, 0); ldh(k, 1); }
    if (role == 2) { if (!gemm_wg) await(E_SDR, 4); publish(ws, C_GRP + 96 + 36, 4, WS_F3 + tb * 128, lds_tag(), lane); STAMP(9); }
    const int fo = q < 2 ? 3 * q : 2 + 2 * q;
    v2u xv[3], bv[3];
    unsigned alo = q * 25600 + lane * 16, ahi = SLOT_HI + q * 10240 + lane * 16, fl = 0, sq = sig_base(lane) + q * 4, one = 1u;
    asm volatile("" : "+v"(alo), "+v"(ahi), "+v"(sq), "+v"(one));
    auto rd = [&](int k, int n) { A[k & 1][n] = lds128((k % 7 < 5 ? alo + (k % 7) * 5120 : ahi + (k % 7 - 5) * 5120) + n * 1024); };
    await<1>(E_W0, 4);
#pragma unroll
    for (int n = 0; n < 5; ++n) rd(0, n);
    if (role == 0) { STAMP(7); STAMPC(14); }
    SCHED();
    auto step = [&](auto kc) {
      constexpr int k = kc;
      if (k == 3 || k == 7 || k == 10) PRIO(k == 3 ? 2 : k == 7 ? 1 : 0);
#pragma unroll
      for (int ti = 0; ti < 2; ++ti) {
#pragma unroll
        for (int n = 0; n < 5; ++n) {
          acc[ti][n] = mfma16b(A[k & 1][n], H[k % 7][ti], acc[ti][n]);
          SCHED();
          if (ti == 0 && k < 13) {
            rd(k + 1, n);
            if (n == 4 && k == 0) signal_at<E_QD - CNT>(sq, one);
            if (n == 4 && k == 2) signal_at<E_QD - CNT + 16>(sq, one);
            if (n == 4 && k == 4) signal_at<E_QD - CNT + 32>(sq, one);
            if (n == 4 && k == 5) signal_at<E_QD - CNT + 48>(sq, one);
            if (n == 4 && k == 9) signal_at<E_QD - CNT + 64>(sq, one);
            SCHED();
          }
          if (ti == 1 && n == 0) {
            if (k == 3) fl = peek(E_W1);
            if (k == 5) fl = *(volatile LAS unsigned*)lp(sq + (E_QF - CNT));
            if (k == 7) fl = *(volatile LAS unsigned*)lp(sq + (E_QF - CNT) + 16);
            if (k == 9) fl = *(volatile LAS unsigned*)lp(sq + (E_QF - CNT) + 32);
            if (k == 12) fl = *(volatile LAS unsigned*)lp(sq + (E_QD - CNT) + 64);
            SCHED();
          }
        }
        if (k < 2) { ldh(3 + 2 * k + ti, 0); ldh(3 + 2 * k + ti, 1); }
        if (k >= 1 && k < 8) ldh(k + 6, ti);
        if constexpr (k >= 8 && k < 11) {
          const int f = min(fo + k - 8, 9), tl = f >= 5, n = f - 5 * tl;
          const int l1 = fresh_lane();
          if (ti == 0) xv[k - 8] = __builtin_amdgcn_raw_buffer_load_b64(xr, (l1 & 15) * 1280 + 8 * (l1 >> 4), (64 * tt + 32 * mh + 16 * tl) * 1280 + 160 * ct + 32 * n, 0);
          else bv[k - 8] = __builtin_amdgcn_raw_buffer_load_b64(make_rsrc(p.b2, D_ * 2), 8 * (l1 >> 4), 160 * ct + 32 * n, 0);
        }
        SCHED();
      }
      if (k == 3) check(fl, E_W1, 4);
      if (k == 5) check(fl, E_QF + q * 4, 1);
      if (k == 7) check(fl, E_QF + 16 + q * 4, 1);
      if (k == 9) check(fl, E_QF + 32 + q * 4, 1);
      if (role == 0 && (k & 1)) STAMPL(22 + k / 2);
      SCHED();
    };
    static_for<13>(step);
    if (role == 0) step(IC<13>{});
    if (role == 0) { STAMP(8); STAMPC(15); } else { STAMP(4); }
    check(fl, E_QD + 64 + q * 4, 2);
#pragma unroll
    for (int f = 0; f < 10; ++f) sts128(alo + mh * 10240 + f * 1024, __builtin_bit_cast(v4u, acc[f / 5][f % 5]));
    fl = peek(E_SRDY);
    const int l2 = fresh_lane(), g2 = l2 >> 4;
    group_sync<0>(E_RED + mh * 4, 4, l2);
    check(fl, E_SRDY, 4);
    if (role == 0) STAMP(9);
    auto finish = [&](int i) {
      const int f = fo + i, tl = f >= 5, n = f - 5 * tl;
      const unsigned ra = mh * 10240 + f * 1024 + l2 * 16;
      const f4v p0 = __builtin_bit_cast(f4v, lds128(ra)), p1 = __builtin_bit_cast(f4v, lds128(ra + 25600));
      const f4v p2 = __builtin_bit_cast(f4v, lds128(ra + 51200)), p3 = __builtin_bit_cast(f4v, lds128(ra + 76800));
      const v2u sv = ldstr(STILE + (16 * n + (l2 >> 2)) * 160 + (32 * mh + 16 * tl + 4 * (l2 & 3)) * 2);
      const float bz[4] = {blo(bv[i].x), bhi(bv[i].x), blo(bv[i].y), bhi(bv[i].y)};
      const float sz[4] = {blo(sv.x), bhi(sv.x), blo(sv.y), bhi(sv.y)};
      const float xz[4] = {blo(xv[i].x), bhi(xv[i].x), blo(xv[i].y), bhi(xv[i].y)};
      float r[4];
#pragma unroll
      for (int j = 0; j < 4; ++j) {
        const float z = (p0[j] + p1[j]) + (p2[j] + p3[j]) + bz[j];
        const float gt = z * __builtin_amdgcn_rcpf(1.f + __builtin_amdgcn_exp2f(z * -1.44269504088896f));
        r[j] = fmaf(sz[j], gt, xz[j]);
      }
      __builtin_amdgcn_raw_buffer_store_b64((v2u){pkbf(r[0], r[1]), pkbf(r[2], r[3])}, make_rsrc(p.out, X_BYTES), (l2 & 15) * 1280 + 8 * g2,
                                            (64 * tt + 32 * mh + 16 * tl) * 1280 + 160 * ct + 32 * n, OUT_AUX);
    };
    finish(0); finish(1);
    if (role == 0) finish(2);
    STAMP(10);
    asm volatile(".Lfused_end_b:");
  } else {
    const unsigned wq = WS_W2T + ct * 54 * 5120;
    if (w == 0) { poll_words(ws, WS_FALL + xcd * 256, lds_tag()); signal(E_FWOK, lane); } else { await<1>(E_FWOK, 1); }
    STAMP(5);
    const unsigned wr = wq + (14 * w - (w == 3)) * 5120;
    v4u R[35];
    if (gemm_wg) await<2>(E_VMQ, 4);
#pragma unroll
    for (int i = 0; i < 5; ++i) {
      const int sl = 4 * i + w, qq = sl / 5;
#pragma unroll
      for (int j = 7 * i; j < 7 * i + 7; ++j) {
        R[j] = __builtin_amdgcn_raw_buffer_load_b128(ws, lane * 16 + (j & 3) * 1024, wr + 7 * 5120 + (j >> 2) * 4096, SC1);
        SCHED();
        asm volatile("s_waitcnt vmcnt(%0)" ::"i"(W2_DEPTH) : "memory");
      }
      if (gemm_wg) {
        await<2>(E_DONE + (sl < 6 ? 7 : sl < 12 ? 8 : 9) * 4, 4);
        dma5_lin_sc1(wss, lane * 16, wq + (14 * qq - (qq == 3) + sl % 5) * 5120, sl * 5120);
      }
    }
    STAMP(6);
    if (!gemm_wg) {
      await<2>(E_R1, 8);
#pragma unroll 1
      for (int i = 0; i < 5; ++i) {
        const int sl = 4 * i + w, qq = sl / 5;
        dma5_lin_sc1(wss, lane * 16, wq + (14 * qq - (qq == 3) + sl % 5) * 5120, sl * 5120);
      }
    }
    WAITV(0);
    signal(E_W0, lane);
    STAMP(7);
    await<2>(E_CONV, 4);
    dma5_lin_sc1(wss, lane * 16, wr + 5 * 5120, SLOT_HI + w * 10240);
    dma5_lin_sc1(wss, lane * 16, wr + 6 * 5120, SLOT_HI + w * 10240 + 5120);
    WAITV(0);
    signal(E_W1, lane);
    STAMP(8);
    PRIO(3);
    const unsigned lo = w * 25600 + lane * 16, hi = SLOT_HI + w * 10240 + lane * 16;
    await<1>(E_QD + w * 4, 2);
#pragma unroll
    for (int i = 0; i < 10; ++i) sts128(lo + i * 1024, R[i]);
    signal(E_QF + w * 4, lane);
    await<1>(E_QD + 16 + w * 4, 2);
#pragma unroll
    for (int i = 10; i < 20; ++i) sts128(lo + i * 1024, R[i]);
    signal(E_QF + 16 + w * 4, lane);
    await<1>(E_QD + 32 + w * 4, 2);
#pragma unroll
    for (int i = 20; i < 25; ++i) sts128(lo + i * 1024, R[i]);
#pragma unroll
    for (int i = 25; i < 30; ++i) sts128(hi + (i - 25) * 1024, R[i]);
    await<1>(E_QD + 48 + w * 4, 2);
#pragma unroll
    for (int i = 30; i < 35; ++i) sts128(hi + (i - 25) * 1024, R[i]);
    signal(E_QF + 32 + w * 4, lane);
    STAMP(9);
    if (w == 0) { poll_words(ws, WS_F3 + (ct * 32 + (lane & 31)) * 128, lds_tag()); signal(E_F3OK, lane); } else { await<1>(E_F3OK, 1); }
    v4u sv[3];
#pragma unroll
    for (int i = 0; i < 3; ++i)
      sv[i] = __builtin_amdgcn_raw_buffer_load_b128(ws, (lane >> 3) * 4096 + (lane & 7) * 16, WS_ST + (80 * ct + 8 * min(w + 4 * i, 9)) * 4096 + tt * 128, SC1);
#pragma unroll
    for (int i = 0; i < 3; ++i) sts128(STILE + (8 * min(w + 4 * i, 9) + (lane >> 3)) * 160 + (lane & 7) * 16, sv[i]);
    signal(E_SRDY, lane);
    STAMP(10);
    asm volatile(".Lfused_end_a:");
  }
  STAMP_FLUSH();
}

}

extern "C" void kernel_launch(void* const* d_in, const int* in_sizes, int n_in, void* d_out, int out_size, void* d_ws, size_t ws_size,
                              hipStream_t stream) {
  Params p;
  p.x = (const unsigned short*)d_in[0];
  p.lA = (const unsigned short*)d_in[1];
  p.Ai = (const unsigned short*)d_in[2];
  p.Cr = (const unsigned short*)d_in[3];
  p.Ci = (const unsigned short*)d_in[4];
  p.ldt = (const unsigned short*)d_in[5];
  p.Dsk = (const unsigned short*)d_in[6];
  p.w1 = (const unsigned short*)d_in[8];
  p.b1 = (const unsigned short*)d_in[9];
  p.w2 = (const unsigned short*)d_in[10];
  p.b2 = (const unsigned short*)d_in[11];
  p.out = (unsigned short*)d_out;
  p.ws = (unsigned char*)d_ws;
  (void)hipFuncSetAttribute((const void*)fused_kernel, hipFuncAttributeMaxDynamicSharedMemorySize, LDS_BYTES);
  fused_kernel<<<256, 768, LDS_BYTES, stream>>>(p);
}
```

```cpp
#define PROBE_BLK 9
#define PROBE_WAVE 0
#define PROBE_PT 2
#include <hip/hip_runtime.h>
#include <hip/hip_bf16.h>

namespace {

typedef unsigned v4u __attribute__((ext_vector_type(4)));
typedef unsigned v2u __attribute__((ext_vector_type(2)));
typedef short s4v __attribute__((ext_vector_type(4)));
typedef float f2v __attribute__((ext_vector_type(2)));
typedef float f4v __attribute__((ext_vector_type(4)));
typedef float f16v __attribute__((ext_vector_type(16)));
typedef _Float16 h2v __attribute__((ext_vector_type(2)));
typedef _Float16 h8v __attribute__((ext_vector_type(8)));
typedef __bf16 b2v __attribute__((ext_vector_type(2)));
typedef __bf16 b8v __attribute__((ext_vector_type(8)));
#define GAS __attribute__((address_space(1)))
#define LAS __attribute__((address_space(3)))
#define DEV __device__ __forceinline__
typedef __amdgpu_buffer_rsrc_t rsrc_t;

constexpr int L_ = 2048, D_ = 640, F_ = 1728;
constexpr unsigned X_BYTES = L_ * D_ * 2, W_BYTES = D_ * F_ * 2;
constexpr int SC1 = 16;
constexpr unsigned SPIN_CAP = 1u << 16;

constexpr unsigned WS_EPOCH = 0;
constexpr unsigned WS_FALL = 4096;
constexpr unsigned WS_STAT = 32768;
constexpr unsigned WS_F2 = 65536;
constexpr unsigned WS_F1 = 5013504;
constexpr unsigned WS_FW = WS_F1 + 32768;
constexpr unsigned WS_F3 = WS_FW + 32768;
constexpr unsigned WS_XNT = 131072;
constexpr unsigned WS_W2T = 2752512;
constexpr unsigned WS_H = 5242880;
constexpr unsigned WS_ST = 12582912;
constexpr unsigned WS_STAMP = 15204352;
constexpr unsigned WS_BYTES = WS_STAMP + 256 * 12 * 64 * 8;

constexpr unsigned LDS_BYTES = 163840;
constexpr unsigned UNIT = 16384;
constexpr unsigned KSCR = 8704;
constexpr unsigned FPOS = 2576;
constexpr unsigned GRP = 7 * UNIT;
constexpr unsigned LNST = GRP + 4 * KSCR;
constexpr unsigned CNT = LNST + 10752;
constexpr unsigned SEXC = CNT + 512;
constexpr unsigned JUNK = CNT + 1024;
constexpr unsigned UCP = 2368;
constexpr unsigned CH_R3 = 4 * UCP, CH_U = 9856, CH_BYTES = 2 * KSCR;
constexpr unsigned SLOT_HI = 102400, STILE = 143360;
constexpr unsigned E_FULL = CNT, E_DONE = CNT + 40;
constexpr unsigned E_W0 = CNT + 80, E_W1 = CNT + 84, E_F2OK = CNT + 88, E_F3OK = CNT + 92, E_RED = CNT + 96;
constexpr unsigned E_QD = CNT + 384, E_QF = CNT + 104;
constexpr unsigned E_GELU = CNT + 184, E_LN = CNT + 188, E_LNPUB = CNT + 192, E_SRDY = CNT + 196, E_VMQ = CNT + 200, E_FWOK = CNT + 204, C_FW = CNT + 364, C_AW = CNT + 152, C_AX = CNT + 156, E_R1 = CNT + 160, E_SDR = CNT + 164, W_ISC = CNT + 472;
constexpr unsigned C_H = CNT + 208, C_F1 = CNT + 212, E_F1OK = CNT + 216, E_CONV = CNT + 220, E_SX = CNT + 368, E_RT = CNT + 360;
constexpr unsigned C_GRP = CNT + 224;
constexpr unsigned W_TAG = CNT + 464;
constexpr int N_ZERO = 116;

struct Params {
  const unsigned short *x, *lA, *Ai, *Cr, *Ci, *ldt, *Dsk, *w1, *b1, *w2, *b2;
  unsigned short* out;
  unsigned char* ws;
};

#define STAMP(i) do { if ((i) == PROBE_PT) t_probe = __builtin_amdgcn_s_memrealtime(); } while (0)
#define STAMPC(i) do { } while (0)
#define STAMPV(i, v) do { } while (0)
#define STAMP_INIT() do { } while (0)
#define STAMP_FLUSH() do { } while (0)

#define PRIO(x) __builtin_amdgcn_s_setprio(x)
#define APRIO 0
#define LT0 t0
#define LF0 f0
#undef TACC
#define TACC(acc, stmt) do { stmt; } while (0)
#define STAMPL(i) do { } while (0)
#define CHK(e, a, n, t) check(e, a, n)
#define HADDR (hv + hs + hf * 512)
#define ROTV 0
#define F2_SLEEP 2
#define OUT_AUX 0
#define SLA 1
#define SLL 2
#define FALL_AT 5
#define W2_DEPTH 11
#define BAR() asm volatile("s_waitcnt lgkmcnt(0)\n\ts_barrier" ::: "memory")
#define WAITV(n) asm volatile("s_waitcnt vmcnt(" #n ")" ::: "memory")
#define SCHED() __builtin_amdgcn_sched_barrier(0)

template <int I> struct IC { constexpr operator int() const { return I; } };
template <int N, int I = 0, class F> DEV void static_for(F&& f) {
  if constexpr (I < N) { f(IC<I>{}); static_for<N, I + 1>(f); }
}
DEV int fresh_lane() {
  int l;
  asm volatile("v_mbcnt_lo_u32_b32 %0, -1, 0\n\tv_mbcnt_hi_u32_b32 %0, -1, %0" : "=v"(l));
  return l;
}
DEV float u2f(unsigned u) { return __builtin_bit_cast(float, u); }
DEV unsigned f2u(float f) { return __builtin_bit_cast(unsigned, f); }
DEV h2v u2h(unsigned u) { return __builtin_bit_cast(h2v, u); }
DEV float blo(unsigned u) { return u2f(u << 16); }
DEV float bhi(unsigned u) { return u2f(u & 0xffff0000u); }
DEV unsigned pkbf(float a, float b) { return __builtin_bit_cast(unsigned, __builtin_convertvector((f2v){a, b}, b2v)); }
DEV unsigned pkh(float a, float b) { return __builtin_bit_cast(unsigned, __builtin_convertvector((f2v){a, b}, h2v)); }
DEV float opq(float x) { asm("" : "+v"(x)); return x; }
DEV float bfr(float a) { return blo(pkbf(a, a)); }
DEV f16v mfma32b(v4u a, v4u b, f16v c) { return __builtin_amdgcn_mfma_f32_32x32x16_bf16(__builtin_bit_cast(b8v, a), __builtin_bit_cast(b8v, b), c, 0, 0, 0); }
DEV f16v mfma32h(v4u a, v4u b, f16v c) { return __builtin_amdgcn_mfma_f32_32x32x16_f16(__builtin_bit_cast(h8v, a), __builtin_bit_cast(h8v, b), c, 0, 0, 0); }
DEV f4v mfma16b(v4u a, v4u b, f4v c) { return __builtin_amdgcn_mfma_f32_16x16x32_bf16(__builtin_bit_cast(b8v, a), __builtin_bit_cast(b8v, b), c, 0, 0, 0); }
DEV f4v mfma16h(v4u a, v4u b, f4v c) { return __builtin_amdgcn_mfma_f32_16x16x32_f16(__builtin_bit_cast(h8v, a), __builtin_bit_cast(h8v, b), c, 0, 0, 0); }

DEV LAS unsigned char* lp(unsigned a) { return (LAS unsigned char*)(size_t)a; }
DEV v4u lds128(unsigned a) { return *(LAS v4u*)lp(a); }
DEV v2u lds64(unsigned a) { return *(LAS v2u*)lp(a); }
DEV unsigned lds32(unsigned a) { return *(LAS unsigned*)lp(a); }
DEV void sts128(unsigned a, v4u v) { *(LAS v4u*)lp(a) = v; }
DEV void sts64(unsigned a, v2u v) { *(LAS v2u*)lp(a) = v; }
DEV void sts32(unsigned a, unsigned v) { *(LAS unsigned*)lp(a) = v; }
DEV v2u ldstr(unsigned a) { return __builtin_bit_cast(v2u, __builtin_amdgcn_ds_read_tr16_b64_v4i16((LAS s4v*)lp(a))); }
DEV v4u ldstr2(unsigned a0, unsigned a1) { const v2u x = ldstr(a0), y = ldstr(a1); return (v4u){x.x, x.y, y.x, y.y}; }

DEV v4u make_srd(const void* ptr, unsigned bytes) {
  return (v4u){(unsigned)(size_t)ptr, (unsigned)((size_t)ptr >> 32) & 0xffffu, bytes, 0x00020000u};
}
DEV rsrc_t make_rsrc(const void* ptr, unsigned bytes) { return __builtin_amdgcn_make_buffer_rsrc((void*)ptr, 0, bytes, 0x00020000); }
#define DMA_HEAD "s_mov_b32 m0, %0\n\ts_nop 0\n\t"
DEV void dma1(v4u srd, unsigned v, unsigned soff, unsigned lds) {
  asm volatile(DMA_HEAD "buffer_load_dwordx4 %1, %2, %3 offen lds" ::"s"(lds), "v"(v), "s"(srd), "s"(soff) : "memory");
}
template <int OFF> DEV void dma_next(v4u srd, unsigned v, unsigned soff) {
  asm volatile("buffer_load_dwordx4 %0, %1, %2 offen offset:%3 lds" ::"v"(v), "s"(srd), "s"(soff), "i"(OFF) : "memory");
}
DEV void dma4(v4u srd, const unsigned (&v)[4], unsigned soff, unsigned lds) {
  asm volatile(DMA_HEAD "buffer_load_dwordx4 %1, %5, %6 offen lds\n\tbuffer_load_dwordx4 %2, %5, %6 offen offset:1024 lds\n\t"
               "buffer_load_dwordx4 %3, %5, %6 offen offset:2048 lds\n\tbuffer_load_dwordx4 %4, %5, %6 offen offset:3072 lds"
               ::"s"(lds), "v"(v[0]), "v"(v[1]), "v"(v[2]), "v"(v[3]), "s"(srd), "s"(soff) : "memory");
}
DEV void dma5_lin_sc1(v4u srd, unsigned v, unsigned soff, unsigned lds) {
  asm volatile(DMA_HEAD "buffer_load_dwordx4 %1, %2, %3 offen sc1 lds\n\tbuffer_load_dwordx4 %1, %2, %3 offen offset:1024 sc1 lds\n\t"
               "buffer_load_dwordx4 %1, %2, %3 offen offset:2048 sc1 lds\n\tbuffer_load_dwordx4 %1, %2, %3 offen offset:3072 sc1 lds"
               ::"s"(lds), "v"(v), "s"(srd), "s"(soff) : "memory");
  asm volatile(DMA_HEAD "buffer_load_dwordx4 %1, %2, %3 offen sc1 lds" ::"s"(lds + 4096), "v"(v), "s"(srd), "s"(soff + 4096) : "memory");
}

DEV void poll_words(rsrc_t ws, unsigned off, unsigned tag) {
#pragma unroll 1
  for (unsigned s = 0; s < SPIN_CAP; ++s) {
    asm volatile("" ::: "memory");
    if (__all(__builtin_amdgcn_raw_buffer_load_b32(ws, off, 0, SC1) == tag)) break;
    __builtin_amdgcn_s_sleep(F2_SLEEP);
  }
  asm volatile("" ::: "memory");
}
DEV unsigned sig_base(int lane) { return lane == 0 ? CNT : JUNK + lane * 4; }
template <int OFF> DEV void signal_at(unsigned base, unsigned one) {
  asm volatile("ds_add_u32 %0, %1 offset:%2" ::"v"(base), "v"(one), "i"(OFF) : "memory");
}
DEV void signal(unsigned a, int lane) {
  asm volatile("ds_add_u32 %0, %1" ::"v"(sig_base(lane) + (a - CNT)), "v"(1u) : "memory");
}
DEV unsigned peek(unsigned a) { return *(volatile LAS unsigned*)lp(a); }
template <int SLEEP = 1> DEV void await(unsigned a, unsigned need) {
#pragma unroll 1
  for (unsigned s = 0; s < (SPIN_CAP << 2); ++s) {
    asm volatile("" : "+s"(s));
    if (__builtin_amdgcn_readfirstlane(peek(a)) >= need) break;
    if (SLEEP) __builtin_amdgcn_s_sleep(SLEEP);
  }
  asm volatile("" ::: "memory");
}
DEV void check(unsigned early, unsigned a, unsigned need) {
  if (__builtin_expect(__builtin_amdgcn_readfirstlane(early) < need, 0)) await<1>(a, need);
  asm volatile("" ::: "memory");
}
DEV unsigned lds_arrive(unsigned cnt, int lane) {
  unsigned old;
  asm volatile("ds_add_rtn_u32 %0, %1, %2\n\ts_waitcnt lgkmcnt(0)" : "=&v"(old) : "v"(sig_base(lane) + (cnt - CNT)), "v"(1u) : "memory");
  return __builtin_amdgcn_readfirstlane(old);
}
template <int SLEEP = 1> DEV void group_sync(unsigned cnt, unsigned target, int lane) {
  if (lds_arrive(cnt, lane) + 1 < target) await<SLEEP>(cnt, target);
}
DEV void publish(rsrc_t ws, unsigned cnt, unsigned nwaves, unsigned flag_off, unsigned tag, int lane) {
  WAITV(0);
  if (lds_arrive(cnt, lane) + 1 == nwaves && lane == 0) __builtin_amdgcn_raw_buffer_store_b32(tag, ws, flag_off, 0, SC1);
}
DEV unsigned epoch_tag(unsigned ev, bool share, int lane) {
  const unsigned e = __builtin_amdgcn_readfirstlane(ev);
  const unsigned tag = e * 0x9E3779B1u + 0x7F4A7C15u;
  if (share && lane == 0) sts64(W_TAG, (v2u){tag, e});
  return tag;
}
DEV unsigned lds_tag() { return __builtin_amdgcn_readfirstlane(lds32(W_TAG)); }

template <int CTRL> DEV float dpp_add(float v) {
  return v + u2f(__builtin_amdgcn_update_dpp(0u, f2u(v), CTRL, 0xf, 0xf, false));
}
template <int CTRL> DEV float dpp_max(float v) {
  return fmaxf(v, u2f(__builtin_amdgcn_update_dpp(0u, f2u(v), CTRL, 0xf, 0xf, false)));
}
DEV float half_sum(float v) {
  v = dpp_add<0x128>(v); v = dpp_add<0x124>(v); v = dpp_add<0x122>(v); v = dpp_add<0x121>(v);
  const auto r = __builtin_amdgcn_permlane16_swap(f2u(v), f2u(v), false, false);
  return u2f(r[0]) + u2f(r[1]);
}
DEV float wave_max(float v) {
  v = dpp_max<0x128>(v); v = dpp_max<0x124>(v); v = dpp_max<0x122>(v); v = dpp_max<0x121>(v);
  const auto r = __builtin_amdgcn_permlane16_swap(f2u(v), f2u(v), false, false);
  v = fmaxf(u2f(r[0]), u2f(r[1]));
  const auto q = __builtin_amdgcn_permlane32_swap(f2u(v), f2u(v), false, false);
  return fmaxf(u2f(q[0]), u2f(q[1]));
}

struct LnIn { v2u v[5]; unsigned e; v4u b[4]; };
DEV LnIn ln_load(v4u xs, v4u wss, v4u w2s, int token, int blk, int lane) {
  LnIn r;
  const unsigned a = token * 1280 + (lane & 31) * 8, b = (blk / 10) * 40960 + (blk % 10) * 128 + (lane >> 3) * 1280 + (lane & 7) * 16;
  asm volatile("buffer_load_dwordx2 %0, %6, %7, 0 offen\n\tbuffer_load_dwordx2 %1, %6, %7, 0 offen offset:256\n\t"
               "buffer_load_dwordx2 %2, %6, %7, 0 offen offset:512\n\tbuffer_load_dwordx2 %3, %6, %7, 0 offen offset:768\n\t"
               "buffer_load_dwordx2 %4, %6, %7, 0 offen offset:1024\n\tbuffer_load_dword %5, off, %8, 0 sc1"
               : "=&v"(r.v[0]), "=&v"(r.v[1]), "=&v"(r.v[2]), "=&v"(r.v[3]), "=&v"(r.v[4]), "=&v"(r.e) : "v"(a), "s"(xs), "s"(wss) : "memory");
  asm volatile("buffer_load_dwordx4 %0, %4, %8, 0 offen\n\tbuffer_load_dwordx4 %1, %5, %8, 0 offen\n\t"
               "buffer_load_dwordx4 %2, %6, %8, 0 offen\n\tbuffer_load_dwordx4 %3, %7, %8, 0 offen"
               : "=&v"(r.b[0]), "=&v"(r.b[1]), "=&v"(r.b[2]), "=&v"(r.b[3]) : "v"(b), "v"(b + 10240), "v"(b + 20480), "v"(b + 30720), "s"(w2s) : "memory");
  return r;
}
template <int N> DEV void ln_wait(LnIn& r) {
  asm volatile("s_waitcnt vmcnt(%10)" : "+v"(r.v[0]), "+v"(r.v[1]), "+v"(r.v[2]), "+v"(r.v[3]), "+v"(r.v[4]), "+v"(r.e), "+v"(r.b[0]), "+v"(r.b[1]),
               "+v"(r.b[2]), "+v"(r.b[3]) : "i"(N) : "memory");
}
DEV f2v ln_compute(const LnIn& in, int row, int lane) {
  float xv[20], s = 0.f, q = 0.f;
#pragma unroll
  for (int m = 0; m < 5; ++m) { xv[4 * m] = blo(in.v[m].x); xv[4 * m + 1] = bhi(in.v[m].x); xv[4 * m + 2] = blo(in.v[m].y); xv[4 * m + 3] = bhi(in.v[m].y); }
#pragma unroll
  for (int m = 0; m < 20; ++m) { s = opq(s + xv[m]); q = opq(fmaf(xv[m], xv[m], q)); }
  s = half_sum(s); q = half_sum(q);
  const float mean = s * (1.f / 640.f);
  const float var = fmaxf(fmaf(-mean, mean, q * (1.f / 640.f)), 0.f);
  const float m1 = bfr(mean), s1 = bfr(__builtin_amdgcn_sqrtf(bfr(bfr(var) + 1e-5f)));
  const float r1 = __builtin_amdgcn_rcpf(s1), c = -m1 * r1;
#pragma unroll
  for (int m = 0; m < 5; ++m) {
    const v2u o = {pkh(opq(fmaf(xv[4 * m], r1, c)), opq(fmaf(xv[4 * m + 1], r1, c))), pkh(opq(fmaf(xv[4 * m + 2], r1, c)), opq(fmaf(xv[4 * m + 3], r1, c)))};
    sts64(LNST + row * 1344 + (lane & 31) * 8 + m * 256, o);
  }
  const float m2 = bfr((mean - m1) * r1), s2 = bfr(__builtin_amdgcn_sqrtf(bfr(bfr(var * r1 * r1) + 1e-5f)));
  return (f2v){opq(fmaf(m2, s1, m1)), opq(s1 * s2)};
}
DEV void ln_publish(rsrc_t ws, int tb, int w, int lane) {
  const unsigned a = LNST + ((lane & 15) >> 2) * 1344 + (lane >> 4) * 32 + (lane & 3) * 8 + w * 128;
  const unsigned o = WS_XNT + tb * 10240 + (w * 64 + lane) * 16;
  const v4u v0 = ldstr2(a, a + 4 * 1344), v1 = ldstr2(a + 512, a + 512 + 4 * 1344), v2 = ldstr2(a + (w < 2 ? 1024 : 0), a + (w < 2 ? 1024 : 0) + 4 * 1344);
  signal(E_LNPUB, lane);
  __builtin_amdgcn_raw_buffer_store_b128(v0, ws, o, 0, SC1);
  __builtin_amdgcn_raw_buffer_store_b128(v1, ws, o + 4096, 0, SC1);
  if (w < 2) __builtin_amdgcn_raw_buffer_store_b128(v2, ws, o + 8192, 0, SC1);
}

struct Cx { float r, i; };
DEV Cx cmul(Cx a, Cx b) { return {opq(fmaf(a.r, b.r, -a.i * b.i)), opq(fmaf(a.r, b.i, a.i * b.r))}; }
DEV Cx csqr(Cx a) { return {opq(fmaf(a.r, a.r, -a.i * a.i)), opq((a.r + a.r) * a.i)}; }
DEV unsigned ch(Cx a) { return pkh(a.r, a.i); }
DEV unsigned hmul(unsigned a, unsigned b) {
  unsigned t, r;
  asm("v_pk_mul_f16 %0, %1, %2 op_sel:[0,0] op_sel_hi:[0,1]" : "=v"(t) : "v"(a), "v"(b));
  asm("v_pk_fma_f16 %0, %1, %2, %3 op_sel:[1,1,0] op_sel_hi:[1,0,1] neg_lo:[0,1,0]" : "=v"(r) : "v"(a), "v"(b), "v"(t));
  return r;
}
DEV float exp_bf(float x) {
  const float t = x * 1.44268798828125f, lo = x * 7.0526077138765e-06f;
  const float n = __builtin_rintf(t);
  return bfr(__builtin_amdgcn_ldexpf(__builtin_amdgcn_exp2f((t - n) + lo), (int)fminf(fmaxf(n, -200.f), 200.f)));
}
DEV Cx cexp_at(float lg, float rh, float rl, float P) {
  const float m = __builtin_amdgcn_exp2f(lg * P), ph = fmaf(rl, P, __builtin_amdgcn_fractf(rh * P));
  return {opq(m * __builtin_amdgcn_cosf(ph)), opq(m * __builtin_amdgcn_sinf(ph))};
}
struct KgIn { unsigned lA, Ai, Cr, Ci; float dt, dsk; };
DEV KgIn kg_load(const Params& p, int d, int lane) {
  KgIn k;
  k.lA = ((const GAS unsigned*)p.lA)[d * 64 + lane]; k.Ai = ((const GAS unsigned*)p.Ai)[d * 64 + lane];
  k.Cr = ((const GAS unsigned*)p.Cr)[d * 64 + lane]; k.Ci = ((const GAS unsigned*)p.Ci)[d * 64 + lane];
  k.dt = u2f((unsigned)((const GAS unsigned short*)p.ldt)[d] << 16);
  k.dsk = u2f((unsigned)((const GAS unsigned short*)p.Dsk)[d] << 16);
  return k;
}
DEV float kg_wave(const KgIn& k, int g, unsigned scr, unsigned fp, unsigned busy, int lane) {
  const float dt = exp_bf(k.dt), c2 = dt + dt;
  const float mx = wave_max(fmaxf(fmaxf(fabsf(blo(k.Cr)), fabsf(bhi(k.Cr))), fmaxf(fabsf(blo(k.Ci)), fabsf(bhi(k.Ci))))) * c2;
  int ex = __builtin_amdgcn_frexp_expf(fmaxf(mx, fabsf(k.dsk) * 0x1p-10f));
  ex = ex < -120 ? -120 : (ex > 120 ? 120 : ex);
  const float cs = c2 * __builtin_amdgcn_ldexpf(1.f, -ex);
  const int sh = g ? 0 : 16;
  const float lg = -(exp_bf(bhi(k.lA << sh)) * dt) * 1.44269504088896f, ai = bhi(k.Ai << sh) * dt;
  const Cx c0 = {opq(bhi(k.Cr << sh) * cs), opq(-bhi(k.Ci << sh) * cs)};
  const float rh = ai * 0.15915494f, rl = fmaf(ai, 0.15915494f, -rh) + ai * 6.4206382e-09f;
  const Cx v1 = cexp_at(lg, -rh, -rl, 1.f), w1 = cexp_at(lg, rh, rl, 8.f), G = cexp_at(lg, -rh, -rl, 256.f);
  const unsigned wa = scr + lane * 4, ra = scr + (lane & 31) * 272 + (lane >> 5) * 16;
  {
    const Cx w2 = csqr(w1), w3 = cmul(w2, w1), w4 = csqr(w2), w8 = csqr(w4), w12 = cmul(w8, w4), w16 = csqr(w8);
    const Cx w20 = cmul(w16, w4), w24 = csqr(w12), w28 = cmul(w24, w4);
    const unsigned o[3] = {ch(w1), ch(w2), ch(w3)};
    const unsigned bs[7] = {ch(w4), ch(w8), ch(w12), ch(w16), ch(w20), ch(w24), ch(w28)};
    await(E_RT, busy);
    sts32(wa, 0x00003c00u);
#pragma unroll
    for (int j = 0; j < 3; ++j) sts32(wa + (1 + j) * 272, o[j]);
#pragma unroll
    for (int b = 0; b < 7; ++b) {
      sts32(wa + (4 + 4 * b) * 272, bs[b]);
#pragma unroll
      for (int j = 0; j < 3; ++j) sts32(wa + (5 + 4 * b + j) * 272, hmul(bs[b], o[j]));
    }
  }
  asm volatile("" ::: "memory");
  v4u y[8];
#pragma unroll
  for (int s = 0; s < 8; ++s) y[s] = lds128(ra + s * 32);
  asm volatile("" ::: "memory");
  {
    const Cx v2 = csqr(v1), v3 = cmul(v2, v1), v4 = csqr(v2), v6 = csqr(v3), x4 = cmul(c0, v4);
    const unsigned h1 = ch(v1), h2 = ch(v2), h3 = ch(v3), h4 = ch(v4), h6 = ch(v6), hx0 = ch(c0), hx4 = ch(x4);
    sts32(wa + 0 * 272, hx0); sts32(wa + 1 * 272, hmul(hx0, h1)); sts32(wa + 2 * 272, hmul(hx0, h2)); sts32(wa + 3 * 272, hmul(hx0, h3));
    sts32(wa + 4 * 272, hx4); sts32(wa + 5 * 272, hmul(hx4, h1)); sts32(wa + 6 * 272, hmul(hx4, h2)); sts32(wa + 7 * 272, hmul(hx4, h3));
    const Cx d1 = cmul(c0, G), d2 = cmul(d1, G), d3 = cmul(d2, G), d4 = cmul(d3, G);
    const Cx G4 = csqr(csqr(G));
    const Cx d5 = cmul(d1, G4), d6 = cmul(d2, G4), d7 = cmul(d3, G4), d8 = cmul(d4, G4);
    const unsigned e1 = ch(d1), e2 = ch(d2), e3 = ch(d3);
    sts32(wa + 8 * 272, e1); sts32(wa + 9 * 272, hmul(e1, h2)); sts32(wa + 10 * 272, hmul(e1, h4)); sts32(wa + 11 * 272, hmul(e1, h6));
    sts32(wa + 12 * 272, e2); sts32(wa + 13 * 272, hmul(e2, h4)); sts32(wa + 14 * 272, e3); sts32(wa + 15 * 272, hmul(e3, h4));
    sts32(wa + 16 * 272, ch(d4)); sts32(wa + 17 * 272, ch(d5)); sts32(wa + 18 * 272, ch(d6)); sts32(wa + 19 * 272, ch(d7));
    sts32(wa + 20 * 272, ch(d8));
  }
  asm volatile("" ::: "memory");
  v4u xx[8];
#pragma unroll
  for (int s = 0; s < 8; ++s) xx[s] = lds128(ra + s * 32);
  SCHED();
  f16v acc = {};
#pragma unroll
  for (int s = 0; s < 8; ++s) acc = mfma32h(xx[s], y[s], acc);
  const int j = lane & 31, h = lane >> 5;
  await(E_LNPUB, busy);
  if (g == 0 && lane == 0) acc[0] += __builtin_amdgcn_ldexpf(k.dsk, -ex);
  sts128(fp + (8 * j + 4 * h) * 4, (v4u){f2u(acc[0]), f2u(acc[1]), f2u(acc[2]), f2u(acc[3])});
  sts64(fp + (h ? 384 + 2 * j : 256 + 4 * j) * 4, (v2u){f2u(acc[4]), f2u(acc[5])});
  sts64(fp + (h ? 448 + 2 * j : 258 + 4 * j) * 4, (v2u){f2u(acc[6]), f2u(acc[7])});
  if (lane <= 32) sts32(fp + (512 + 128 * h + j) * 4, f2u(acc[8]));
  if (h == 0) {
#pragma unroll
    for (int i = 1; i < 4; ++i) sts32(fp + (512 + j + 32 * i) * 4, f2u(acc[8 + i]));
  }
  return __builtin_amdgcn_ldexpf(1.f, ex);
}
DEV void kg_table0(unsigned cb, unsigned fp, int g, int lane) {
  auto f1 = [&](int i) { return opq(u2f(lds32(fp + i * 4)) + u2f(lds32(fp + FPOS + i * 4))); };
  if (g == 0) {
    const int j = lane & 31, h = lane >> 5;
    const unsigned a = fp + (h ? 256 + 4 * j : 8 * j) * 4;
    const v4u l0 = lds128(a), h0 = lds128(a + 16), l1 = lds128(a + FPOS), h1 = lds128(a + FPOS + 16);
    const float e0 = opq(u2f(l0.x) + u2f(l1.x)), e1 = opq(u2f(l0.y) + u2f(l1.y)), e2 = opq(u2f(l0.z) + u2f(l1.z)), e3 = opq(u2f(l0.w) + u2f(l1.w));
    const float e4 = opq(u2f(h0.x) + u2f(h1.x)), e5 = opq(u2f(h0.y) + u2f(h1.y)), e6 = opq(u2f(h0.z) + u2f(h1.z)), e7 = opq(u2f(h0.w) + u2f(h1.w));
    const v4u o0 = {pkh(e0, e1), pkh(e2, e3), pkh(e4, e5), pkh(e6, e7)};
    const v4u o1 = {pkh(e0, e0), pkh(e1, e2), pkh(e2, e2), pkh(e3, e4)};
    sts128(cb + 32 + lane * 16, h ? o1 : o0);
    const float fa = f1(512), fb = f1(513);
    const unsigned aa = pkh(fa, fa), bb = pkh(fb, fb);
    if (lane < 4) sts128(cb + 32 + (128 + lane) * 16, lane == 0 ? (v4u){aa, aa, pkh(fa, fb), bb} : lane == 1 ? (v4u){bb, bb, 0u, 0u} : (v4u){0u, 0u, 0u, 0u});
  } else {
    const float a = f1(384 + 2 * lane), b = f1(385 + 2 * lane), c = f1(386 + 2 * lane);
    sts128(cb + 32 + (64 + lane) * 16, (v4u){pkh(a, a), pkh(a, b), pkh(b, b), pkh(c, c)});
    const float lo = f1(640 - 2 * lane), hi = f1(lane ? 641 - 2 * lane : 640);
    sts32(cb + CH_R3 + lane * 4, pkh(lane ? hi : 0.f, lo));
    if (lane < 16) sts32(cb + CH_R3 + 256 + lane * 4, 0u);
    if (lane < 32) sts32(cb + (lane >> 3) * UCP + (lane & 7) * 4, 0u);
  }
}
DEV void kg_copies(unsigned cb, int g, int lane) {
  auto one = [&](int chunk) {
    const unsigned o = cb + 32 + chunk * 16;
    const v2u pv = lds64(o - 8);
    const v4u e = lds128(o);
    const unsigned a0 = __builtin_amdgcn_alignbit(pv.y, pv.x, 16), a1 = __builtin_amdgcn_alignbit(e.x, pv.y, 16), a2 = __builtin_amdgcn_alignbit(e.y, e.x, 16),
                   a3 = __builtin_amdgcn_alignbit(e.z, e.y, 16), a4 = __builtin_amdgcn_alignbit(e.w, e.z, 16);
    sts128(o + UCP, (v4u){a1, a2, a3, a4});
    sts128(o + 2 * UCP, (v4u){pv.y, e.x, e.y, e.z});
    sts128(o + 3 * UCP, (v4u){a0, a1, a2, a3});
  };
  one(64 * g + lane);
  if (lane < 2) one(128 + 2 * g + lane);
}
DEV void conv_load_u(rsrc_t ws, unsigned grp, int d0, int w, int lane) {
  v4u v[2];
#pragma unroll
  for (int i = 0; i < 2; ++i)
    v[i] = __builtin_amdgcn_raw_buffer_load_b128(ws, (lane >> 1) * 10240 + (lane & 1) * 16, WS_XNT + (64 * w + 32 * i) * 10240 + d0 * 16, SC1);
  if (lane < 16) sts128(grp + (w >> 1) * CH_BYTES + CH_U + (w & 1) * 256 + lane * 16, (v4u){0u, 0u, 0u, 0u});
#pragma unroll
  for (int i = 0; i < 2; ++i) sts128(grp + (lane & 1) * CH_BYTES + CH_U + 512 + (64 * w + 32 * i + (lane >> 1)) * 16, v[i]);
}
template <class F1>
DEV float ssm_prepare(rsrc_t ws, const KgIn& k, unsigned grp, unsigned fp, unsigned busy, unsigned cnt, int d0, int w, int lane, F1 f1, unsigned isc_out = 0) {
  const int ci = w >> 1, g = w & 1;
  const float isc = kg_wave(k, g, grp + w * KSCR, fp + w * FPOS, busy, lane);
  if (isc_out && grp == GRP) sts32(isc_out + ci * 4, f2u(isc));
  group_sync(cnt, 4, lane);
  kg_table0(grp + ci * CH_BYTES, fp + 2 * ci * FPOS, g, lane);
  group_sync(cnt + 4 + ci * 4, 2, lane);
  kg_copies(grp + ci * CH_BYTES, g, lane);
  f1();
  conv_load_u(ws, grp, d0, w, lane);
  group_sync(cnt + 12, 4, lane);
  return isc;
}

template <int J0, int NJ, class Go, class Done>
DEV void conv_tiles(rsrc_t ws, unsigned cb, int d, float isc, int lane, Go go, Done done) {
  constexpr int B0 = J0 > 3 ? J0 - 3 : 0, NB = J0 + NJ - B0;
  const int g = lane >> 4, c16 = lane & 15;
  const unsigned ga = cb + (c16 >> 2) * UCP + 64 - 16 * g + 8 * (lane & 3);
  const unsigned ba = cb + CH_U + 480 + 32 * c16 + 16 * g;
  f4v acc[NJ] = {};
  {
    unsigned w7[4], w9[4];
#pragma unroll
    for (int i = 0; i < 4; ++i) {
      const int k0 = 8 * g + 2 * i - c16;
      w7[i] = ((k0 >= 5 && k0 <= 11) ? 0x3c00u : 0u) | ((k0 >= 4 && k0 <= 10) ? 0x3c000000u : 0u);
      w9[i] = ((k0 >= 4 && k0 <= 12) ? 0x3c00u : 0u) | ((k0 >= 3 && k0 <= 11) ? 0x3c000000u : 0u);
    }
    const v4u W7 = {w7[0], w7[1], w7[2], w7[3]}, W9 = {w9[0], w9[1], w9[2], w9[3]};
    const unsigned r3 = cb + CH_R3 + (127 - c16 + 4 * g) * 4;
    const v4u g4 = ldstr2(ga + 2048, ga + 2048 - 8);
#pragma unroll
    for (int b = 0; b + 4 < J0 + NJ; ++b) {
      const f4v b7 = mfma16h(lds128(ba - 32 + 512 * b), W7, (f4v){}), b9 = mfma16h(lds128(ba - 16 + 512 * b), W9, (f4v){});
      const v4u bx = {pkh(b7[0], b9[0]), pkh(b7[1], b9[1]), pkh(b7[2], b9[2]), pkh(b7[3], b9[3])};
#pragma unroll
      for (int j = (b + 4 > J0 ? b + 4 : J0); j < J0 + NJ; ++j) {
        const unsigned a = r3 - 64 * (j - b);
        acc[j - J0] = mfma16h(bx, (v4u){lds32(a), lds32(a + 4), lds32(a + 8), lds32(a + 12)}, acc[j - J0]);
      }
      if (b + 4 >= J0) acc[b + 4 - J0] = mfma16h(g4, lds128(ba + 512 * b), acc[b + 4 - J0]);
    }
  }
  v4u G[2][4], B[2][NB];
  auto rd = [&](int e, int i) {
    constexpr int top = NJ;
    if (i == 0) G[e & 1][0] = ldstr2(ga + 64 * e, ga + 64 * e - 8);
    else if (i <= top) B[e & 1][NB - top + i - 1] = lds128(ba + 512 * (B0 + NB - top + i - 1) - 64 * e);
    else {
      const int k = i - top - 1;
      if (NB == top || (k & 1) == 0) { const int c = NB == top ? k + 1 : k / 2 + 1; G[e & 1][c] = ldstr2(ga + 512 * c + 64 * e, ga + 512 * c + 64 * e - 8); }
      else { const int bi = NB - top - 1 - k / 2; B[e & 1][bi] = lds128(ba + 512 * (B0 + bi) - 64 * e); }
    }
  };
#pragma unroll
  for (int i = 0; i < 4 + NB; ++i) rd(0, i);
  SCHED();
  go();
#pragma unroll
  for (int e = 0; e < 8; ++e) {
    int m = 0;
#pragma unroll
    for (int c = 0; c < 4; ++c)
#pragma unroll
      for (int j = (c > J0 ? c : J0); j < J0 + NJ; ++j) {
        acc[j - J0] = mfma16h(G[e & 1][c], B[e & 1][j - c - B0], acc[j - J0]);
        if (e < 7 && m < 4 + NB) rd(e + 1, m);
        if (e == 6 && m == 3 + NB) done();
        ++m;
        SCHED();
      }
  }
  unsigned pk[NJ][2];
#pragma unroll
  for (int j = 0; j < NJ; ++j) { pk[j][0] = pkbf(opq(acc[j][0] * isc), opq(acc[j][1] * isc)); pk[j][1] = pkbf(opq(acc[j][2] * isc), opq(acc[j][3] * isc)); }
  const unsigned so = WS_ST + d * 4096 + J0 * 512;
#pragma unroll
  for (int j = 0; j + 1 < NJ; j += 2) {
    const auto x = __builtin_amdgcn_permlane16_swap(pk[j][0], pk[j + 1][0], false, false);
    const auto y = __builtin_amdgcn_permlane16_swap(pk[j][1], pk[j + 1][1], false, false);
    __builtin_amdgcn_raw_buffer_store_b128((v4u){x[0], y[0], x[1], y[1]}, ws, so + (j + (g & 1)) * 512 + 32 * c16 + 16 * (g >> 1), 0, SC1);
  }
  __builtin_amdgcn_raw_buffer_store_b64((v2u){pk[NJ - 1][0], pk[NJ - 1][1]}, ws, so + (NJ - 1) * 512 + 32 * c16 + 8 * g, 0, SC1);
}
template <class Go, class Done>
DEV void conv_channel(rsrc_t ws, unsigned cb, int d, int half, float isc, int lane, Go go, Done done) {
  if (half) conv_tiles<5, 3>(ws, cb, d, isc, lane, go, done);
  else conv_tiles<0, 5>(ws, cb, d, isc, lane, go, done);
}

DEV void retile(rsrc_t ws, int blk, const v4u (&rows)[4], unsigned stg, int lane) {
#pragma unroll
  for (int j = 0; j < 4; ++j) sts128(stg + j * 1024 + (lane >> 3) * 128 + 16 * ((lane & 7) ^ (((lane >> 4) & 3) << 1)), rows[j]);
  asm volatile("" ::: "memory");
  const int g = lane >> 4, pp = lane & 3, row = 16 * (g >> 1) + 4 * (g & 1) + ((lane & 15) >> 2);
  v4u f[4];
#pragma unroll
  for (int j = 0; j < 4; ++j) {
    const unsigned a = stg + row * 128 + 16 * ((2 * j + (pp >> 1)) ^ (((row >> 1) & 3) << 1)) + 8 * (pp & 1);
    f[j] = ldstr2(a, a + 1024);
  }
  signal(E_RT, lane);
#pragma unroll
  for (int j = 0; j < 4; ++j) {
    const int ng = 4 * (blk % 10) + j;
    __builtin_amdgcn_raw_buffer_store_b128(f[j], ws, lane * 16 + WS_W2T + (((ng / 5) * 54 + blk / 10) * 5 + ng % 5) * 1024, 0, SC1);
  }
}

__global__ __launch_bounds__(768, 3) void fused_kernel(Params p) {
  extern __shared__ __attribute__((aligned(16))) unsigned char lds_base[];
  unsigned long long pc;
  unsigned code_bytes;
  asm volatile("s_getpc_b64 %0\n.Lfused_begin:\n\ts_mov_b32 %1, .Lfused_end_a-.Lfused_begin\n\ts_max_i32 %1, %1, .Lfused_end_b-.Lfused_begin"
               : "=s"(pc), "=s"(code_bytes) : : "scc");
  const int lane = threadIdx.x & 63, wave = __builtin_amdgcn_readfirstlane(threadIdx.x >> 6), role = wave >> 2, w = wave & 3;
  const int xcd = blockIdx.x & 7, slot = blockIdx.x >> 3, tb = 32 * xcd + slot;
  const bool gemm_wg = slot < 28;
  const int rp = 4 * (xcd >> 1) + slot / 7, cp = 7 * (xcd & 1) + slot % 7, t0 = 128 * rp, f0 = 128 * cp;
  const unsigned long long t_entry = __builtin_amdgcn_s_memrealtime();
  unsigned long long t_probe = t_entry;
  STAMP_INIT();
  STAMP(0);
  asm volatile("" ::"v"((LAS unsigned char*)lds_base));

  const unsigned vx0 = (lane >> 3) * 1280 + 16 * ((lane & 7) ^ (lane >> 4));
  const unsigned wc = (lane & 15) ^ ((lane >> 4) << 2);
  const unsigned vw0 = (cp == 13 && wc >= 8) ? 0x40000000u : (lane >> 4) * 3456 + 16 * wc;
  const unsigned vx[4] = {vx0, (vx0 ^ 64) + 9216, vx0 + 2 * 9216, (vx0 ^ 64) + 3 * 9216};
  const unsigned vw[4] = {vw0, vw0 + 12800, vw0 + 2 * 12800, vw0 + 3 * 12800};
  auto stage_x = [&](int i) { dma4(make_srd(p.x, X_BYTES), vx, (LT0 + 32 * w) * 1280 + 128 * ((i + ROTV) % 10), ((2 * i) % 7) * UNIT + w * 4096); };
  auto stage_w = [&](int i) { dma4(make_srd(p.w1, W_BYTES), vw, (64 * ((i + ROTV) % 10) + 16 * w) * 3456 + 2 * LF0, ((2 * i + 1) % 7) * UNIT + w * 4096); };
  const rsrc_t ws = make_rsrc(p.ws, WS_BYTES), xr = make_rsrc(p.x, X_BYTES);
  const v4u wss = make_srd(p.ws, WS_BYTES);
  const bool last_pair = role == (gemm_wg ? 2 : 1);
  const unsigned grp = last_pair ? GRP : (role >> 1) * (4 * KSCR + 4 * FPOS), fpos = last_pair ? LNST : grp + 4 * KSCR, cnt = C_GRP + role * 48;
  auto stage2_or_skip = [&] {
    dma4(make_srd(p.x, gemm_wg ? X_BYTES : 0u), vx, (LT0 + 32 * w) * 1280 + 128 * 2, gemm_wg ? 4 * UNIT + w * 4096 : 98304u);
    dma4(make_srd(p.w1, gemm_wg ? W_BYTES : 0u), vw, (64 * 2 + 16 * w) * 3456 + 2 * LF0, gemm_wg ? 5 * UNIT + w * 4096 : 98304u);
  };
  if (role == 0 && gemm_wg) { stage_x(0); stage_w(0); }
  if (wave >= 10) dma1(make_srd((const void*)pc, code_bytes & ~15u), lane * 16, (2 * slot + wave - 10) * 1024, JUNK);

  const int dg = 80 * xcd + (gemm_wg ? 2 * slot : 56 + 6 * (slot - 28) + 2 * role);
  const int tt = 4 * xcd + (slot >> 3), ct = slot & 7;
  if (threadIdx.x < N_ZERO) sts32(CNT + threadIdx.x * 4, 0u);

  if (role == 2) {
    const KgIn kin = kg_load(p, dg + (w >> 1), lane);
    BAR();
    float isc = ssm_prepare(ws, kin, grp, fpos, gemm_wg ? 4 : 0, cnt, dg, w, lane, [&] {
      STAMP(2);
      if (gemm_wg && w == 0) {
        await<SLA>(E_VMQ, 4);
        poll_words(ws, WS_FALL + xcd * 256 + 128, lds_tag());
        signal(E_F1OK, lane);
        if (blockIdx.x == 0 && lane == 0) __builtin_amdgcn_raw_buffer_store_b32(lds32(W_TAG + 4) + 1u, ws, WS_EPOCH, 0, SC1);
      } else {
        await<SLA>(E_F1OK, 1);
      }
      STAMP(3);
    });
    STAMP(6);
    unsigned cb = grp + (w >> 1) * CH_BYTES;
    int d = dg + (w >> 1);
#pragma unroll 1
    for (int last = gemm_wg; last < 2; ++last) {
      conv_channel(ws, cb, d, w & 1, isc, lane, [&] { if (gemm_wg) await(E_GELU, 4); STAMP(7); }, [&] { signal(last ? E_CONV : E_R1, lane); });
      if (!last) {
        await(C_GRP + 48 + 12, 4);
        cb = GRP + (w >> 1) * CH_BYTES;
        d -= 2;
        isc = u2f(peek(W_ISC + (w >> 1) * 4));
        STAMP(5);
      }
    }
    STAMP(8);
  }

  if (role == 1) {
    const int token = 8 * tb + 2 * w + (lane >> 5);
    PRIO(3);
    const int fi = 4 * xcd + slot - 28, blk = w < 2 ? 2 * tb + w : (!gemm_wg && w == 2 && fi < 28) ? 512 + fi : -1;
    LnIn ln = ln_load(make_srd(p.x, X_BYTES), wss, make_srd(p.w2, blk < 0 ? 0u : W_BYTES), token, max(blk, 0), lane);
    stage2_or_skip();
    BAR();
    ln_wait<8>(ln);
    const unsigned tag = epoch_tag(ln.e, w == 0, lane);
    if (blk >= 0) retile(ws, blk, ln.b, GRP + w * KSCR, lane); else signal(E_RT, lane);
    STAMP(1);
    const f2v stat = ln_compute(ln, 2 * w + (lane >> 5), lane);
    signal(E_LN, lane);
    publish(ws, C_FW, 4, WS_FW + tb * 128, tag, lane);
    signal(E_FULL + 8, lane);
    STAMP(2);
    if ((lane & 31) == 0) __builtin_amdgcn_raw_buffer_store_b128((v4u){f2u(stat.x), tag, f2u(stat.y), tag}, ws, WS_STAT + token * 16, 0, SC1);
    await(E_LN, 4);
    ln_publish(ws, tb, w, lane);
    STAMP(3);
    PRIO(0);
    publish(ws, C_F1, 4, WS_F1 + tb * 128, tag, lane);
    STAMP(4);
    if (gemm_wg && cp == 13 && (rp & 1) == 0) {
      poll_words(ws, WS_FW + (64 * w + lane) * 128, tag);
      if (lds_arrive(C_AW, lane) == 3 && lane == 0) __builtin_amdgcn_raw_buffer_store_b32(tag, ws, WS_FALL + (rp >> 1) * 256, 0, SC1);
      poll_words(ws, WS_F1 + (64 * w + lane) * 128, tag);
      if (lds_arrive(C_AX, lane) == 3 && lane == 0) __builtin_amdgcn_raw_buffer_store_b32(tag, ws, WS_FALL + (rp >> 1) * 256 + 128, 0, SC1);
    }
    if (gemm_wg) await<SLL>(E_DONE + FALL_AT * 4, 4);
  }

  if (role == 2) {
  } else if (!gemm_wg) {
    const KgIn kin = kg_load(p, dg + (w >> 1), lane);
    if (role == 0) BAR();
    const float isc = ssm_prepare(ws, kin, grp, fpos, role ? 4 : 0, cnt, dg, w, lane, [&] {
      STAMP(17);
      if (wave == 0) { await(E_LN, 4); poll_words(ws, WS_FALL + xcd * 256 + 128, lds_tag()); signal(E_F1OK, lane); } else { await<2>(E_F1OK, 1); }
      STAMP(18);
    }, W_ISC);
    STAMP(16);
    if (role == 0) {
      conv_channel(ws, grp + (w >> 1) * CH_BYTES, dg + (w >> 1), w & 1, isc, lane, [&] {}, [&] { signal(E_R1, lane); });
      WAITV(0);
      signal(E_SDR, lane);
      STAMP(5);
    }
  } else if (role == 0) {
    const int wf = w & 1, wt = w >> 1, h = lane >> 5;
    BAR();
    stage_x(1); stage_w(1);
    PRIO(2);
    unsigned aa[2][2], ba[2][4];
    {
      const int q = (lane & 15) >> 2, pp = lane & 3, fb = (lane >> 4) & 1;
#pragma unroll
      for (int mi = 0; mi < 2; ++mi) {
        aa[0][mi] = (8 * h + q) * 256 + 16 * ((8 * wf + 4 * (mi ^ wt) + 2 * fb + (pp >> 1)) ^ (q << 2)) + 8 * (pp & 1);
        aa[1][mi] = aa[0][mi] + 4 * UNIT;
        asm volatile("" : "+v"(aa[1][mi]));
      }
#pragma unroll
      for (int s = 0; s < 4; ++s) {
        ba[0][s] = (64 * wt + (lane & 31)) * 128 + 16 * ((2 * s + h) ^ ((lane >> 1) & 7));
        ba[1][s] = ba[0][s] + 4 * UNIT;
        asm volatile("" : "+v"(ba[1][s]));
      }
    }
    f16v acc[2][2] = {};
    v4u A[3][2], B[3][2], st[2];
    unsigned b1v[2], fl = 0, dn = 0, sbase = sig_base(lane), one = 1u, vxo = vx0 ^ 64;
    asm volatile("" : "+v"(sbase), "+v"(one), "+v"(vxo));
    f4v cs4 = {};
    const unsigned sel1 = (((lane >> 2) ^ (lane >> 4)) & 1) ? 0u : 0x3f803f80u;
    const v4u sel = {sel1, sel1, sel1, sel1};
    auto rdA = [&](int n, int mi) {
      const int u = (2 * (n >> 2) + 1) % 7;
      const unsigned a = aa[u >> 2][mi] + (u & 3) * UNIT + (n & 3) * 4096;
      A[n % 3][mi] = ldstr2(a, a + 1024);
    };
    auto rdB = [&](int n, int ni) {
      const int u = (2 * (n >> 2)) % 7;
      B[n % 3][ni] = lds128(ba[u >> 2][n & 3] + (u & 3) * UNIT + ni * 4096);
    };
    const v4u xs = make_srd(p.x, X_BYTES), w1s = make_srd(p.w1, W_BYTES);
    unsigned sx[4], sw[4], mb = w * 4096;
#pragma unroll
    for (int j = 0; j < 4; ++j) {
      sx[j] = (LT0 + 32 * w) * 1280 + ((3 + ROTV) % 10) * 128 + j * 9216;
      sw[j] = (((3 + ROTV) % 10) * 64 + 16 * w) * 3456 + 2 * LF0 + j * 12800;
      asm volatile("" : "+s"(sx[j]), "+s"(sw[j]));
    }
    auto piece = [&](auto qc) {
      constexpr int q = qc, i = (q + 8) / 8 + 2 + ((q >> 2) & 1), j = q & 3;
      if constexpr (q >= -4 && q < 52) {
        if constexpr (q & 4) {
          if constexpr (j == 0) dma1(xs, vx0, sx[0], mb + ((2 * i) % 7) * UNIT);
          else dma_next<j * 1024>(xs, (j & 1) ? vxo : vx0, sx[j]);
          sx[j] += (i + ROTV) % 10 == 9 ? -9 * 128 : 128;
          asm volatile("" : "+s"(sx[j]));
        } else {
          if constexpr (j == 0) dma1(w1s, vw0, sw[0], mb + ((2 * i + 1) % 7) * UNIT);
          else dma_next<j * 1024>(w1s, vw0, sw[j]);
          sw[j] += (i + ROTV) % 10 == 9 ? -9 * 64 * 3456 : 64 * 3456;
          asm volatile("" : "+s"(sw[j]));
        }
      }
    };
    auto load_stats = [&] {
#pragma unroll
      for (int i = 0; i < 2; ++i) st[i] = __builtin_amdgcn_raw_buffer_load_b128(ws, (lane & 31) * 16, WS_STAT + (t0 + 64 * wt + 32 * i) * 16, SC1);
    };
    WAITV(8);
    group_sync(E_FULL, 4, lane);
    STAMP(2); STAMPC(11);
    rdA(0, 0); rdB(0, 0); rdB(0, 1); rdA(0, 1); rdA(1, 0); rdB(1, 0); rdB(1, 1); rdA(1, 1);
    SCHED();
    unsigned tag = 0, tf = 0, td = 0, tv = 0;
    v4u eb[2];
    static_for<40>([&](auto nc) {
      constexpr int n = nc, sg = n >> 2, r = n & 3;
      acc[0][0] = mfma32b(A[n % 3][0], B[n % 3][0], acc[0][0]);
      SCHED();
      if (n < 38) { rdA(n + 2, 0); rdB(n + 2, 0); }
      SCHED();
      acc[0][1] = mfma32b(A[n % 3][0], B[n % 3][1], acc[0][1]);
      SCHED();
      piece(IC<2 * (n - 3)>{});
      if (r == 1 && sg < 9) fl = *(volatile LAS unsigned*)lp(sbase + (E_FULL - CNT) + (sg + 1) * 4);
      if (r == 2 && sg < 7) dn = *(volatile LAS unsigned*)lp(sbase + (E_DONE - CNT) + sg * 4);
      if (n == 34) { dn = *(volatile LAS unsigned*)lp(sbase + (E_LN - CNT)); tag = *(volatile LAS unsigned*)lp(sbase + (W_TAG - CNT)); }
      SCHED();
      cs4 = mfma16b(sel, A[n % 3][0], cs4);
      SCHED();
      acc[1][0] = mfma32b(A[n % 3][1], B[n % 3][0], acc[1][0]);
      SCHED();
      if (n < 38) { rdB(n + 2, 1); rdA(n + 2, 1); }
      if (r == 1) signal_at<E_DONE - CNT + sg * 4>(sbase, one);
      if (n == 39) { sts32(SEXC + w * 128 + (lane & 31) * 4, f2u(cs4[0])); signal(E_SX + w * 4, lane); }
      SCHED();
      acc[1][1] = mfma32b(A[n % 3][1], B[n % 3][1], acc[1][1]);
      SCHED();
      piece(IC<2 * (n - 3) + 1>{});
      if (r == 1 && sg < 9) { CHK(fl, E_FULL + (sg + 1) * 4, 4, tf); if (sg & 1) STAMPL(16 + (sg >> 1)); }
      if (r == 2 && sg < 7) CHK(dn, E_DONE + sg * 4, 4, td);
      if (r == 0 && sg < 9 && sg != 1) {
        if (sg == 0) WAITV(0); else if (sg < 8) TACC(tv, WAITV(8)); else TACC(tv, WAITV(4));
        signal_at<E_FULL - CNT + (sg + 1) * 4>(sbase, one);
      }
      if (n == 29) load_stats();
      if (n == 30) {
#pragma unroll
        for (int i = 0; i < 2; ++i)
          b1v[i] = __builtin_amdgcn_raw_buffer_load_b16(make_rsrc(p.b1, F_ * 2), (lane & 31) * 2, (f0 + 64 * wf + 32 * (i ^ wt)) * 2, 0);
        signal_at<E_VMQ - CNT>(sbase, one);
      }
      if (n == 35) {
        if (__builtin_expect(__builtin_amdgcn_readfirstlane(dn) < 4, 0)) { await(E_LN, 4); tag = peek(W_TAG); }
        tag = __builtin_amdgcn_readfirstlane(tag);
      }
      if (n == 37) {
        if (__builtin_expect(!__all(st[0].y == tag && st[0].w == tag && st[1].y == tag && st[1].w == tag), 0)) {
#pragma unroll 1
          for (unsigned s = 0; s < SPIN_CAP; ++s) {
            asm volatile("" ::: "memory");
            load_stats();
            if (__all(st[0].y == tag && st[0].w == tag && st[1].y == tag && st[1].w == tag)) break;
          }
        }
      }
      if (n == 37 || n == 38) {
        constexpr int i = n == 38;
        const float mu = -u2f(st[i].x), ir = u2f(st[i].z), mh = bfr(mu), ih = bfr(ir);
        eb[i] = h ? (v4u){0u, 0u, 0u, 0u} : (v4u){pkbf(mh, opq(mu - mh)), pkbf(mh, 0.f), pkbf(ih, opq(ir - ih)), 0u};
        asm volatile("" : "+v"(eb[i]));
      }
      SCHED();
    });
    STAMP(3); STAMPC(12); STAMPV(29, tf); STAMPV(30, td); STAMPV(31, tv);
    auto ext = [&](int mi, float S) {
      const float sh = bfr(S);
      const v4u ea = h ? (v4u){0u, 0u, 0u, 0u} : (v4u){pkbf(sh, sh), pkbf(S - sh, 0.f), b1v[mi] | (b1v[mi] << 16), 0u};
      acc[mi][0] = mfma32b(ea, eb[0], acc[mi][0]);
      acc[mi][1] = mfma32b(ea, eb[1], acc[mi][1]);
    };
    fl = peek(E_SX + (w ^ 2) * 4);
    float so = u2f(peek(SEXC + (w ^ 2) * 128 + (lane & 31) * 4));
    ext(0, cs4[0]);
    SCHED();
    if (__builtin_expect(__builtin_amdgcn_readfirstlane(fl) < 1, 0)) {
      await(E_SX + (w ^ 2) * 4, 1);
      so = u2f(peek(SEXC + (w ^ 2) * 128 + (lane & 31) * 4));
    }
    ext(1, so);
    STAMP(1);
    if (f0 + 64 * wf < F_) {
      const unsigned hv = ((lane >> 4) & 1) * 55296 + h * 256 + (lane & 15) * 16;
#pragma unroll
      for (int mi = 0; mi < 2; ++mi) {
#pragma unroll
        for (int ni = 0; ni < 2; ++ni) {
          const float ir = u2f(st[ni].z), rho = __builtin_amdgcn_rcpf(ir);
          const float k1 = -2.302208198f * rho, k2 = k1 * 0.044715f * rho * rho;
          const unsigned hs = WS_H + ((t0 + 64 * wt + 32 * ni) / 16) * 55296 + ((f0 + 64 * wf) / 32 + (mi ^ wt)) * 1024;
#pragma unroll
          for (int hf = 0; hf < 2; ++hf) {
            unsigned o[4];
#pragma unroll
            for (int r = 0; r < 4; ++r) {
              const f2v a = {acc[mi][ni][8 * hf + 2 * r], acc[mi][ni][8 * hf + 2 * r + 1]};
              const f2v x = a * (a * a * k2 + k1);
              const f2v e = {__builtin_amdgcn_exp2f(x.x), __builtin_amdgcn_exp2f(x.y)};
              const f2v dd = e * ir + ir;
              const f2v rr = {__builtin_amdgcn_rcpf(dd.x), __builtin_amdgcn_rcpf(dd.y)};
              const f2v gl = a * rr;
              o[r] = pkbf(gl.x, gl.y);
            }
            __builtin_amdgcn_raw_buffer_store_b128((v4u){o[0], o[1], o[2], o[3]}, ws, HADDR, 0, SC1);
            SCHED();
          }
        }
      }
    }
    signal(E_GELU, lane);
    STAMP(4); STAMPC(13);
    publish(ws, C_H, 4, WS_F2 + (rp * 16 + cp) * 128, tag, lane);
    STAMP(5);
  }

  if (role != 1) {
    const int mh = w & 1, q = role + (w >> 1);
    if (wave == 0) {
      PRIO(0);
      poll_words(ws, WS_F2 + ((tt >> 1) * 16 + min(lane, 13)) * 128, lds_tag());
      signal(E_F2OK, lane);
    } else {
      await<1>(E_F2OK, 1);
    }
    if (role == 0) STAMP(6);
    PRIO(3);
    v4u H[7][2], A[2][5];
    f4v acc[2][5] = {};
    const unsigned hs = WS_H + (4 * tt + 2 * mh) * 55296 + (14 * q - (q == 3)) * 1024;
    const unsigned hv = lane * 16;
    auto ldh = [&](int k, int ti) { H[k % 7][ti] = __builtin_amdgcn_raw_buffer_load_b128(ws, hv, hs + ti * 55296 + k * 1024, SC1); SCHED(); };
#pragma unroll
    for (int k = 0; k < 3; ++k) { ldh(k, 0); ldh(k, 1); }
    if (role == 2) { if (!gemm_wg) await(E_SDR, 4); publish(ws, C_GRP + 96 + 36, 4, WS_F3 + tb * 128, lds_tag(), lane); STAMP(9); }
    const int fo = q < 2 ? 3 * q : 2 + 2 * q;
    v2u xv[3], bv[3];
    unsigned alo = q * 25600 + lane * 16, ahi = SLOT_HI + q * 10240 + lane * 16, fl = 0, sq = sig_base(lane) + q * 4, one = 1u;
    asm volatile("" : "+v"(alo), "+v"(ahi), "+v"(sq), "+v"(one));
    auto rd = [&](int k, int n) { A[k & 1][n] = lds128((k % 7 < 5 ? alo + (k % 7) * 5120 : ahi + (k % 7 - 5) * 5120) + n * 1024); };
    await<1>(E_W0, 4);
#pragma unroll
    for (int n = 0; n < 5; ++n) rd(0, n);
    if (role == 0) { STAMP(7); STAMPC(14); }
    SCHED();
    auto step = [&](auto kc) {
      constexpr int k = kc;
      if (k == 3 || k == 7 || k == 10) PRIO(k == 3 ? 2 : k == 7 ? 1 : 0);
#pragma unroll
      for (int ti = 0; ti < 2; ++ti) {
#pragma unroll
        for (int n = 0; n < 5; ++n) {
          acc[ti][n] = mfma16b(A[k & 1][n], H[k % 7][ti], acc[ti][n]);
          SCHED();
          if (ti == 0 && k < 13) {
            rd(k + 1, n);
            if (n == 4 && k == 0) signal_at<E_QD - CNT>(sq, one);
            if (n == 4 && k == 2) signal_at<E_QD - CNT + 16>(sq, one);
            if (n == 4 && k == 4) signal_at<E_QD - CNT + 32>(sq, one);
            if (n == 4 && k == 5) signal_at<E_QD - CNT + 48>(sq, one);
            if (n == 4 && k == 9) signal_at<E_QD - CNT + 64>(sq, one);
            SCHED();
          }
          if (ti == 1 && n == 0) {
            if (k == 3) fl = peek(E_W1);
            if (k == 5) fl = *(volatile LAS unsigned*)lp(sq + (E_QF - CNT));
            if (k == 7) fl = *(volatile LAS unsigned*)lp(sq + (E_QF - CNT) + 16);
            if (k == 9) fl = *(volatile LAS unsigned*)lp(sq + (E_QF - CNT) + 32);
            if (k == 12) fl = *(volatile LAS unsigned*)lp(sq + (E_QD - CNT) + 64);
            SCHED();
          }
        }
        if (k < 2) { ldh(3 + 2 * k + ti, 0); ldh(3 + 2 * k + ti, 1); }
        if (k >= 1 && k < 8) ldh(k + 6, ti);
        if constexpr (k >= 8 && k < 11) {
          const int f = min(fo + k - 8, 9), tl = f >= 5, n = f - 5 * tl;
          const int l1 = fresh_lane();
          if (ti == 0) xv[k - 8] = __builtin_amdgcn_raw_buffer_load_b64(xr, (l1 & 15) * 1280 + 8 * (l1 >> 4), (64 * tt + 32 * mh + 16 * tl) * 1280 + 160 * ct + 32 * n, 0);
          else bv[k - 8] = __builtin_amdgcn_raw_buffer_load_b64(make_rsrc(p.b2, D_ * 2), 8 * (l1 >> 4), 160 * ct + 32 * n, 0);
        }
        SCHED();
      }
      if (k == 3) check(fl, E_W1, 4);
      if (k == 5) check(fl, E_QF + q * 4, 1);
      if (k == 7) check(fl, E_QF + 16 + q * 4, 1);
      if (k == 9) check(fl, E_QF + 32 + q * 4, 1);
      if (role == 0 && (k & 1)) STAMPL(22 + k / 2);
      SCHED();
    };
    static_for<13>(step);
    if (role == 0) step(IC<13>{});
    if (role == 0) { STAMP(8); STAMPC(15); } else { STAMP(4); }
    check(fl, E_QD + 64 + q * 4, 2);
#pragma unroll
    for (int f = 0; f < 10; ++f) sts128(alo + mh * 10240 + f * 1024, __builtin_bit_cast(v4u, acc[f / 5][f % 5]));
    fl = peek(E_SRDY);
    const int l2 = fresh_lane(), g2 = l2 >> 4;
    group_sync<0>(E_RED + mh * 4, 4, l2);
    check(fl, E_SRDY, 4);
    if (role == 0) STAMP(9);
    auto finish = [&](int i) {
      const int f = fo + i, tl = f >= 5, n = f - 5 * tl;
      const unsigned ra = mh * 10240 + f * 1024 + l2 * 16;
      const f4v p0 = __builtin_bit_cast(f4v, lds128(ra)), p1 = __builtin_bit_cast(f4v, lds128(ra + 25600));
      const f4v p2 = __builtin_bit_cast(f4v, lds128(ra + 51200)), p3 = __builtin_bit_cast(f4v, lds128(ra + 76800));
      const v2u sv = ldstr(STILE + (16 * n + (l2 >> 2)) * 160 + (32 * mh + 16 * tl + 4 * (l2 & 3)) * 2);
      const float bz[4] = {blo(bv[i].x), bhi(bv[i].x), blo(bv[i].y), bhi(bv[i].y)};
      const float sz[4] = {blo(sv.x), bhi(sv.x), blo(sv.y), bhi(sv.y)};
      const float xz[4] = {blo(xv[i].x), bhi(xv[i].x), blo(xv[i].y), bhi(xv[i].y)};
      float r[4];
#pragma unroll
      for (int j = 0; j < 4; ++j) {
        const float z = (p0[j] + p1[j]) + (p2[j] + p3[j]) + bz[j];
        const float gt = z * __builtin_amdgcn_rcpf(1.f + __builtin_amdgcn_exp2f(z * -1.44269504088896f));
        r[j] = fmaf(sz[j], gt, xz[j]);
      }
      __builtin_amdgcn_raw_buffer_store_b64((v2u){pkbf(r[0], r[1]), pkbf(r[2], r[3])}, make_rsrc(p.out, X_BYTES), (l2 & 15) * 1280 + 8 * g2,
                                            (64 * tt + 32 * mh + 16 * tl) * 1280 + 160 * ct + 32 * n, OUT_AUX);
    };
    finish(0); finish(1);
    if (role == 0) finish(2);
    STAMP(10);
    asm volatile(".Lfused_end_b:");
  } else {
    const unsigned wq = WS_W2T + ct * 54 * 5120;
    if (w == 0) { poll_words(ws, WS_FALL + xcd * 256, lds_tag()); signal(E_FWOK, lane); } else { await<1>(E_FWOK, 1); }
    STAMP(5);
    const unsigned wr = wq + (14 * w - (w == 3)) * 5120;
    v4u R[35];
    if (gemm_wg) await<2>(E_VMQ, 4);
#pragma unroll
    for (int i = 0; i < 5; ++i) {
      const int sl = 4 * i + w, qq = sl / 5;
#pragma unroll
      for (int j = 7 * i; j < 7 * i + 7; ++j) {
        R[j] = __builtin_amdgcn_raw_buffer_load_b128(ws, lane * 16 + (j & 3) * 1024, wr + 7 * 5120 + (j >> 2) * 4096, SC1);
        SCHED();
        asm volatile("s_waitcnt vmcnt(%0)" ::"i"(W2_DEPTH) : "memory");
      }
      if (gemm_wg) {
        await<2>(E_DONE + (sl < 6 ? 7 : sl < 12 ? 8 : 9) * 4, 4);
        dma5_lin_sc1(wss, lane * 16, wq + (14 * qq - (qq == 3) + sl % 5) * 5120, sl * 5120);
      }
    }
    STAMP(6);
    if (!gemm_wg) {
      await<2>(E_R1, 8);
#pragma unroll 1
      for (int i = 0; i < 5; ++i) {
        const int sl = 4 * i + w, qq = sl / 5;
        dma5_lin_sc1(wss, lane * 16, wq + (14 * qq - (qq == 3) + sl % 5) * 5120, sl * 5120);
      }
    }
    WAITV(0);
    signal(E_W0, lane);
    STAMP(7);
    await<2>(E_CONV, 4);
    dma5_lin_sc1(wss, lane * 16, wr + 5 * 5120, SLOT_HI + w * 10240);
    dma5_lin_sc1(wss, lane * 16, wr + 6 * 5120, SLOT_HI + w * 10240 + 5120);
    WAITV(0);
    signal(E_W1, lane);
    STAMP(8);
    PRIO(3);
    const unsigned lo = w * 25600 + lane * 16, hi = SLOT_HI + w * 10240 + lane * 16;
    await<1>(E_QD + w * 4, 2);
#pragma unroll
    for (int i = 0; i < 10; ++i) sts128(lo + i * 1024, R[i]);
    signal(E_QF + w * 4, lane);
    await<1>(E_QD + 16 + w * 4, 2);
#pragma unroll
    for (int i = 10; i < 20; ++i) sts128(lo + i * 1024, R[i]);
    signal(E_QF + 16 + w * 4, lane);
    await<1>(E_QD + 32 + w * 4, 2);
#pragma unroll
    for (int i = 20; i < 25; ++i) sts128(lo + i * 1024, R[i]);
#pragma unroll
    for (int i = 25; i < 30; ++i) sts128(hi + (i - 25) * 1024, R[i]);
    await<1>(E_QD + 48 + w * 4, 2);
#pragma unroll
    for (int i = 30; i < 35; ++i) sts128(hi + (i - 25) * 1024, R[i]);
    signal(E_QF + 32 + w * 4, lane);
    STAMP(9);
    if (w == 0) { poll_words(ws, WS_F3 + (ct * 32 + (lane & 31)) * 128, lds_tag()); signal(E_F3OK, lane); } else { await<1>(E_F3OK, 1); }
    v4u sv[3];
#pragma unroll
    for (int i = 0; i < 3; ++i)
      sv[i] = __builtin_amdgcn_raw_buffer_load_b128(ws, (lane >> 3) * 4096 + (lane & 7) * 16, WS_ST + (80 * ct + 8 * min(w + 4 * i, 9)) * 4096 + tt * 128, SC1);
#pragma unroll
    for (int i = 0; i < 3; ++i) sts128(STILE + (8 * min(w + 4 * i, 9) + (lane >> 3)) * 160 + (lane & 7) * 16, sv[i]);
    signal(E_SRDY, lane);
    STAMP(10);
    asm volatile(".Lfused_end_a:");
  }
  if (blockIdx.x == PROBE_BLK && wave == PROBE_WAVE) for (int i_ = 0; i_ < 200000 && __builtin_amdgcn_s_memrealtime() - t_probe < 3000ull; ++i_) __builtin_amdgcn_s_sleep(1);
}

}

extern "C" void kernel_launch(void* const* d_in, const int* in_sizes, int n_in, void* d_out, int out_size, void* d_ws, size_t ws_size,
                              hipStream_t stream) {
  Params p;
  p.x = (const unsigned short*)d_in[0];
  p.lA = (const unsigned short*)d_in[1];
  p.Ai = (const unsigned short*)d_in[2];
  p.Cr = (const unsigned short*)d_in[3];
  p.Ci = (const unsigned short*)d_in[4];
  p.ldt = (const unsigned short*)d_in[5];
  p.Dsk = (const unsigned short*)d_in[6];
  p.w1 = (const unsigned short*)d_in[8];
  p.b1 = (const unsigned short*)d_in[9];
  p.w2 = (const unsigned short*)d_in[10];
  p.b2 = (const unsigned short*)d_in[11];
  p.out = (unsigned short*)d_out;
  p.ws = (unsigned char*)d_ws;
  (void)hipFuncSetAttribute((const void*)fused_kernel, hipFuncAttributeMaxDynamicSharedMemorySize, LDS_BYTES);
  fused_kernel<<<256, 768, LDS_BYTES, stream>>>(p);
}
```

```cpp
#include <hip/hip_runtime.h>
#include <hip/hip_bf16.h>

namespace {

typedef unsigned v4u __attribute__((ext_vector_type(4)));
typedef unsigned v2u __attribute__((ext_vector_type(2)));
typedef short s4v __attribute__((ext_vector_type(4)));
typedef float f2v __attribute__((ext_vector_type(2)));
typedef float f4v __attribute__((ext_vector_type(4)));
typedef float f16v __attribute__((ext_vector_type(16)));
typedef _Float16 h2v __attribute__((ext_vector_type(2)));
typedef _Float16 h8v __attribute__((ext_vector_type(8)));
typedef __bf16 b2v __attribute__((ext_vector_type(2)));
typedef __bf16 b8v __attribute__((ext_vector_type(8)));
#define GAS __attribute__((address_space(1)))
#define LAS __attribute__((address_space(3)))
#define DEV __device__ __forceinline__
typedef __amdgpu_buffer_rsrc_t rsrc_t;

constexpr int L_ = 2048, D_ = 640, F_ = 1728;
constexpr unsigned X_BYTES = L_ * D_ * 2, W_BYTES = D_ * F_ * 2;
constexpr int SC1 = 16;
constexpr unsigned SPIN_CAP = 1u << 16;

constexpr unsigned WS_EPOCH = 0;
constexpr unsigned WS_FALL = 4096;
constexpr unsigned WS_STAT = 32768;
constexpr unsigned WS_F2 = 65536;
constexpr unsigned WS_F1 = 5013504;
constexpr unsigned WS_F3 = WS_F1 + 32768;
constexpr unsigned WS_XNT = 131072;
constexpr unsigned WS_W2T = 2752512;
constexpr unsigned WS_H = 5242880;
constexpr unsigned WS_ST = 12582912;
constexpr unsigned WS_STAMP = 15204352;
constexpr unsigned WS_BYTES = WS_STAMP + 256 * 12 * 64 * 8;

constexpr unsigned LDS_BYTES = 163840;
constexpr unsigned UNIT = 16384;
constexpr unsigned KSCR = 8704;
constexpr unsigned FPOS = 2576;
constexpr unsigned GRP = 7 * UNIT;
constexpr unsigned LNST = GRP + 4 * KSCR;
constexpr unsigned CNT = LNST + 10752;
constexpr unsigned SEXC = CNT + 512;
constexpr unsigned JUNK = CNT + 1024;
constexpr unsigned UCP = 2368;
constexpr unsigned CH_R3 = 4 * UCP, CH_U = 9856, CH_BYTES = 2 * KSCR;
constexpr unsigned SLOT_HI = 102400, STILE = 143360;
constexpr unsigned E_FULL = CNT, E_DONE = CNT + 40;
constexpr unsigned E_W0 = CNT + 80, E_W1 = CNT + 84, E_F2OK = CNT + 88, E_F3OK = CNT + 92, E_RED = CNT + 96;
constexpr unsigned E_QD = CNT + 384, E_QF = CNT + 104;
constexpr unsigned E_GELU = CNT + 184, E_LN = CNT + 188, E_LNPUB = CNT + 192, E_SRDY = CNT + 196, E_VMQ = CNT + 200, E_FWOK = CNT + 204, C_AX = CNT + 156, E_R1 = CNT + 160, E_SDR = CNT + 164, W_ISC = CNT + 472;
constexpr unsigned C_H = CNT + 208, C_F1 = CNT + 212, E_F1OK = CNT + 216, E_CONV = CNT + 220, E_SX = CNT + 368, E_RT = CNT + 360;
constexpr unsigned C_GRP = CNT + 224;
constexpr unsigned W_TAG = CNT + 464;
constexpr int N_ZERO = 116;

struct Params {
  const unsigned short *x, *lA, *Ai, *Cr, *Ci, *ldt, *Dsk, *w1, *b1, *w2, *b2;
  unsigned short* out;
  unsigned char* ws;
};

#define STAMP(i) do { } while (0)
#define STAMPC(i) do { } while (0)
#define STAMPV(i, v) do { } while (0)
#define STAMP_INIT() do { } while (0)
#define STAMP_FLUSH() do { } while (0)

#define PRIO(x) __builtin_amdgcn_s_setprio(x)
#define STAMPL(i) do { } while (0)
#define F2_SLEEP 2
#define OUT_AUX 0
#define SLA 1
#define SLL 2
#define RPACE 1
#define FALL_AT 4
#define U_AT E_VMQ
#define ROT7 ((slot % 7 + (slot / 7 * 7 + 2) / 4) % 7)
#define BAR() asm volatile("s_waitcnt lgkmcnt(0)\n\ts_barrier" ::: "memory")
#define WAITV(n) asm volatile("s_waitcnt vmcnt(" #n ")" ::: "memory")
#define SCHED() __builtin_amdgcn_sched_barrier(0)

template <int I> struct IC { constexpr operator int() const { return I; } };
template <int N, int I = 0, class F> DEV void static_for(F&& f) {
  if constexpr (I < N) { f(IC<I>{}); static_for<N, I + 1>(f); }
}
DEV int fresh_lane() {
  int l;
  asm volatile("v_mbcnt_lo_u32_b32 %0, -1, 0\n\tv_mbcnt_hi_u32_b32 %0, -1, %0" : "=v"(l));
  return l;
}
DEV float u2f(unsigned u) { return __builtin_bit_cast(float, u); }
DEV unsigned f2u(float f) { return __builtin_bit_cast(unsigned, f); }
DEV h2v u2h(unsigned u) { return __builtin_bit_cast(h2v, u); }
DEV float blo(unsigned u) { return u2f(u << 16); }
DEV float bhi(unsigned u) { return u2f(u & 0xffff0000u); }
DEV unsigned pkbf(float a, float b) { return __builtin_bit_cast(unsigned, __builtin_convertvector((f2v){a, b}, b2v)); }
DEV unsigned pkh(float a, float b) { return __builtin_bit_cast(unsigned, __builtin_convertvector((f2v){a, b}, h2v)); }
DEV float opq(float x) { asm("" : "+v"(x)); return x; }
DEV float bfr(float a) { return blo(pkbf(a, a)); }
DEV f16v mfma32b(v4u a, v4u b, f16v c) { return __builtin_amdgcn_mfma_f32_32x32x16_bf16(__builtin_bit_cast(b8v, a), __builtin_bit_cast(b8v, b), c, 0, 0, 0); }
DEV f16v mfma32h(v4u a, v4u b, f16v c) { return __builtin_amdgcn_mfma_f32_32x32x16_f16(__builtin_bit_cast(h8v, a), __builtin_bit_cast(h8v, b), c, 0, 0, 0); }
DEV f4v mfma16b(v4u a, v4u b, f4v c) { return __builtin_amdgcn_mfma_f32_16x16x32_bf16(__builtin_bit_cast(b8v, a), __builtin_bit_cast(b8v, b), c, 0, 0, 0); }
DEV f4v mfma16h(v4u a, v4u b, f4v c) { return __builtin_amdgcn_mfma_f32_16x16x32_f16(__builtin_bit_cast(h8v, a), __builtin_bit_cast(h8v, b), c, 0, 0, 0); }

DEV LAS unsigned char* lp(unsigned a) { return (LAS unsigned char*)(size_t)a; }
DEV v4u lds128(unsigned a) { return *(LAS v4u*)lp(a); }
DEV v2u lds64(unsigned a) { return *(LAS v2u*)lp(a); }
DEV unsigned lds32(unsigned a) { return *(LAS unsigned*)lp(a); }
DEV void sts128(unsigned a, v4u v) { *(LAS v4u*)lp(a) = v; }
DEV void sts64(unsigned a, v2u v) { *(LAS v2u*)lp(a) = v; }
DEV void sts32(unsigned a, unsigned v) { *(LAS unsigned*)lp(a) = v; }
DEV v2u ldstr(unsigned a) { return __builtin_bit_cast(v2u, __builtin_amdgcn_ds_read_tr16_b64_v4i16((LAS s4v*)lp(a))); }
DEV v4u ldstr2(unsigned a0, unsigned a1) { const v2u x = ldstr(a0), y = ldstr(a1); return (v4u){x.x, x.y, y.x, y.y}; }

DEV v4u make_srd(const void* ptr, unsigned bytes) {
  return (v4u){(unsigned)(size_t)ptr, (unsigned)((size_t)ptr >> 32) & 0xffffu, bytes, 0x00020000u};
}
DEV rsrc_t make_rsrc(const void* ptr, unsigned bytes) { return __builtin_amdgcn_make_buffer_rsrc((void*)ptr, 0, bytes, 0x00020000); }
#define DMA_HEAD "s_mov_b32 m0, %0\n\ts_nop 0\n\t"
DEV void dma1(v4u srd, unsigned v, unsigned soff, unsigned lds) {
  asm volatile(DMA_HEAD "buffer_load_dwordx4 %1, %2, %3 offen lds" ::"s"(lds), "v"(v), "s"(srd), "s"(soff) : "memory");
}
template <int OFF> DEV void dma_next(v4u srd, unsigned v, unsigned soff) {
  asm volatile("buffer_load_dwordx4 %0, %1, %2 offen offset:%3 lds" ::"v"(v), "s"(srd), "s"(soff), "i"(OFF) : "memory");
}
DEV void dma4(v4u srd, const unsigned (&v)[4], unsigned soff, unsigned lds) {
  asm volatile(DMA_HEAD "buffer_load_dwordx4 %1, %5, %6 offen lds\n\tbuffer_load_dwordx4 %2, %5, %6 offen offset:1024 lds\n\t"
               "buffer_load_dwordx4 %3, %5, %6 offen offset:2048 lds\n\tbuffer_load_dwordx4 %4, %5, %6 offen offset:3072 lds"
               ::"s"(lds), "v"(v[0]), "v"(v[1]), "v"(v[2]), "v"(v[3]), "s"(srd), "s"(soff) : "memory");
}
DEV void dma5_lin_sc1(v4u srd, unsigned v, unsigned soff, unsigned lds) {
  asm volatile(DMA_HEAD "buffer_load_dwordx4 %1, %2, %3 offen sc1 lds\n\tbuffer_load_dwordx4 %1, %2, %3 offen offset:1024 sc1 lds\n\t"
               "buffer_load_dwordx4 %1, %2, %3 offen offset:2048 sc1 lds\n\tbuffer_load_dwordx4 %1, %2, %3 offen offset:3072 sc1 lds"
               ::"s"(lds), "v"(v), "s"(srd), "s"(soff) : "memory");
  asm volatile(DMA_HEAD "buffer_load_dwordx4 %1, %2, %3 offen sc1 lds" ::"s"(lds + 4096), "v"(v), "s"(srd), "s"(soff + 4096) : "memory");
}

DEV void poll_words(rsrc_t ws, unsigned off, unsigned tag) {
#pragma unroll 1
  for (unsigned s = 0; s < SPIN_CAP; ++s) {
    asm volatile("" ::: "memory");
    if (__all(__builtin_amdgcn_raw_buffer_load_b32(ws, off, 0, SC1) == tag)) break;
    __builtin_amdgcn_s_sleep(F2_SLEEP);
  }
  asm volatile("" ::: "memory");
}
DEV unsigned sig_base(int lane) { return lane == 0 ? CNT : JUNK + lane * 4; }
template <int OFF> DEV void signal_at(unsigned base, unsigned one) {
  asm volatile("ds_add_u32 %0, %1 offset:%2" ::"v"(base), "v"(one), "i"(OFF) : "memory");
}
DEV void signal(unsigned a, int lane) {
  asm volatile("ds_add_u32 %0, %1" ::"v"(sig_base(lane) + (a - CNT)), "v"(1u) : "memory");
}
DEV unsigned peek(unsigned a) { return *(volatile LAS unsigned*)lp(a); }
template <int SLEEP = 1> DEV void await(unsigned a, unsigned need) {
#pragma unroll 1
  for (unsigned s = 0; s < (SPIN_CAP << 2); ++s) {
    asm volatile("" : "+s"(s));
    if (__builtin_amdgcn_readfirstlane(peek(a)) >= need) break;
    if (SLEEP) __builtin_amdgcn_s_sleep(SLEEP);
  }
  asm volatile("" ::: "memory");
}
DEV void check(unsigned early, unsigned a, unsigned need) {
  if (__builtin_expect(__builtin_amdgcn_readfirstlane(early) < need, 0)) await<1>(a, need);
  asm volatile("" ::: "memory");
}
DEV unsigned lds_arrive(unsigned cnt, int lane) {
  unsigned old;
  asm volatile("ds_add_rtn_u32 %0, %1, %2\n\ts_waitcnt lgkmcnt(0)" : "=&v"(old) : "v"(sig_base(lane) + (cnt - CNT)), "v"(1u) : "memory");
  return __builtin_amdgcn_readfirstlane(old);
}
template <int SLEEP = 1> DEV void group_sync(unsigned cnt, unsigned target, int lane) {
  if (lds_arrive(cnt, lane) + 1 < target) await<SLEEP>(cnt, target);
}
DEV void publish(rsrc_t ws, unsigned cnt, unsigned nwaves, unsigned flag_off, unsigned tag, int lane) {
  WAITV(0);
  if (lds_arrive(cnt, lane) + 1 == nwaves && lane == 0) __builtin_amdgcn_raw_buffer_store_b32(tag, ws, flag_off, 0, SC1);
}
DEV unsigned epoch_tag(unsigned ev, bool share, int lane) {
  const unsigned e = __builtin_amdgcn_readfirstlane(ev);
  const unsigned tag = e * 0x9E3779B1u + 0x7F4A7C15u;
  if (share && lane == 0) sts64(W_TAG, (v2u){tag, e});
  return tag;
}
DEV unsigned lds_tag() { return __builtin_amdgcn_readfirstlane(lds32(W_TAG)); }

template <int CTRL> DEV float dpp_add(float v) {
  return v + u2f(__builtin_amdgcn_update_dpp(0u, f2u(v), CTRL, 0xf, 0xf, false));
}
template <int CTRL> DEV float dpp_max(float v) {
  return fmaxf(v, u2f(__builtin_amdgcn_update_dpp(0u, f2u(v), CTRL, 0xf, 0xf, false)));
}
DEV float half_sum(float v) {
  v = dpp_add<0x128>(v); v = dpp_add<0x124>(v); v = dpp_add<0x122>(v); v = dpp_add<0x121>(v);
  const auto r = __builtin_amdgcn_permlane16_swap(f2u(v), f2u(v), false, false);
  return u2f(r[0]) + u2f(r[1]);
}
DEV float wave_max(float v) {
  v = dpp_max<0x128>(v); v = dpp_max<0x124>(v); v = dpp_max<0x122>(v); v = dpp_max<0x121>(v);
  const auto r = __builtin_amdgcn_permlane16_swap(f2u(v), f2u(v), false, false);
  v = fmaxf(u2f(r[0]), u2f(r[1]));
  const auto q = __builtin_amdgcn_permlane32_swap(f2u(v), f2u(v), false, false);
  return fmaxf(u2f(q[0]), u2f(q[1]));
}

struct LnIn { v2u v[5]; unsigned e; v4u b[4]; };
DEV LnIn ln_load(v4u xs, v4u wss, v4u w2s, int token, int blk, int lane) {
  LnIn r;
  const unsigned a = token * 1280 + (lane & 31) * 8, b = (blk / 10) * 40960 + (blk % 10) * 128 + (lane >> 3) * 1280 + (lane & 7) * 16;
  asm volatile("buffer_load_dwordx2 %0, %6, %7, 0 offen\n\tbuffer_load_dwordx2 %1, %6, %7, 0 offen offset:256\n\t"
               "buffer_load_dwordx2 %2, %6, %7, 0 offen offset:512\n\tbuffer_load_dwordx2 %3, %6, %7, 0 offen offset:768\n\t"
               "buffer_load_dwordx2 %4, %6, %7, 0 offen offset:1024\n\tbuffer_load_dword %5, off, %8, 0 sc1"
               : "=&v"(r.v[0]), "=&v"(r.v[1]), "=&v"(r.v[2]), "=&v"(r.v[3]), "=&v"(r.v[4]), "=&v"(r.e) : "v"(a), "s"(xs), "s"(wss) : "memory");
  asm volatile("buffer_load_dwordx4 %0, %4, %8, 0 offen\n\tbuffer_load_dwordx4 %1, %5, %8, 0 offen\n\t"
               "buffer_load_dwordx4 %2, %6, %8, 0 offen\n\tbuffer_load_dwordx4 %3, %7, %8, 0 offen"
               : "=&v"(r.b[0]), "=&v"(r.b[1]), "=&v"(r.b[2]), "=&v"(r.b[3]) : "v"(b), "v"(b + 10240), "v"(b + 20480), "v"(b + 30720), "s"(w2s) : "memory");
  return r;
}
template <int N> DEV void ln_wait(LnIn& r) {
  asm volatile("s_waitcnt vmcnt(%10)" : "+v"(r.v[0]), "+v"(r.v[1]), "+v"(r.v[2]), "+v"(r.v[3]), "+v"(r.v[4]), "+v"(r.e), "+v"(r.b[0]), "+v"(r.b[1]),
               "+v"(r.b[2]), "+v"(r.b[3]) : "i"(N) : "memory");
}
DEV f2v ln_compute(const LnIn& in, int row, int lane) {
  float xv[20], s = 0.f, q = 0.f;
#pragma unroll
  for (int m = 0; m < 5; ++m) { xv[4 * m] = blo(in.v[m].x); xv[4 * m + 1] = bhi(in.v[m].x); xv[4 * m + 2] = blo(in.v[m].y); xv[4 * m + 3] = bhi(in.v[m].y); }
#pragma unroll
  for (int m = 0; m < 20; ++m) { s = opq(s + xv[m]); q = opq(fmaf(xv[m], xv[m], q)); }
  s = half_sum(s); q = half_sum(q);
  const float mean = s * (1.f / 640.f);
  const float var = fmaxf(fmaf(-mean, mean, q * (1.f / 640.f)), 0.f);
  const float m1 = bfr(mean), s1 = bfr(__builtin_amdgcn_sqrtf(bfr(bfr(var) + 1e-5f)));
  const float r1 = __builtin_amdgcn_rcpf(s1), c = -m1 * r1;
#pragma unroll
  for (int m = 0; m < 5; ++m) {
    const v2u o = {pkh(opq(fmaf(xv[4 * m], r1, c)), opq(fmaf(xv[4 * m + 1], r1, c))), pkh(opq(fmaf(xv[4 * m + 2], r1, c)), opq(fmaf(xv[4 * m + 3], r1, c)))};
    sts64(LNST + row * 1344 + (lane & 31) * 8 + m * 256, o);
  }
  const float m2 = bfr((mean - m1) * r1), s2 = bfr(__builtin_amdgcn_sqrtf(bfr(bfr(var * r1 * r1) + 1e-5f)));
  return (f2v){opq(fmaf(m2, s1, m1)), opq(s1 * s2)};
}
DEV void ln_publish(rsrc_t ws, int tb, int w, int lane) {
  const unsigned a = LNST + ((lane & 15) >> 2) * 1344 + (lane >> 4) * 32 + (lane & 3) * 8 + w * 128;
  const unsigned o = WS_XNT + tb * 10240 + (w * 64 + lane) * 16;
  const v4u v0 = ldstr2(a, a + 4 * 1344), v1 = ldstr2(a + 512, a + 512 + 4 * 1344), v2 = ldstr2(a + (w < 2 ? 1024 : 0), a + (w < 2 ? 1024 : 0) + 4 * 1344);
  __builtin_amdgcn_raw_buffer_store_b128(v0, ws, o, 0, SC1);
  __builtin_amdgcn_raw_buffer_store_b128(v1, ws, o + 4096, 0, SC1);
  if (w < 2) __builtin_amdgcn_raw_buffer_store_b128(v2, ws, o + 8192, 0, SC1);
}

struct Cx { float r, i; };
DEV Cx cmul(Cx a, Cx b) { return {opq(fmaf(a.r, b.r, -a.i * b.i)), opq(fmaf(a.r, b.i, a.i * b.r))}; }
DEV Cx csqr(Cx a) { return {opq(fmaf(a.r, a.r, -a.i * a.i)), opq((a.r + a.r) * a.i)}; }
DEV unsigned ch(Cx a) { return pkh(a.r, a.i); }
DEV unsigned hmul(unsigned a, unsigned b) {
  unsigned t, r;
  asm("v_pk_mul_f16 %0, %1, %2 op_sel:[0,0] op_sel_hi:[0,1]" : "=v"(t) : "v"(a), "v"(b));
  asm("v_pk_fma_f16 %0, %1, %2, %3 op_sel:[1,1,0] op_sel_hi:[1,0,1] neg_lo:[0,1,0]" : "=v"(r) : "v"(a), "v"(b), "v"(t));
  return r;
}
DEV float exp_bf(float x) {
  const float t = x * 1.44268798828125f, lo = x * 7.0526077138765e-06f;
  const float n = __builtin_rintf(t);
  return bfr(__builtin_amdgcn_ldexpf(__builtin_amdgcn_exp2f((t - n) + lo), (int)fminf(fmaxf(n, -200.f), 200.f)));
}
DEV Cx cexp_at(float lg, float rh, float rl, float P) {
  const float m = __builtin_amdgcn_exp2f(lg * P), ph = fmaf(rl, P, __builtin_amdgcn_fractf(rh * P));
  return {opq(m * __builtin_amdgcn_cosf(ph)), opq(m * __builtin_amdgcn_sinf(ph))};
}
struct KgIn { unsigned lA, Ai, Cr, Ci; float dt, dsk; };
DEV KgIn kg_load(const Params& p, int d, int lane) {
  KgIn k;
  k.lA = ((const GAS unsigned*)p.lA)[d * 64 + lane]; k.Ai = ((const GAS unsigned*)p.Ai)[d * 64 + lane];
  k.Cr = ((const GAS unsigned*)p.Cr)[d * 64 + lane]; k.Ci = ((const GAS unsigned*)p.Ci)[d * 64 + lane];
  k.dt = u2f((unsigned)((const GAS unsigned short*)p.ldt)[d] << 16);
  k.dsk = u2f((unsigned)((const GAS unsigned short*)p.Dsk)[d] << 16);
  return k;
}
template <class Hook>
DEV float kg_wave(const KgIn& k, int g, unsigned scr, unsigned fp, int lane, Hook hook) {
  const float dt = exp_bf(k.dt), c2 = dt + dt;
  const float mx = wave_max(fmaxf(fmaxf(fabsf(blo(k.Cr)), fabsf(bhi(k.Cr))), fmaxf(fabsf(blo(k.Ci)), fabsf(bhi(k.Ci))))) * c2;
  int ex = __builtin_amdgcn_frexp_expf(fmaxf(mx, fabsf(k.dsk) * 0x1p-10f));
  ex = ex < -120 ? -120 : (ex > 120 ? 120 : ex);
  const float cs = c2 * __builtin_amdgcn_ldexpf(1.f, -ex);
  const int sh = g ? 0 : 16;
  const float lg = -(exp_bf(bhi(k.lA << sh)) * dt) * 1.44269504088896f, ai = bhi(k.Ai << sh) * dt;
  const Cx c0 = {opq(bhi(k.Cr << sh) * cs), opq(-bhi(k.Ci << sh) * cs)};
  const float rh = ai * 0.15915494f, rl = fmaf(ai, 0.15915494f, -rh) + ai * 6.4206382e-09f;
  const Cx v1 = cexp_at(lg, -rh, -rl, 1.f), w1 = cexp_at(lg, rh, rl, 8.f), G = cexp_at(lg, -rh, -rl, 256.f);
  const unsigned wa = scr + lane * 4, ra = scr + (lane & 31) * 272 + (lane >> 5) * 16;
  {
    const Cx w2 = csqr(w1), w3 = cmul(w2, w1), w4 = csqr(w2), w8 = csqr(w4), w12 = cmul(w8, w4), w16 = csqr(w8);
    const Cx w20 = cmul(w16, w4), w24 = csqr(w12), w28 = cmul(w24, w4);
    const unsigned o[3] = {ch(w1), ch(w2), ch(w3)};
    const unsigned bs[7] = {ch(w4), ch(w8), ch(w12), ch(w16), ch(w20), ch(w24), ch(w28)};
    hook(0);
    sts32(wa, 0x00003c00u);
#pragma unroll
    for (int j = 0; j < 3; ++j) sts32(wa + (1 + j) * 272, o[j]);
#pragma unroll
    for (int b = 0; b < 7; ++b) {
      sts32(wa + (4 + 4 * b) * 272, bs[b]);
#pragma unroll
      for (int j = 0; j < 3; ++j) sts32(wa + (5 + 4 * b + j) * 272, hmul(bs[b], o[j]));
    }
  }
  asm volatile("" ::: "memory");
  v4u y[8];
#pragma unroll
  for (int s = 0; s < 8; ++s) y[s] = lds128(ra + s * 32);
  asm volatile("" ::: "memory");
  {
    const Cx v2 = csqr(v1), v3 = cmul(v2, v1), v4 = csqr(v2), v6 = csqr(v3), x4 = cmul(c0, v4);
    const unsigned h1 = ch(v1), h2 = ch(v2), h3 = ch(v3), h4 = ch(v4), h6 = ch(v6), hx0 = ch(c0), hx4 = ch(x4);
    sts32(wa + 0 * 272, hx0); sts32(wa + 1 * 272, hmul(hx0, h1)); sts32(wa + 2 * 272, hmul(hx0, h2)); sts32(wa + 3 * 272, hmul(hx0, h3));
    sts32(wa + 4 * 272, hx4); sts32(wa + 5 * 272, hmul(hx4, h1)); sts32(wa + 6 * 272, hmul(hx4, h2)); sts32(wa + 7 * 272, hmul(hx4, h3));
    const Cx d1 = cmul(c0, G), d2 = cmul(d1, G), d3 = cmul(d2, G), d4 = cmul(d3, G);
    const Cx G4 = csqr(csqr(G));
    const Cx d5 = cmul(d1, G4), d6 = cmul(d2, G4), d7 = cmul(d3, G4), d8 = cmul(d4, G4);
    const unsigned e1 = ch(d1), e2 = ch(d2), e3 = ch(d3);
    sts32(wa + 8 * 272, e1); sts32(wa + 9 * 272, hmul(e1, h2)); sts32(wa + 10 * 272, hmul(e1, h4)); sts32(wa + 11 * 272, hmul(e1, h6));
    sts32(wa + 12 * 272, e2); sts32(wa + 13 * 272, hmul(e2, h4)); sts32(wa + 14 * 272, e3); sts32(wa + 15 * 272, hmul(e3, h4));
    sts32(wa + 16 * 272, ch(d4)); sts32(wa + 17 * 272, ch(d5)); sts32(wa + 18 * 272, ch(d6)); sts32(wa + 19 * 272, ch(d7));
    sts32(wa + 20 * 272, ch(d8));
  }
  asm volatile("" ::: "memory");
  v4u xx[8];
#pragma unroll
  for (int s = 0; s < 8; ++s) xx[s] = lds128(ra + s * 32);
  SCHED();
  f16v acc = {};
#pragma unroll
  for (int s = 0; s < 8; ++s) acc = mfma32h(xx[s], y[s], acc);
  const int j = lane & 31, h = lane >> 5;
  hook(1);
  if (g == 0 && lane == 0) acc[0] += __builtin_amdgcn_ldexpf(k.dsk, -ex);
  sts128(fp + (8 * j + 4 * h) * 4, (v4u){f2u(acc[0]), f2u(acc[1]), f2u(acc[2]), f2u(acc[3])});
  sts64(fp + (h ? 384 + 2 * j : 256 + 4 * j) * 4, (v2u){f2u(acc[4]), f2u(acc[5])});
  sts64(fp + (h ? 448 + 2 * j : 258 + 4 * j) * 4, (v2u){f2u(acc[6]), f2u(acc[7])});
  if (lane <= 32) sts32(fp + (512 + 128 * h + j) * 4, f2u(acc[8]));
  if (h == 0) {
#pragma unroll
    for (int i = 1; i < 4; ++i) sts32(fp + (512 + j + 32 * i) * 4, f2u(acc[8 + i]));
  }
  return __builtin_amdgcn_ldexpf(1.f, ex);
}
DEV void kg_table0(unsigned cb, unsigned fp, int g, int lane) {
  auto f1 = [&](int i) { return opq(u2f(lds32(fp + i * 4)) + u2f(lds32(fp + FPOS + i * 4))); };
  if (g == 0) {
    const int j = lane & 31, h = lane >> 5;
    const unsigned a = fp + (h ? 256 + 4 * j : 8 * j) * 4;
    const v4u l0 = lds128(a), h0 = lds128(a + 16), l1 = lds128(a + FPOS), h1 = lds128(a + FPOS + 16);
    const float e0 = opq(u2f(l0.x) + u2f(l1.x)), e1 = opq(u2f(l0.y) + u2f(l1.y)), e2 = opq(u2f(l0.z) + u2f(l1.z)), e3 = opq(u2f(l0.w) + u2f(l1.w));
    const float e4 = opq(u2f(h0.x) + u2f(h1.x)), e5 = opq(u2f(h0.y) + u2f(h1.y)), e6 = opq(u2f(h0.z) + u2f(h1.z)), e7 = opq(u2f(h0.w) + u2f(h1.w));
    const v4u o0 = {pkh(e0, e1), pkh(e2, e3), pkh(e4, e5), pkh(e6, e7)};
    const v4u o1 = {pkh(e0, e0), pkh(e1, e2), pkh(e2, e2), pkh(e3, e4)};
    sts128(cb + 32 + lane * 16, h ? o1 : o0);
    const float fa = f1(512), fb = f1(513);
    const unsigned aa = pkh(fa, fa), bb = pkh(fb, fb);
    if (lane < 4) sts128(cb + 32 + (128 + lane) * 16, lane == 0 ? (v4u){aa, aa, pkh(fa, fb), bb} : lane == 1 ? (v4u){bb, bb, 0u, 0u} : (v4u){0u, 0u, 0u, 0u});
  } else {
    const float a = f1(384 + 2 * lane), b = f1(385 + 2 * lane), c = f1(386 + 2 * lane);
    sts128(cb + 32 + (64 + lane) * 16, (v4u){pkh(a, a), pkh(a, b), pkh(b, b), pkh(c, c)});
    const float lo = f1(640 - 2 * lane), hi = f1(lane ? 641 - 2 * lane : 640);
    sts32(cb + CH_R3 + lane * 4, pkh(lane ? hi : 0.f, lo));
    if (lane < 16) sts32(cb + CH_R3 + 256 + lane * 4, 0u);
    if (lane < 32) sts32(cb + (lane >> 3) * UCP + (lane & 7) * 4, 0u);
  }
}
DEV void kg_copies(unsigned cb, int g, int lane) {
  auto one = [&](int chunk) {
    const unsigned o = cb + 32 + chunk * 16;
    const v2u pv = lds64(o - 8);
    const v4u e = lds128(o);
    const unsigned a0 = __builtin_amdgcn_alignbit(pv.y, pv.x, 16), a1 = __builtin_amdgcn_alignbit(e.x, pv.y, 16), a2 = __builtin_amdgcn_alignbit(e.y, e.x, 16),
                   a3 = __builtin_amdgcn_alignbit(e.z, e.y, 16), a4 = __builtin_amdgcn_alignbit(e.w, e.z, 16);
    sts128(o + UCP, (v4u){a1, a2, a3, a4});
    sts128(o + 2 * UCP, (v4u){pv.y, e.x, e.y, e.z});
    sts128(o + 3 * UCP, (v4u){a0, a1, a2, a3});
  };
  one(64 * g + lane);
  if (lane < 2) one(128 + 2 * g + lane);
}
DEV void conv_load_u(rsrc_t ws, unsigned grp, int d0, int w, int lane) {
  v4u v[2];
#pragma unroll
  for (int i = 0; i < 2; ++i)
    v[i] = __builtin_amdgcn_raw_buffer_load_b128(ws, (lane >> 1) * 10240 + (lane & 1) * 16, WS_XNT + (64 * w + 32 * i) * 10240 + d0 * 16, SC1);
  if (lane < 16) sts128(grp + (w >> 1) * CH_BYTES + CH_U + (w & 1) * 256 + lane * 16, (v4u){0u, 0u, 0u, 0u});
#pragma unroll
  for (int i = 0; i < 2; ++i) sts128(grp + (lane & 1) * CH_BYTES + CH_U + 512 + (64 * w + 32 * i + (lane >> 1)) * 16, v[i]);
}
template <class Hook, class F1>
DEV float ssm_prepare(rsrc_t ws, const KgIn& k, unsigned grp, unsigned fp, unsigned cnt, int d0, int w, int lane, Hook hook, F1 f1) {
  const int ci = w >> 1, g = w & 1;
  const float isc = kg_wave(k, g, grp + w * KSCR, fp + w * FPOS, lane, hook);
  if (grp == GRP) sts32(W_ISC + ci * 4, f2u(isc));
  group_sync(cnt, 4, lane);
  kg_table0(grp + ci * CH_BYTES, fp + 2 * ci * FPOS, g, lane);
  group_sync(cnt + 4 + ci * 4, 2, lane);
  kg_copies(grp + ci * CH_BYTES, g, lane);
  f1();
  conv_load_u(ws, grp, d0, w, lane);
  group_sync(cnt + 12, 4, lane);
  return isc;
}

template <int J0, int NJ, class Go, class Done>
DEV void conv_tiles(rsrc_t ws, unsigned cb, int d, float isc, int lane, Go go, Done done) {
  constexpr int B0 = J0 > 3 ? J0 - 3 : 0, NB = J0 + NJ - B0;
  const int g = lane >> 4, c16 = lane & 15;
  const unsigned ga = cb + (c16 >> 2) * UCP + 64 - 16 * g + 8 * (lane & 3);
  const unsigned ba = cb + CH_U + 480 + 32 * c16 + 16 * g;
  f4v acc[NJ] = {};
  {
    unsigned w7[4], w9[4];
#pragma unroll
    for (int i = 0; i < 4; ++i) {
      const int k0 = 8 * g + 2 * i - c16;
      w7[i] = ((k0 >= 5 && k0 <= 11) ? 0x3c00u : 0u) | ((k0 >= 4 && k0 <= 10) ? 0x3c000000u : 0u);
      w9[i] = ((k0 >= 4 && k0 <= 12) ? 0x3c00u : 0u) | ((k0 >= 3 && k0 <= 11) ? 0x3c000000u : 0u);
    }
    const v4u W7 = {w7[0], w7[1], w7[2], w7[3]}, W9 = {w9[0], w9[1], w9[2], w9[3]};
    const unsigned r3 = cb + CH_R3 + (127 - c16 + 4 * g) * 4;
    const v4u g4 = ldstr2(ga + 2048, ga + 2048 - 8);
#pragma unroll
    for (int b = 0; b + 4 < J0 + NJ; ++b) {
      const f4v b7 = mfma16h(lds128(ba - 32 + 512 * b), W7, (f4v){}), b9 = mfma16h(lds128(ba - 16 + 512 * b), W9, (f4v){});
      const v4u bx = {pkh(b7[0], b9[0]), pkh(b7[1], b9[1]), pkh(b7[2], b9[2]), pkh(b7[3], b9[3])};
#pragma unroll
      for (int j = (b + 4 > J0 ? b + 4 : J0); j < J0 + NJ; ++j) {
        const unsigned a = r3 - 64 * (j - b);
        acc[j - J0] = mfma16h(bx, (v4u){lds32(a), lds32(a + 4), lds32(a + 8), lds32(a + 12)}, acc[j - J0]);
      }
      if (b + 4 >= J0) acc[b + 4 - J0] = mfma16h(g4, lds128(ba + 512 * b), acc[b + 4 - J0]);
    }
  }
  v4u G[2][4], B[2][NB];
  auto rd = [&](int e, int i) {
    constexpr int top = NJ;
    if (i == 0) G[e & 1][0] = ldstr2(ga + 64 * e, ga + 64 * e - 8);
    else if (i <= top) B[e & 1][NB - top + i - 1] = lds128(ba + 512 * (B0 + NB - top + i - 1) - 64 * e);
    else {
      const int k = i - top - 1;
      if (NB == top || (k & 1) == 0) { const int c = NB == top ? k + 1 : k / 2 + 1; G[e & 1][c] = ldstr2(ga + 512 * c + 64 * e, ga + 512 * c + 64 * e - 8); }
      else { const int bi = NB - top - 1 - k / 2; B[e & 1][bi] = lds128(ba + 512 * (B0 + bi) - 64 * e); }
    }
  };
#pragma unroll
  for (int i = 0; i < 4 + NB; ++i) rd(0, i);
  SCHED();
  go();
#pragma unroll
  for (int e = 0; e < 8; ++e) {
    int m = 0;
#pragma unroll
    for (int c = 0; c < 4; ++c)
#pragma unroll
      for (int j = (c > J0 ? c : J0); j < J0 + NJ; ++j) {
        acc[j - J0] = mfma16h(G[e & 1][c], B[e & 1][j - c - B0], acc[j - J0]);
        if (e < 7 && m < 4 + NB) rd(e + 1, m);
        if (e == 6 && m == 3 + NB) done();
        ++m;
        SCHED();
      }
  }
  unsigned pk[NJ][2];
#pragma unroll
  for (int j = 0; j < NJ; ++j) { pk[j][0] = pkbf(opq(acc[j][0] * isc), opq(acc[j][1] * isc)); pk[j][1] = pkbf(opq(acc[j][2] * isc), opq(acc[j][3] * isc)); }
  const unsigned so = WS_ST + d * 4096 + J0 * 512;
#pragma unroll
  for (int j = 0; j + 1 < NJ; j += 2) {
    const auto x = __builtin_amdgcn_permlane16_swap(pk[j][0], pk[j + 1][0], false, false);
    const auto y = __builtin_amdgcn_permlane16_swap(pk[j][1], pk[j + 1][1], false, false);
    __builtin_amdgcn_raw_buffer_store_b128((v4u){x[0], y[0], x[1], y[1]}, ws, so + (j + (g & 1)) * 512 + 32 * c16 + 16 * (g >> 1), 0, SC1);
  }
  __builtin_amdgcn_raw_buffer_store_b64((v2u){pk[NJ - 1][0], pk[NJ - 1][1]}, ws, so + (NJ - 1) * 512 + 32 * c16 + 8 * g, 0, SC1);
}
template <class Go, class Done>
DEV void conv_channel(rsrc_t ws, unsigned cb, int d, int half, float isc, int lane, Go go, Done done) {
  if (half) conv_tiles<5, 3>(ws, cb, d, isc, lane, go, done);
  else conv_tiles<0, 5>(ws, cb, d, isc, lane, go, done);
}

DEV void retile(v4u (&rows)[4], unsigned stg, int lane) {
#pragma unroll
  for (int j = 0; j < 4; ++j) sts128(stg + j * 1024 + (lane >> 3) * 128 + 16 * ((lane & 7) ^ (((lane >> 4) & 3) << 1)), rows[j]);
  asm volatile("" ::: "memory");
  const int g = lane >> 4, pp = lane & 3, row = 16 * (g >> 1) + 4 * (g & 1) + ((lane & 15) >> 2);
#pragma unroll
  for (int j = 0; j < 4; ++j) {
    const unsigned a = stg + row * 128 + 16 * ((2 * j + (pp >> 1)) ^ (((row >> 1) & 3) << 1)) + 8 * (pp & 1);
    rows[j] = ldstr2(a, a + 1024);
  }
}
DEV void retile_store(rsrc_t ws, int blk, const v4u (&f)[4], int lane) {
#pragma unroll
  for (int j = 0; j < 4; ++j) {
    const int ng = 4 * (blk % 10) + j;
    __builtin_amdgcn_raw_buffer_store_b128(f[j], ws, lane * 16 + WS_W2T + (((ng / 5) * 54 + blk / 10) * 5 + ng % 5) * 1024, 0, SC1);
  }
}

__global__ __launch_bounds__(768, 3) void fused_kernel(Params p) {
  extern __shared__ __attribute__((aligned(16))) unsigned char lds_base[];
  unsigned long long pc;
  unsigned code_bytes;
  asm volatile("s_getpc_b64 %0\n.Lfused_begin:\n\ts_mov_b32 %1, .Lfused_end_a-.Lfused_begin\n\ts_max_i32 %1, %1, .Lfused_end_b-.Lfused_begin"
               : "=s"(pc), "=s"(code_bytes) : : "scc");
  const int lane = threadIdx.x & 63, wave = __builtin_amdgcn_readfirstlane(threadIdx.x >> 6), role = wave >> 2, w = wave & 3;
  const int xcd = blockIdx.x & 7, slot = blockIdx.x >> 3, tb = 32 * xcd + slot;
  const bool gemm_wg = slot < 28;
  const int rp = 4 * (xcd >> 1) + slot / 7, cp = 7 * (xcd & 1) + slot % 7, t0 = 128 * rp, f0 = 128 * cp;
  STAMP_INIT();
  STAMP(0);
  asm volatile("" ::"v"((LAS unsigned char*)lds_base));

  auto lane_x = [&] { const int l = fresh_lane(); return (unsigned)((l >> 3) * 1280 + 16 * ((l & 7) ^ (l >> 4))); };
  auto lane_w = [&] {
    const int l = fresh_lane(), c = (l & 15) ^ ((l >> 4) << 2);
    return (cp == 13 && c >= 8) ? 0x40000000u : (unsigned)((l >> 4) * 3456 + 16 * c);
  };
  const unsigned sx0 = (t0 + 32 * w) * 1280, sw0 = 16 * w * 3456 + 2 * f0;
  const int rot = ROT7;
  auto slice = [&](int i) { return i < 3 ? i : 3 + (i - 3 + rot) % 7; };
  auto stage_x = [&](int i) {
    const unsigned v0 = lane_x(), v[4] = {v0, (v0 ^ 64) + 9216, v0 + 2 * 9216, (v0 ^ 64) + 3 * 9216};
    dma4(make_srd(p.x, X_BYTES), v, sx0 + 128 * slice(i), ((2 * i) % 7) * UNIT + w * 4096);
  };
  auto stage_w = [&](int i) {
    const unsigned v0 = lane_w(), v[4] = {v0, v0 + 12800, v0 + 2 * 12800, v0 + 3 * 12800};
    dma4(make_srd(p.w1, W_BYTES), v, sw0 + 64 * 3456 * slice(i), ((2 * i + 1) % 7) * UNIT + w * 4096);
  };
  const rsrc_t ws = make_rsrc(p.ws, WS_BYTES), xr = make_rsrc(p.x, X_BYTES);
  const v4u wss = make_srd(p.ws, WS_BYTES);
  if (role == 0 && gemm_wg) { PRIO(3); stage_x(0); stage_w(0); }
  if (wave >= 10) dma1(make_srd((const void*)pc, code_bytes & ~15u), lane * 16, (2 * slot + wave - 10) * 1024, JUNK);
  const int tt = 4 * xcd + (slot >> 3), ct = slot & 7;
  const unsigned wq = WS_W2T + ct * 54 * 5120, wr = wq + (14 * w - (w == 3)) * 5120;
  if (threadIdx.x < N_ZERO) sts32(CNT + threadIdx.x * 4, 0u);
  auto w2_regs = [&](v4u (&R)[35], int lo, int hi) {
#pragma unroll
    for (int j = lo; j < hi; ++j) { R[j] = __builtin_amdgcn_raw_buffer_load_b128(ws, lane * 16 + (j & 3) * 1024, wr + 7 * 5120 + (j >> 2) * 4096, SC1); SCHED(); if (RPACE) { __builtin_amdgcn_s_sleep(RPACE); SCHED(); } }
  };
  auto feed_gemm2 = [&](const v4u (&R)[35]) {
    STAMP(6);
#pragma unroll 1
    for (int i = 0; i < 5; ++i) {
      const int sl = 4 * i + w, qq = sl / 5;
      if (gemm_wg) await<2>(E_DONE + (sl < 6 ? 7 : sl < 12 ? 8 : 9) * 4, 4);
      dma5_lin_sc1(wss, lane * 16, wq + (14 * qq - (qq == 3) + sl % 5) * 5120, sl * 5120);
    }
    WAITV(0);
    signal(E_W0, lane);
    STAMP(7);
    await<2>(E_CONV, 4);
    dma5_lin_sc1(wss, lane * 16, wr + 5 * 5120, SLOT_HI + w * 10240);
    dma5_lin_sc1(wss, lane * 16, wr + 6 * 5120, SLOT_HI + w * 10240 + 5120);
    WAITV(0);
    signal(E_W1, lane);
    STAMP(8);
    PRIO(3);
    const unsigned lo = w * 25600 + lane * 16, hi = SLOT_HI + w * 10240 + lane * 16;
    await<1>(E_QD + w * 4, 2);
#pragma unroll
    for (int i = 0; i < 10; ++i) sts128(lo + i * 1024, R[i]);
    signal(E_QF + w * 4, lane);
    await<1>(E_QD + 16 + w * 4, 2);
#pragma unroll
    for (int i = 10; i < 20; ++i) sts128(lo + i * 1024, R[i]);
    signal(E_QF + 16 + w * 4, lane);
    await<1>(E_QD + 32 + w * 4, 2);
#pragma unroll
    for (int i = 20; i < 25; ++i) sts128(lo + i * 1024, R[i]);
#pragma unroll
    for (int i = 25; i < 30; ++i) sts128(hi + (i - 25) * 1024, R[i]);
    await<1>(E_QD + 48 + w * 4, 2);
#pragma unroll
    for (int i = 30; i < 35; ++i) sts128(hi + (i - 25) * 1024, R[i]);
    signal(E_QF + 32 + w * 4, lane);
    STAMP(9);
    if (w == 0) { poll_words(ws, WS_F3 + (ct * 32 + (lane & 31)) * 128, lds_tag()); signal(E_F3OK, lane); } else { await<1>(E_F3OK, 1); }
    v4u sv[3];
#pragma unroll
    for (int i = 0; i < 3; ++i)
      sv[i] = __builtin_amdgcn_raw_buffer_load_b128(ws, (lane >> 3) * 4096 + (lane & 7) * 16, WS_ST + (80 * ct + 8 * min(w + 4 * i, 9)) * 4096 + tt * 128, SC1);
#pragma unroll
    for (int i = 0; i < 3; ++i) sts128(STILE + (8 * min(w + 4 * i, 9) + (lane >> 3)) * 160 + (lane & 7) * 16, sv[i]);
    signal(E_SRDY, lane);
    STAMP(10);
    STAMP_FLUSH();
  };

  if (role == 1) {
    const int token = 8 * tb + 2 * w + (lane >> 5);
    const int fi = 4 * xcd + slot - 28, blk = w < 2 ? 2 * tb + w : (!gemm_wg && w == 2 && fi < 28) ? 512 + fi : -1;
    PRIO(3);
    LnIn ln = ln_load(make_srd(p.x, X_BYTES), wss, make_srd(p.w2, blk < 0 ? 0u : W_BYTES), token, max(blk, 0), lane);
    BAR();
    ln_wait<0>(ln);
    const unsigned tag = epoch_tag(ln.e, w == 0, lane);
    retile(ln.b, GRP + w * KSCR, lane);
    signal(E_RT, lane);
    STAMP(1);
    const f2v stat = ln_compute(ln, 2 * w + (lane >> 5), lane);
    group_sync<0>(E_LN, 4, lane);
    if ((lane & 31) == 0) __builtin_amdgcn_raw_buffer_store_b128((v4u){f2u(stat.x), tag, f2u(stat.y), tag}, ws, WS_STAT + token * 16, 0, SC1);
    ln_publish(ws, tb, w, lane);
    if (blk >= 0) retile_store(ws, blk, ln.b, lane);
    STAMP(11);
    PRIO(0);
    publish(ws, C_F1, 4, WS_F1 + tb * 128, tag, lane);
    STAMP(12);
    if (gemm_wg) {
      await<SLL>(E_DONE + FALL_AT * 4, 4);
      STAMP(2);
      if (w == 0) {
        poll_words(ws, WS_FALL + xcd * 128, tag);
        signal(E_F1OK, lane);
        if (blockIdx.x == 0 && lane == 0) __builtin_amdgcn_raw_buffer_store_b32(lds32(W_TAG + 4) + 1u, ws, WS_EPOCH, 0, SC1);
      } else {
        await<1>(E_F1OK, 1);
      }
      STAMP(5);
      v4u R[35];
      w2_regs(R, 0, 35);
      feed_gemm2(R);
      asm volatile(".Lfused_end_a:");
      return;
    }
  }
  if (!gemm_wg || role == 2) {
    const bool last_pair = role == (gemm_wg ? 2 : 1);
    const unsigned grp = last_pair ? GRP : (role >> 1) * (4 * KSCR + 4 * FPOS), cnt = C_GRP + role * 48;
    const int dg = 80 * xcd + (gemm_wg ? 2 * slot : 56 + 6 * (slot - 28) + 2 * role);
    const KgIn kin = kg_load(p, dg + (w >> 1), lane);
    if (role != 1) {
      BAR();
      if (gemm_wg) {
        stage_x(1); stage_w(1); stage_x(2); stage_w(2);
        WAITV(8); signal(E_FULL + 4, lane);
        WAITV(0); signal(E_FULL + 8, lane);
      }
    }
    float isc = ssm_prepare(ws, kin, grp, last_pair ? LNST : grp + 4 * KSCR, cnt, dg, w, lane,
      [&](int i) { if (last_pair) await(i ? C_F1 : E_RT, 4); },
      [&] {
        STAMP(2);
        if (role == 0) {
          await(E_LN, 4);
          const unsigned tag = lds_tag();
          poll_words(ws, WS_F1 + (64 * w + lane) * 128, tag);
          if (lds_arrive(C_AX, lane) == 3) {
            if (slot == 28 && lane == 0) __builtin_amdgcn_raw_buffer_store_b32(tag, ws, WS_FALL + xcd * 128, 0, SC1);
            signal(E_F1OK, lane);
          }
          await<1>(E_F1OK, 1);
        } else {
          await<SLA>(E_F1OK, 1);
          if (gemm_wg) await<SLA>(U_AT, 4);
        }
        STAMP(3);
      });
    STAMP(role == 2 ? 6 : 16);
    if (role != 1) {
      unsigned cb = grp + (w >> 1) * CH_BYTES;
      int d = dg + (w >> 1);
#pragma unroll 1
      for (int last = gemm_wg; last < 2; ++last) {
        conv_channel(ws, cb, d, w & 1, isc, lane, [&] { if (gemm_wg) await(E_GELU, 4); if (role) STAMP(7); }, [&] { signal(last ? E_CONV : E_R1, lane); });
        if (role == 0) break;
        if (!last) {
          await(C_GRP + 48 + 12, 4);
          cb = GRP + (w >> 1) * CH_BYTES;
          d -= 2;
          isc = u2f(peek(W_ISC + (w >> 1) * 4));
        }
      }
      if (role == 0) { WAITV(0); signal(E_SDR, lane); STAMP(5); } else { STAMP(8); }
    } else {
      STAMP(5);
      v4u R[35];
      w2_regs(R, 0, 35);
      await<2>(E_R1, 8);
      feed_gemm2(R);
      return;
    }
  } else if (cp == 13 && (w & 1)) {
    BAR();
    WAITV(0); signal(E_FULL, lane);
#pragma unroll 1
    for (int i = 0; i < 10; ++i) signal(E_DONE + i * 4, lane);
    stage_x(3);
#pragma unroll 1
    for (int i = 3; i < 10; ++i) {
      await<1>(E_DONE + (i - 3) * 4, 4);
      stage_w(i);
      if (i < 9) { stage_x(i + 1); WAITV(4); } else { WAITV(0); }
      signal(E_FULL + i * 4, lane);
    }
    signal(E_VMQ, lane);
    await(E_LN, 4);
    const unsigned tag = lds_tag();
    signal(E_GELU, lane);
    publish(ws, C_H, 4, WS_F2 + (rp * 16 + cp) * 128, tag, lane);
  } else {
    const int wf = w & 1, wt = w >> 1, h = lane >> 5;
    BAR();
    PRIO(2);
    unsigned aa[2][2], ba[2][4];
    {
      const int q = (lane & 15) >> 2, pp = lane & 3, fb = (lane >> 4) & 1;
#pragma unroll
      for (int mi = 0; mi < 2; ++mi) {
        aa[0][mi] = (8 * h + q) * 256 + 16 * ((8 * wf + 4 * (mi ^ wt) + 2 * fb + (pp >> 1)) ^ (q << 2)) + 8 * (pp & 1);
        aa[1][mi] = aa[0][mi] + 4 * UNIT;
        asm volatile("" : "+v"(aa[0][mi]), "+v"(aa[1][mi]));
      }
#pragma unroll
      for (int s = 0; s < 4; ++s) {
        ba[0][s] = (64 * wt + (lane & 31)) * 128 + 16 * ((2 * s + h) ^ ((lane >> 1) & 7));
        ba[1][s] = ba[0][s] + 4 * UNIT;
        asm volatile("" : "+v"(ba[0][s]), "+v"(ba[1][s]));
      }
    }
    f16v acc[2][2] = {};
    v4u A[3][2], B[3][2], st[2];
    unsigned b1v[2], fl = 0, dn = 0, sbase = sig_base(lane), one = 1u;
    asm volatile("" : "+v"(sbase), "+v"(one));
    f4v cs4 = {};
    const unsigned sel1 = (((lane >> 2) ^ (lane >> 4)) & 1) ? 0u : 0x3f803f80u;
    const v4u sel = {sel1, sel1, sel1, sel1};
    auto rdA = [&](int n, int mi) {
      const int u = (2 * (n >> 2) + 1) % 7;
      const unsigned a = aa[u >> 2][mi] + (u & 3) * UNIT + (n & 3) * 4096;
      A[n % 3][mi] = ldstr2(a, a + 1024);
    };
    auto rdB = [&](int n, int ni) {
      const int u = (2 * (n >> 2)) % 7;
      B[n % 3][ni] = lds128(ba[u >> 2][n & 3] + (u & 3) * UNIT + ni * 4096);
    };
    const v4u xs = make_srd(p.x, X_BYTES), w1s = make_srd(p.w1, W_BYTES);
    const unsigned vx0 = lane_x(), vxo = vx0 ^ 64, vw0 = lane_w();
    unsigned sx[4], sw[4], mb = w * 4096;
#pragma unroll
    for (int j = 0; j < 4; ++j) {
      sx[j] = sx0 + (3 + rot) * 128 + j * 9216;
      sw[j] = sw0 + (3 + rot) * 64 * 3456 + j * 12800;
      asm volatile("" : "+s"(sx[j]), "+s"(sw[j]));
    }
    auto piece = [&](auto qc) {
      constexpr int q = qc, i = (q + 8) / 8 + 2 + ((q >> 2) & 1), j = q & 3;
      if constexpr (q >= -4 && q < 52) {
        if constexpr (q & 4) {
          if constexpr (j == 0) dma1(xs, vx0, sx[0], mb + ((2 * i) % 7) * UNIT);
          else dma_next<j * 1024>(xs, (j & 1) ? vxo : vx0, sx[j]);
          sx[j] += i + rot == 9 ? -6 * 128 : 128;
          asm volatile("" : "+s"(sx[j]));
        } else {
          if constexpr (j == 0) dma1(w1s, vw0, sw[0], mb + ((2 * i + 1) % 7) * UNIT);
          else dma_next<j * 1024>(w1s, vw0, sw[j]);
          sw[j] += i + rot == 9 ? -6 * 64 * 3456 : 64 * 3456;
          asm volatile("" : "+s"(sw[j]));
        }
      }
    };
    auto load_stats = [&] {
#pragma unroll
      for (int i = 0; i < 2; ++i) st[i] = __builtin_amdgcn_raw_buffer_load_b128(ws, (lane & 31) * 16, WS_STAT + (t0 + 64 * wt + 32 * i) * 16, SC1);
    };
    WAITV(0);
    group_sync(E_FULL, 4, lane);
    STAMP(2); STAMPC(11);
    rdA(0, 0); rdB(0, 0); rdB(0, 1); rdA(0, 1); rdA(1, 0); rdB(1, 0); rdB(1, 1); rdA(1, 1);
    SCHED();
    unsigned tag = 0;
    v4u eb[2];
    static_for<40>([&](auto nc) {
      constexpr int n = nc, sg = n >> 2, r = n & 3;
      acc[0][0] = mfma32b(A[n % 3][0], B[n % 3][0], acc[0][0]);
      SCHED();
      if (n < 38) { rdA(n + 2, 0); rdB(n + 2, 0); }
      SCHED();
      acc[0][1] = mfma32b(A[n % 3][0], B[n % 3][1], acc[0][1]);
      SCHED();
      piece(IC<2 * (n - 3)>{});
      if (r == 2 && sg < 7) dn = *(volatile LAS unsigned*)lp(sbase + (E_DONE - CNT) + sg * 4);
      if (r == 1 && sg < 9) fl = *(volatile LAS unsigned*)lp(sbase + (E_FULL - CNT) + (sg + 1) * 4);
      if (n == 34) { dn = *(volatile LAS unsigned*)lp(sbase + (E_LN - CNT)); tag = *(volatile LAS unsigned*)lp(sbase + (W_TAG - CNT)); }
      SCHED();
      cs4 = mfma16b(sel, A[n % 3][0], cs4);
      SCHED();
      acc[1][0] = mfma32b(A[n % 3][1], B[n % 3][0], acc[1][0]);
      SCHED();
      if (n < 38) { rdB(n + 2, 1); rdA(n + 2, 1); }
      if (r == 1) signal_at<E_DONE - CNT + sg * 4>(sbase, one);
      if (n == 39) { sts32(SEXC + w * 128 + (lane & 31) * 4, f2u(cs4[0])); signal(E_SX + w * 4, lane); }
      SCHED();
      acc[1][1] = mfma32b(A[n % 3][1], B[n % 3][1], acc[1][1]);
      SCHED();
      piece(IC<2 * (n - 3) + 1>{});
      if (r == 1 && sg < 9) { check(fl, E_FULL + (sg + 1) * 4, 4); if (sg & 1) STAMPL(16 + (sg >> 1)); }
      if (r == 2 && sg < 7) check(dn, E_DONE + sg * 4, 4);
      if (r == 0 && sg >= 2 && sg < 9) {
        if (sg < 8) WAITV(8); else WAITV(4);
        signal_at<E_FULL - CNT + (sg + 1) * 4>(sbase, one);
      }
      if (n == 29) load_stats();
      if (n == 30) {
#pragma unroll
        for (int i = 0; i < 2; ++i)
          b1v[i] = __builtin_amdgcn_raw_buffer_load_b16(make_rsrc(p.b1, F_ * 2), (lane & 31) * 2, (f0 + 64 * wf + 32 * (i ^ wt)) * 2, 0);
        signal_at<E_VMQ - CNT>(sbase, one);
      }
      if (n == 35) {
        if (__builtin_expect(__builtin_amdgcn_readfirstlane(dn) < 4, 0)) { await(E_LN, 4); tag = peek(W_TAG); }
        tag = __builtin_amdgcn_readfirstlane(tag);
      }
      if (n == 37) {
        if (__builtin_expect(!__all(st[0].y == tag && st[0].w == tag && st[1].y == tag && st[1].w == tag), 0)) {
#pragma unroll 1
          for (unsigned s = 0; s < SPIN_CAP; ++s) {
            asm volatile("" ::: "memory");
            load_stats();
            if (__all(st[0].y == tag && st[0].w == tag && st[1].y == tag && st[1].w == tag)) break;
          }
        }
      }
      if (n == 37 || n == 38) {
        constexpr int i = n == 38;
        const float mu = -u2f(st[i].x), ir = u2f(st[i].z), mh = bfr(mu), ih = bfr(ir);
        eb[i] = h ? (v4u){0u, 0u, 0u, 0u} : (v4u){pkbf(mh, opq(mu - mh)), pkbf(mh, 0.f), pkbf(ih, opq(ir - ih)), 0u};
        asm volatile("" : "+v"(eb[i]));
      }
      SCHED();
    });
    STAMP(3); STAMPC(12);
    auto ext = [&](int mi, float S) {
      const float sh = bfr(S);
      const v4u ea = h ? (v4u){0u, 0u, 0u, 0u} : (v4u){pkbf(sh, sh), pkbf(S - sh, 0.f), b1v[mi] | (b1v[mi] << 16), 0u};
      acc[mi][0] = mfma32b(ea, eb[0], acc[mi][0]);
      acc[mi][1] = mfma32b(ea, eb[1], acc[mi][1]);
    };
    fl = peek(E_SX + (w ^ 2) * 4);
    float so = u2f(peek(SEXC + (w ^ 2) * 128 + (lane & 31) * 4));
    ext(0, cs4[0]);
    SCHED();
    if (__builtin_expect(__builtin_amdgcn_readfirstlane(fl) < 1, 0)) {
      await(E_SX + (w ^ 2) * 4, 1);
      so = u2f(peek(SEXC + (w ^ 2) * 128 + (lane & 31) * 4));
    }
    ext(1, so);
    STAMP(1);
    {
      const unsigned hv = ((lane >> 4) & 1) * 55296 + h * 256 + (lane & 15) * 16;
#pragma unroll
      for (int mi = 0; mi < 2; ++mi) {
#pragma unroll
        for (int ni = 0; ni < 2; ++ni) {
          const float ir = u2f(st[ni].z), rho = __builtin_amdgcn_rcpf(ir);
          const float k1 = -2.302208198f * rho, k2 = k1 * 0.044715f * rho * rho;
          const unsigned hs = WS_H + ((t0 + 64 * wt + 32 * ni) / 16) * 55296 + ((f0 + 64 * wf) / 32 + (mi ^ wt)) * 1024;
#pragma unroll
          for (int hf = 0; hf < 2; ++hf) {
            unsigned o[4];
#pragma unroll
            for (int r = 0; r < 4; ++r) {
              const f2v a = {acc[mi][ni][8 * hf + 2 * r], acc[mi][ni][8 * hf + 2 * r + 1]};
              const f2v x = a * (a * a * k2 + k1);
              const f2v e = {__builtin_amdgcn_exp2f(x.x), __builtin_amdgcn_exp2f(x.y)};
              const f2v dd = e * ir + ir;
              const f2v rr = {__builtin_amdgcn_rcpf(dd.x), __builtin_amdgcn_rcpf(dd.y)};
              const f2v gl = a * rr;
              o[r] = pkbf(gl.x, gl.y);
            }
            __builtin_amdgcn_raw_buffer_store_b128((v4u){o[0], o[1], o[2], o[3]}, ws, hv + hs + hf * 512, 0, SC1);
            SCHED();
          }
        }
      }
    }
    signal(E_GELU, lane);
    STAMP(4); STAMPC(13);
    publish(ws, C_H, 4, WS_F2 + (rp * 16 + cp) * 128, tag, lane);
    STAMP(5);
  }

  {
    const int mh = w & 1, q = role + (w >> 1);
    if (wave == 0) {
      PRIO(0);
      poll_words(ws, WS_F2 + ((tt >> 1) * 16 + min(lane, 13)) * 128, lds_tag());
      signal(E_F2OK, lane);
    } else {
      await<1>(E_F2OK, 1);
    }
    if (role == 0) STAMP(6);
    PRIO(3);
    v4u H[7][2], A[2][5];
    f4v acc[2][5] = {};
    const unsigned hs = WS_H + (4 * tt + 2 * mh) * 55296 + (14 * q - (q == 3)) * 1024;
    const unsigned hv = lane * 16;
    auto ldh = [&](int k, int ti) { H[k % 7][ti] = __builtin_amdgcn_raw_buffer_load_b128(ws, hv, hs + ti * 55296 + k * 1024, SC1); SCHED(); };
#pragma unroll
    for (int k = 0; k < 3; ++k) { ldh(k, 0); ldh(k, 1); }
    if (role == 2) { if (!gemm_wg) await(E_SDR, 4); publish(ws, C_GRP + 96 + 36, 4, WS_F3 + tb * 128, lds_tag(), lane); STAMP(9); }
    const int fo = q < 2 ? 3 * q : 2 + 2 * q;
    v2u xv[3], bv[3];
    unsigned alo = q * 25600 + lane * 16, ahi = SLOT_HI + q * 10240 + lane * 16, fl = 0, sq = sig_base(lane) + q * 4, one = 1u;
    asm volatile("" : "+v"(alo), "+v"(ahi), "+v"(sq), "+v"(one));
    auto rd = [&](int k, int n) { A[k & 1][n] = lds128((k % 7 < 5 ? alo + (k % 7) * 5120 : ahi + (k % 7 - 5) * 5120) + n * 1024); };
    await<1>(E_W0, 4);
#pragma unroll
    for (int n = 0; n < 5; ++n) rd(0, n);
    if (role == 0) { STAMP(7); STAMPC(14); }
    SCHED();
    auto step = [&](auto kc) {
      constexpr int k = kc;
      if (k == 3 || k == 7 || k == 10) PRIO(k == 3 ? 2 : k == 7 ? 1 : 0);
#pragma unroll
      for (int ti = 0; ti < 2; ++ti) {
#pragma unroll
        for (int n = 0; n < 5; ++n) {
          acc[ti][n] = mfma16b(A[k & 1][n], H[k % 7][ti], acc[ti][n]);
          SCHED();
          if (ti == 0 && k < 13) {
            rd(k + 1, n);
            if (n == 4 && k == 0) signal_at<E_QD - CNT>(sq, one);
            if (n == 4 && k == 2) signal_at<E_QD - CNT + 16>(sq, one);
            if (n == 4 && k == 4) signal_at<E_QD - CNT + 32>(sq, one);
            if (n == 4 && k == 5) signal_at<E_QD - CNT + 48>(sq, one);
            if (n == 4 && k == 9) signal_at<E_QD - CNT + 64>(sq, one);
            SCHED();
          }
          if (ti == 1 && n == 0) {
            if (k == 3) fl = peek(E_W1);
            if (k == 5) fl = *(volatile LAS unsigned*)lp(sq + (E_QF - CNT));
            if (k == 7) fl = *(volatile LAS unsigned*)lp(sq + (E_QF - CNT) + 16);
            if (k == 9) fl = *(volatile LAS unsigned*)lp(sq + (E_QF - CNT) + 32);
            if (k == 12) fl = *(volatile LAS unsigned*)lp(sq + (E_QD - CNT) + 64);
            SCHED();
          }
        }
        if (k < 2) { ldh(3 + 2 * k + ti, 0); ldh(3 + 2 * k + ti, 1); }
        if (k >= 1 && k < 8) ldh(k + 6, ti);
        if constexpr (k >= 8 && k < 11) {
          const int f = min(fo + k - 8, 9), tl = f >= 5, n = f - 5 * tl;
          const int l1 = fresh_lane();
          if (ti == 0) xv[k - 8] = __builtin_amdgcn_raw_buffer_load_b64(xr, (l1 & 15) * 1280 + 8 * (l1 >> 4), (64 * tt + 32 * mh + 16 * tl) * 1280 + 160 * ct + 32 * n, 0);
          else bv[k - 8] = __builtin_amdgcn_raw_buffer_load_b64(make_rsrc(p.b2, D_ * 2), 8 * (l1 >> 4), 160 * ct + 32 * n, 0);
        }
        SCHED();
      }
      if (k == 3) check(fl, E_W1, 4);
      if (k == 5) check(fl, E_QF + q * 4, 1);
      if (k == 7) check(fl, E_QF + 16 + q * 4, 1);
      if (k == 9) check(fl, E_QF + 32 + q * 4, 1);
      if (role == 0 && (k & 1)) STAMPL(22 + k / 2);
      SCHED();
    };
    static_for<13>(step);
    if (role == 0) step(IC<13>{});
    if (role == 0) { STAMP(8); STAMPC(15); } else { STAMP(4); }
    check(fl, E_QD + 64 + q * 4, 2);
#pragma unroll
    for (int f = 0; f < 10; ++f) sts128(alo + mh * 10240 + f * 1024, __builtin_bit_cast(v4u, acc[f / 5][f % 5]));
    fl = peek(E_SRDY);
    const int l2 = fresh_lane(), g2 = l2 >> 4;
    group_sync<0>(E_RED + mh * 4, 4, l2);
    check(fl, E_SRDY, 4);
    if (role == 0) STAMP(9);
    auto finish = [&](int i) {
      const int f = fo + i, tl = f >= 5, n = f - 5 * tl;
      const unsigned ra = mh * 10240 + f * 1024 + l2 * 16;
      const f4v p0 = __builtin_bit_cast(f4v, lds128(ra)), p1 = __builtin_bit_cast(f4v, lds128(ra + 25600));
      const f4v p2 = __builtin_bit_cast(f4v, lds128(ra + 51200)), p3 = __builtin_bit_cast(f4v, lds128(ra + 76800));
      const v2u sv = ldstr(STILE + (16 * n + (l2 >> 2)) * 160 + (32 * mh + 16 * tl + 4 * (l2 & 3)) * 2);
      const float bz[4] = {blo(bv[i].x), bhi(bv[i].x), blo(bv[i].y), bhi(bv[i].y)};
      const float sz[4] = {blo(sv.x), bhi(sv.x), blo(sv.y), bhi(sv.y)};
      const float xz[4] = {blo(xv[i].x), bhi(xv[i].x), blo(xv[i].y), bhi(xv[i].y)};
      float r[4];
#pragma unroll
      for (int j = 0; j < 4; ++j) {
        const float z = (p0[j] + p1[j]) + (p2[j] + p3[j]) + bz[j];
        const float gt = z * __builtin_amdgcn_rcpf(1.f + __builtin_amdgcn_exp2f(z * -1.44269504088896f));
        r[j] = fmaf(sz[j], gt, xz[j]);
      }
      __builtin_amdgcn_raw_buffer_store_b64((v2u){pkbf(r[0], r[1]), pkbf(r[2], r[3])}, make_rsrc(p.out, X_BYTES), (l2 & 15) * 1280 + 8 * g2,
                                            (64 * tt + 32 * mh + 16 * tl) * 1280 + 160 * ct + 32 * n, OUT_AUX);
    };
    finish(0); finish(1);
    if (role == 0) finish(2);
    STAMP(10);
    asm volatile(".Lfused_end_b:");
  }
  STAMP_FLUSH();
}

}

extern "C" void kernel_launch(void* const* d_in, const int* in_sizes, int n_in, void* d_out, int out_size, void* d_ws, size_t ws_size,
                              hipStream_t stream) {
  Params p;
  p.x = (const unsigned short*)d_in[0];
  p.lA = (const unsigned short*)d_in[1];
  p.Ai = (const unsigned short*)d_in[2];
  p.Cr = (const unsigned short*)d_in[3];
  p.Ci = (const unsigned short*)d_in[4];
  p.ldt = (const unsigned short*)d_in[5];
  p.Dsk = (const unsigned short*)d_in[6];
  p.w1 = (const unsigned short*)d_in[8];
  p.b1 = (const unsigned short*)d_in[9];
  p.w2 = (const unsigned short*)d_in[10];
  p.b2 = (const unsigned short*)d_in[11];
  p.out = (unsigned short*)d_out;
  p.ws = (unsigned char*)d_ws;
  (void)hipFuncSetAttribute((const void*)fused_kernel, hipFuncAttributeMaxDynamicSharedMemorySize, LDS_BYTES);
  fused_kernel<<<256, 768, LDS_BYTES, stream>>>(p);
}
```

```cpp
#include <hip/hip_runtime.h>
#include <hip/hip_bf16.h>

namespace {

typedef unsigned v4u __attribute__((ext_vector_type(4)));
typedef unsigned v2u __attribute__((ext_vector_type(2)));
typedef short s4v __attribute__((ext_vector_type(4)));
typedef float f2v __attribute__((ext_vector_type(2)));
typedef float f4v __attribute__((ext_vector_type(4)));
typedef float f16v __attribute__((ext_vector_type(16)));
typedef _Float16 h2v __attribute__((ext_vector_type(2)));
typedef _Float16 h8v __attribute__((ext_vector_type(8)));
typedef __bf16 b2v __attribute__((ext_vector_type(2)));
typedef __bf16 b8v __attribute__((ext_vector_type(8)));
#define GAS __attribute__((address_space(1)))
#define LAS __attribute__((address_space(3)))
#define DEV __device__ __forceinline__
typedef __amdgpu_buffer_rsrc_t rsrc_t;

constexpr int L_ = 2048, D_ = 640, F_ = 1728;
constexpr unsigned X_BYTES = L_ * D_ * 2, W_BYTES = D_ * F_ * 2;
constexpr int SC1 = 16;
constexpr unsigned SPIN_CAP = 1u << 16;

constexpr unsigned WS_EPOCH = 0;
constexpr unsigned WS_FALL = 4096;
constexpr unsigned WS_STAT = 32768;
constexpr unsigned WS_F2 = 65536;
constexpr unsigned WS_F1 = 5013504;
constexpr unsigned WS_F3 = WS_F1 + 32768;
constexpr unsigned WS_XNT = 131072;
constexpr unsigned WS_W2T = 2752512;
constexpr unsigned WS_H = 5242880;
constexpr unsigned WS_ST = 12582912;
constexpr unsigned WS_STAMP = 15204352;
constexpr unsigned WS_BYTES = WS_STAMP + 256 * 12 * 64 * 8;

constexpr unsigned LDS_BYTES = 163840;
constexpr unsigned UNIT = 16384;
constexpr unsigned KSCR = 8704;
constexpr unsigned FPOS = 2576;
constexpr unsigned GRP = 7 * UNIT;
constexpr unsigned LNST = GRP + 4 * KSCR;
constexpr unsigned CNT = LNST + 10752;
constexpr unsigned SEXC = CNT + 512;
constexpr unsigned JUNK = CNT + 1024;
constexpr unsigned UCP = 2368;
constexpr unsigned CH_R3 = 4 * UCP, CH_U = 9856, CH_BYTES = 2 * KSCR;
constexpr unsigned SLOT_HI = 102400, STILE = 143360;
constexpr unsigned E_FULL = CNT, E_DONE = CNT + 40;
constexpr unsigned E_W0 = CNT + 80, E_W1 = CNT + 84, E_F2OK = CNT + 88, E_F3OK = CNT + 92, E_RED = CNT + 96;
constexpr unsigned E_QD = CNT + 384, E_QF = CNT + 104;
constexpr unsigned E_GELU = CNT + 184, E_LN = CNT + 188, E_LNPUB = CNT + 192, E_SRDY = CNT + 196, E_VMQ = CNT + 200, E_FWOK = CNT + 204, C_AX = CNT + 156, E_R1 = CNT + 160, E_SDR = CNT + 164, W_ISC = CNT + 472;
constexpr unsigned C_H = CNT + 208, C_F1 = CNT + 212, E_F1OK = CNT + 216, E_CONV = CNT + 220, E_SX = CNT + 368, E_RT = CNT + 360;
constexpr unsigned C_GRP = CNT + 224;
constexpr unsigned W_TAG = CNT + 464;
constexpr int N_ZERO = 116;

struct Params {
  const unsigned short *x, *lA, *Ai, *Cr, *Ci, *ldt, *Dsk, *w1, *b1, *w2, *b2;
  unsigned short* out;
  unsigned char* ws;
};

#define STAMP(i) do { } while (0)
#define STAMPC(i) do { } while (0)
#define STAMPV(i, v) do { } while (0)
#define STAMP_INIT() do { } while (0)
#define STAMP_FLUSH() do { } while (0)

#define PRIO(x) __builtin_amdgcn_s_setprio(x)
#define STAMPL(i) do { } while (0)
#define F2_SLEEP 2
#define OUT_AUX 0
#define SLA 1
#define SLL 2
#define RPACE 1
#define FALL_AT 4
#define U_AT E_VMQ
#define ROT7 ((slot % 7 + (slot / 7 * 7 + 2) / 4) % 7)
#define BAR() asm volatile("s_waitcnt lgkmcnt(0)\n\ts_barrier" ::: "memory")
#define WAITV(n) asm volatile("s_waitcnt vmcnt(" #n ")" ::: "memory")
#define SCHED() __builtin_amdgcn_sched_barrier(0)

template <int I> struct IC { constexpr operator int() const { return I; } };
template <int N, int I = 0, class F> DEV void static_for(F&& f) {
  if constexpr (I < N) { f(IC<I>{}); static_for<N, I + 1>(f); }
}
DEV int fresh_lane() {
  int l;
  asm volatile("v_mbcnt_lo_u32_b32 %0, -1, 0\n\tv_mbcnt_hi_u32_b32 %0, -1, %0" : "=v"(l));
  return l;
}
DEV float u2f(unsigned u) { return __builtin_bit_cast(float, u); }
DEV unsigned f2u(float f) { return __builtin_bit_cast(unsigned, f); }
DEV h2v u2h(unsigned u) { return __builtin_bit_cast(h2v, u); }
DEV float blo(unsigned u) { return u2f(u << 16); }
DEV float bhi(unsigned u) { return u2f(u & 0xffff0000u); }
DEV unsigned pkbf(float a, float b) { return __builtin_bit_cast(unsigned, __builtin_convertvector((f2v){a, b}, b2v)); }
DEV unsigned pkh(float a, float b) { return __builtin_bit_cast(unsigned, __builtin_convertvector((f2v){a, b}, h2v)); }
DEV float opq(float x) { asm("" : "+v"(x)); return x; }
DEV float bfr(float a) { return blo(pkbf(a, a)); }
DEV f16v mfma32b(v4u a, v4u b, f16v c) { return __builtin_amdgcn_mfma_f32_32x32x16_bf16(__builtin_bit_cast(b8v, a), __builtin_bit_cast(b8v, b), c, 0, 0, 0); }
DEV f16v mfma32h(v4u a, v4u b, f16v c) { return __builtin_amdgcn_mfma_f32_32x32x16_f16(__builtin_bit_cast(h8v, a), __builtin_bit_cast(h8v, b), c, 0, 0, 0); }
DEV f4v mfma16b(v4u a, v4u b, f4v c) { return __builtin_amdgcn_mfma_f32_16x16x32_bf16(__builtin_bit_cast(b8v, a), __builtin_bit_cast(b8v, b), c, 0, 0, 0); }
DEV f4v mfma16h(v4u a, v4u b, f4v c) { return __builtin_amdgcn_mfma_f32_16x16x32_f16(__builtin_bit_cast(h8v, a), __builtin_bit_cast(h8v, b), c, 0, 0, 0); }

DEV LAS unsigned char* lp(unsigned a) { return (LAS unsigned char*)(size_t)a; }
DEV v4u lds128(unsigned a) { return *(LAS v4u*)lp(a); }
DEV v2u lds64(unsigned a) { return *(LAS v2u*)lp(a); }
DEV unsigned lds32(unsigned a) { return *(LAS unsigned*)lp(a); }
DEV void sts128(unsigned a, v4u v) { *(LAS v4u*)lp(a) = v; }
DEV void sts64(unsigned a, v2u v) { *(LAS v2u*)lp(a) = v; }
DEV void sts32(unsigned a, unsigned v) { *(LAS unsigned*)lp(a) = v; }
DEV v2u ldstr(unsigned a) { return __builtin_bit_cast(v2u, __builtin_amdgcn_ds_read_tr16_b64_v4i16((LAS s4v*)lp(a))); }
DEV v4u ldstr2(unsigned a0, unsigned a1) { const v2u x = ldstr(a0), y = ldstr(a1); return (v4u){x.x, x.y, y.x, y.y}; }

DEV v4u make_srd(const void* ptr, unsigned bytes) {
  return (v4u){(unsigned)(size_t)ptr, (unsigned)((size_t)ptr >> 32) & 0xffffu, bytes, 0x00020000u};
}
DEV rsrc_t make_rsrc(const void* ptr, unsigned bytes) { return __builtin_amdgcn_make_buffer_rsrc((void*)ptr, 0, bytes, 0x00020000); }
#define DMA_HEAD "s_mov_b32 m0, %0\n\ts_nop 0\n\t"
DEV void dma1(v4u srd, unsigned v, unsigned soff, unsigned lds) {
  asm volatile(DMA_HEAD "buffer_load_dwordx4 %1, %2, %3 offen lds" ::"s"(lds), "v"(v), "s"(srd), "s"(soff) : "memory");
}
template <int OFF> DEV void dma_next(v4u srd, unsigned v, unsigned soff) {
  asm volatile("buffer_load_dwordx4 %0, %1, %2 offen offset:%3 lds" ::"v"(v), "s"(srd), "s"(soff), "i"(OFF) : "memory");
}
DEV void touch(v4u srd, unsigned v) {
  asm volatile(DMA_HEAD "buffer_load_dword %1, %2, 0 offen lds" ::"s"(JUNK + 768), "v"(v), "s"(srd) : "memory");
}
DEV void dma4(v4u srd, const unsigned (&v)[4], unsigned soff, unsigned lds) {
  asm volatile(DMA_HEAD "buffer_load_dwordx4 %1, %5, %6 offen lds\n\tbuffer_load_dwordx4 %2, %5, %6 offen offset:1024 lds\n\t"
               "buffer_load_dwordx4 %3, %5, %6 offen offset:2048 lds\n\tbuffer_load_dwordx4 %4, %5, %6 offen offset:3072 lds"
               ::"s"(lds), "v"(v[0]), "v"(v[1]), "v"(v[2]), "v"(v[3]), "s"(srd), "s"(soff) : "memory");
}
DEV void dma5_lin_sc1(v4u srd, unsigned v, unsigned soff, unsigned lds) {
  asm volatile(DMA_HEAD "buffer_load_dwordx4 %1, %2, %3 offen sc1 lds\n\tbuffer_load_dwordx4 %1, %2, %3 offen offset:1024 sc1 lds\n\t"
               "buffer_load_dwordx4 %1, %2, %3 offen offset:2048 sc1 lds\n\tbuffer_load_dwordx4 %1, %2, %3 offen offset:3072 sc1 lds"
               ::"s"(lds), "v"(v), "s"(srd), "s"(soff) : "memory");
  asm volatile(DMA_HEAD "buffer_load_dwordx4 %1, %2, %3 offen sc1 lds" ::"s"(lds + 4096), "v"(v), "s"(srd), "s"(soff + 4096) : "memory");
}

DEV void poll_words(rsrc_t ws, unsigned off, unsigned tag) {
#pragma unroll 1
  for (unsigned s = 0; s < SPIN_CAP; ++s) {
    asm volatile("" ::: "memory");
    if (__all(__builtin_amdgcn_raw_buffer_load_b32(ws, off, 0, SC1) == tag)) break;
    __builtin_amdgcn_s_sleep(F2_SLEEP);
  }
  asm volatile("" ::: "memory");
}
DEV unsigned sig_base(int lane) { return lane == 0 ? CNT : JUNK + lane * 4; }
template <int OFF> DEV void signal_at(unsigned base, unsigned one) {
  asm volatile("ds_add_u32 %0, %1 offset:%2" ::"v"(base), "v"(one), "i"(OFF) : "memory");
}
DEV void signal(unsigned a, int lane) {
  asm volatile("ds_add_u32 %0, %1" ::"v"(sig_base(lane) + (a - CNT)), "v"(1u) : "memory");
}
DEV unsigned peek(unsigned a) { return *(volatile LAS unsigned*)lp(a); }
template <int SLEEP = 1> DEV void await(unsigned a, unsigned need) {
#pragma unroll 1
  for (unsigned s = 0; s < (SPIN_CAP << 2); ++s) {
    asm volatile("" : "+s"(s));
    if (__builtin_amdgcn_readfirstlane(peek(a)) >= need) break;
    if (SLEEP) __builtin_amdgcn_s_sleep(SLEEP);
  }
  asm volatile("" ::: "memory");
}
DEV void check(unsigned early, unsigned a, unsigned need) {
  if (__builtin_expect(__builtin_amdgcn_readfirstlane(early) < need, 0)) await<1>(a, need);
  asm volatile("" ::: "memory");
}
DEV unsigned lds_arrive(unsigned cnt, int lane) {
  unsigned old;
  asm volatile("ds_add_rtn_u32 %0, %1, %2\n\ts_waitcnt lgkmcnt(0)" : "=&v"(old) : "v"(sig_base(lane) + (cnt - CNT)), "v"(1u) : "memory");
  return __builtin_amdgcn_readfirstlane(old);
}
template <int SLEEP = 1> DEV void group_sync(unsigned cnt, unsigned target, int lane) {
  if (lds_arrive(cnt, lane) + 1 < target) await<SLEEP>(cnt, target);
}
DEV void publish(rsrc_t ws, unsigned cnt, unsigned nwaves, unsigned flag_off, unsigned tag, int lane) {
  WAITV(0);
  if (lds_arrive(cnt, lane) + 1 == nwaves && lane == 0) __builtin_amdgcn_raw_buffer_store_b32(tag, ws, flag_off, 0, SC1);
}
DEV unsigned epoch_tag(unsigned ev, bool share, int lane) {
  const unsigned e = __builtin_amdgcn_readfirstlane(ev);
  const unsigned tag = e * 0x9E3779B1u + 0x7F4A7C15u;
  if (share && lane == 0) sts64(W_TAG, (v2u){tag, e});
  return tag;
}
DEV unsigned lds_tag() { return __builtin_amdgcn_readfirstlane(lds32(W_TAG)); }

template <int CTRL> DEV float dpp_add(float v) {
  return v + u2f(__builtin_amdgcn_update_dpp(0u, f2u(v), CTRL, 0xf, 0xf, false));
}
template <int CTRL> DEV float dpp_max(float v) {
  return fmaxf(v, u2f(__builtin_amdgcn_update_dpp(0u, f2u(v), CTRL, 0xf, 0xf, false)));
}
DEV float half_sum(float v) {
  v = dpp_add<0x128>(v); v = dpp_add<0x124>(v); v = dpp_add<0x122>(v); v = dpp_add<0x121>(v);
  const auto r = __builtin_amdgcn_permlane16_swap(f2u(v), f2u(v), false, false);
  return u2f(r[0]) + u2f(r[1]);
}
DEV float wave_max(float v) {
  v = dpp_max<0x128>(v); v = dpp_max<0x124>(v); v = dpp_max<0x122>(v); v = dpp_max<0x121>(v);
  const auto r = __builtin_amdgcn_permlane16_swap(f2u(v), f2u(v), false, false);
  v = fmaxf(u2f(r[0]), u2f(r[1]));
  const auto q = __builtin_amdgcn_permlane32_swap(f2u(v), f2u(v), false, false);
  return fmaxf(u2f(q[0]), u2f(q[1]));
}

struct LnIn { v2u v[5]; unsigned e; v4u b[4]; };
DEV LnIn ln_load(v4u xs, v4u wss, v4u w2s, int token, int blk, int lane) {
  LnIn r;
  const unsigned a = token * 1280 + (lane & 31) * 8, b = (blk / 10) * 40960 + (blk % 10) * 128 + (lane >> 3) * 1280 + (lane & 7) * 16;
  asm volatile("buffer_load_dwordx2 %0, %6, %7, 0 offen\n\tbuffer_load_dwordx2 %1, %6, %7, 0 offen offset:256\n\t"
               "buffer_load_dwordx2 %2, %6, %7, 0 offen offset:512\n\tbuffer_load_dwordx2 %3, %6, %7, 0 offen offset:768\n\t"
               "buffer_load_dwordx2 %4, %6, %7, 0 offen offset:1024\n\tbuffer_load_dword %5, off, %8, 0 sc1"
               : "=&v"(r.v[0]), "=&v"(r.v[1]), "=&v"(r.v[2]), "=&v"(r.v[3]), "=&v"(r.v[4]), "=&v"(r.e) : "v"(a), "s"(xs), "s"(wss) : "memory");
  asm volatile("buffer_load_dwordx4 %0, %4, %8, 0 offen\n\tbuffer_load_dwordx4 %1, %5, %8, 0 offen\n\t"
               "buffer_load_dwordx4 %2, %6, %8, 0 offen\n\tbuffer_load_dwordx4 %3, %7, %8, 0 offen"
               : "=&v"(r.b[0]), "=&v"(r.b[1]), "=&v"(r.b[2]), "=&v"(r.b[3]) : "v"(b), "v"(b + 10240), "v"(b + 20480), "v"(b + 30720), "s"(w2s) : "memory");
  return r;
}
template <int N> DEV void ln_wait(LnIn& r) {
  asm volatile("s_waitcnt vmcnt(%10)" : "+v"(r.v[0]), "+v"(r.v[1]), "+v"(r.v[2]), "+v"(r.v[3]), "+v"(r.v[4]), "+v"(r.e), "+v"(r.b[0]), "+v"(r.b[1]),
               "+v"(r.b[2]), "+v"(r.b[3]) : "i"(N) : "memory");
}
DEV f2v ln_compute(const LnIn& in, int row, int lane) {
  float xv[20], s = 0.f, q = 0.f;
#pragma unroll
  for (int m = 0; m < 5; ++m) { xv[4 * m] = blo(in.v[m].x); xv[4 * m + 1] = bhi(in.v[m].x); xv[4 * m + 2] = blo(in.v[m].y); xv[4 * m + 3] = bhi(in.v[m].y); }
#pragma unroll
  for (int m = 0; m < 20; ++m) { s = opq(s + xv[m]); q = opq(fmaf(xv[m], xv[m], q)); }
  s = half_sum(s); q = half_sum(q);
  const float mean = s * (1.f / 640.f);
  const float var = fmaxf(fmaf(-mean, mean, q * (1.f / 640.f)), 0.f);
  const float m1 = bfr(mean), s1 = bfr(__builtin_amdgcn_sqrtf(bfr(bfr(var) + 1e-5f)));
  const float r1 = __builtin_amdgcn_rcpf(s1), c = -m1 * r1;
#pragma unroll
  for (int m = 0; m < 5; ++m) {
    const v2u o = {pkh(opq(fmaf(xv[4 * m], r1, c)), opq(fmaf(xv[4 * m + 1], r1, c))), pkh(opq(fmaf(xv[4 * m + 2], r1, c)), opq(fmaf(xv[4 * m + 3], r1, c)))};
    sts64(LNST + row * 1344 + (lane & 31) * 8 + m * 256, o);
  }
  const float m2 = bfr((mean - m1) * r1), s2 = bfr(__builtin_amdgcn_sqrtf(bfr(bfr(var * r1 * r1) + 1e-5f)));
  return (f2v){opq(fmaf(m2, s1, m1)), opq(s1 * s2)};
}
DEV void ln_publish(rsrc_t ws, int tb, int w, int lane) {
  const unsigned a = LNST + ((lane & 15) >> 2) * 1344 + (lane >> 4) * 32 + (lane & 3) * 8 + w * 128;
  const unsigned o = WS_XNT + tb * 10240 + (w * 64 + lane) * 16;
  const v4u v0 = ldstr2(a, a + 4 * 1344), v1 = ldstr2(a + 512, a + 512 + 4 * 1344), v2 = ldstr2(a + (w < 2 ? 1024 : 0), a + (w < 2 ? 1024 : 0) + 4 * 1344);
  __builtin_amdgcn_raw_buffer_store_b128(v0, ws, o, 0, SC1);
  __builtin_amdgcn_raw_buffer_store_b128(v1, ws, o + 4096, 0, SC1);
  if (w < 2) __builtin_amdgcn_raw_buffer_store_b128(v2, ws, o + 8192, 0, SC1);
}

struct Cx { float r, i; };
DEV Cx cmul(Cx a, Cx b) { return {opq(fmaf(a.r, b.r, -a.i * b.i)), opq(fmaf(a.r, b.i, a.i * b.r))}; }
DEV Cx csqr(Cx a) { return {opq(fmaf(a.r, a.r, -a.i * a.i)), opq((a.r + a.r) * a.i)}; }
DEV unsigned ch(Cx a) { return pkh(a.r, a.i); }
DEV unsigned hmul(unsigned a, unsigned b) {
  unsigned t, r;
  asm("v_pk_mul_f16 %0, %1, %2 op_sel:[0,0] op_sel_hi:[0,1]" : "=v"(t) : "v"(a), "v"(b));
  asm("v_pk_fma_f16 %0, %1, %2, %3 op_sel:[1,1,0] op_sel_hi:[1,0,1] neg_lo:[0,1,0]" : "=v"(r) : "v"(a), "v"(b), "v"(t));
  return r;
}
DEV float exp_bf(float x) {
  const float t = x * 1.44268798828125f, lo = x * 7.0526077138765e-06f;
  const float n = __builtin_rintf(t);
  return bfr(__builtin_amdgcn_ldexpf(__builtin_amdgcn_exp2f((t - n) + lo), (int)fminf(fmaxf(n, -200.f), 200.f)));
}
DEV Cx cexp_at(float lg, float rh, float rl, float P) {
  const float m = __builtin_amdgcn_exp2f(lg * P), ph = fmaf(rl, P, __builtin_amdgcn_fractf(rh * P));
  return {opq(m * __builtin_amdgcn_cosf(ph)), opq(m * __builtin_amdgcn_sinf(ph))};
}
struct KgIn { unsigned lA, Ai, Cr, Ci, ldt, dsk; };
DEV KgIn kg_load(const Params& p, int d, int lane) {
  KgIn k;
  k.lA = ((const GAS unsigned*)p.lA)[d * 64 + lane]; k.Ai = ((const GAS unsigned*)p.Ai)[d * 64 + lane];
  k.Cr = ((const GAS unsigned*)p.Cr)[d * 64 + lane]; k.Ci = ((const GAS unsigned*)p.Ci)[d * 64 + lane];
  k.ldt = __builtin_amdgcn_raw_buffer_load_b32(make_rsrc(p.ldt, D_ * 2), 0, (d >> 1) * 4, 0);
  k.dsk = __builtin_amdgcn_raw_buffer_load_b32(make_rsrc(p.Dsk, D_ * 2), 0, (d >> 1) * 4, 0);
  return k;
}
template <class Hook>
DEV float kg_wave(const KgIn& k, int ci, int g, unsigned scr, unsigned fp, int lane, Hook hook) {
  const float dsk = ci ? bhi(k.dsk) : blo(k.dsk), dt = exp_bf(ci ? bhi(k.ldt) : blo(k.ldt)), c2 = dt + dt;
  const float mx = wave_max(fmaxf(fmaxf(fabsf(blo(k.Cr)), fabsf(bhi(k.Cr))), fmaxf(fabsf(blo(k.Ci)), fabsf(bhi(k.Ci))))) * c2;
  int ex = __builtin_amdgcn_frexp_expf(fmaxf(mx, fabsf(dsk) * 0x1p-10f));
  ex = ex < -120 ? -120 : (ex > 120 ? 120 : ex);
  const float cs = c2 * __builtin_amdgcn_ldexpf(1.f, -ex);
  const int sh = g ? 0 : 16;
  const float lg = -(exp_bf(bhi(k.lA << sh)) * dt) * 1.44269504088896f, ai = bhi(k.Ai << sh) * dt;
  const Cx c0 = {opq(bhi(k.Cr << sh) * cs), opq(-bhi(k.Ci << sh) * cs)};
  const float rh = ai * 0.15915494f, rl = fmaf(ai, 0.15915494f, -rh) + ai * 6.4206382e-09f;
  const Cx v1 = cexp_at(lg, -rh, -rl, 1.f), w1 = cexp_at(lg, rh, rl, 8.f), G = cexp_at(lg, -rh, -rl, 256.f);
  const unsigned wa = scr + lane * 4, ra = scr + (lane & 31) * 272 + (lane >> 5) * 16;
  {
    const Cx w2 = csqr(w1), w3 = cmul(w2, w1), w4 = csqr(w2), w8 = csqr(w4), w12 = cmul(w8, w4), w16 = csqr(w8);
    const Cx w20 = cmul(w16, w4), w24 = csqr(w12), w28 = cmul(w24, w4);
    const unsigned o[3] = {ch(w1), ch(w2), ch(w3)};
    const unsigned bs[7] = {ch(w4), ch(w8), ch(w12), ch(w16), ch(w20), ch(w24), ch(w28)};
    hook(0);
    sts32(wa, 0x00003c00u);
#pragma unroll
    for (int j = 0; j < 3; ++j) sts32(wa + (1 + j) * 272, o[j]);
#pragma unroll
    for (int b = 0; b < 7; ++b) {
      sts32(wa + (4 + 4 * b) * 272, bs[b]);
#pragma unroll
      for (int j = 0; j < 3; ++j) sts32(wa + (5 + 4 * b + j) * 272, hmul(bs[b], o[j]));
    }
  }
  asm volatile("" ::: "memory");
  v4u y[8];
#pragma unroll
  for (int s = 0; s < 8; ++s) y[s] = lds128(ra + s * 32);
  asm volatile("" ::: "memory");
  {
    const Cx v2 = csqr(v1), v3 = cmul(v2, v1), v4 = csqr(v2), v6 = csqr(v3), x4 = cmul(c0, v4);
    const unsigned h1 = ch(v1), h2 = ch(v2), h3 = ch(v3), h4 = ch(v4), h6 = ch(v6), hx0 = ch(c0), hx4 = ch(x4);
    sts32(wa + 0 * 272, hx0); sts32(wa + 1 * 272, hmul(hx0, h1)); sts32(wa + 2 * 272, hmul(hx0, h2)); sts32(wa + 3 * 272, hmul(hx0, h3));
    sts32(wa + 4 * 272, hx4); sts32(wa + 5 * 272, hmul(hx4, h1)); sts32(wa + 6 * 272, hmul(hx4, h2)); sts32(wa + 7 * 272, hmul(hx4, h3));
    const Cx d1 = cmul(c0, G), d2 = cmul(d1, G), d3 = cmul(d2, G), d4 = cmul(d3, G);
    const Cx G4 = csqr(csqr(G));
    const Cx d5 = cmul(d1, G4), d6 = cmul(d2, G4), d7 = cmul(d3, G4), d8 = cmul(d4, G4);
    const unsigned e1 = ch(d1), e2 = ch(d2), e3 = ch(d3);
    sts32(wa + 8 * 272, e1); sts32(wa + 9 * 272, hmul(e1, h2)); sts32(wa + 10 * 272, hmul(e1, h4)); sts32(wa + 11 * 272, hmul(e1, h6));
    sts32(wa + 12 * 272, e2); sts32(wa + 13 * 272, hmul(e2, h4)); sts32(wa + 14 * 272, e3); sts32(wa + 15 * 272, hmul(e3, h4));
    sts32(wa + 16 * 272, ch(d4)); sts32(wa + 17 * 272, ch(d5)); sts32(wa + 18 * 272, ch(d6)); sts32(wa + 19 * 272, ch(d7));
    sts32(wa + 20 * 272, ch(d8));
  }
  asm volatile("" ::: "memory");
  v4u xx[8];
#pragma unroll
  for (int s = 0; s < 8; ++s) xx[s] = lds128(ra + s * 32);
  SCHED();
  f16v acc = {};
#pragma unroll
  for (int s = 0; s < 8; ++s) acc = mfma32h(xx[s], y[s], acc);
  const int j = lane & 31, h = lane >> 5;
  hook(1);
  if (g == 0 && lane == 0) acc[0] += __builtin_amdgcn_ldexpf(dsk, -ex);
  sts128(fp + (8 * j + 4 * h) * 4, (v4u){f2u(acc[0]), f2u(acc[1]), f2u(acc[2]), f2u(acc[3])});
  sts64(fp + (h ? 384 + 2 * j : 256 + 4 * j) * 4, (v2u){f2u(acc[4]), f2u(acc[5])});
  sts64(fp + (h ? 448 + 2 * j : 258 + 4 * j) * 4, (v2u){f2u(acc[6]), f2u(acc[7])});
  if (lane <= 32) sts32(fp + (512 + 128 * h + j) * 4, f2u(acc[8]));
  if (h == 0) {
#pragma unroll
    for (int i = 1; i < 4; ++i) sts32(fp + (512 + j + 32 * i) * 4, f2u(acc[8 + i]));
  }
  return __builtin_amdgcn_ldexpf(1.f, ex);
}
DEV void kg_table0(unsigned cb, unsigned fp, int g, int lane) {
  auto f1 = [&](int i) { return opq(u2f(lds32(fp + i * 4)) + u2f(lds32(fp + FPOS + i * 4))); };
  if (g == 0) {
    const int j = lane & 31, h = lane >> 5;
    const unsigned a = fp + (h ? 256 + 4 * j : 8 * j) * 4;
    const v4u l0 = lds128(a), h0 = lds128(a + 16), l1 = lds128(a + FPOS), h1 = lds128(a + FPOS + 16);
    const float e0 = opq(u2f(l0.x) + u2f(l1.x)), e1 = opq(u2f(l0.y) + u2f(l1.y)), e2 = opq(u2f(l0.z) + u2f(l1.z)), e3 = opq(u2f(l0.w) + u2f(l1.w));
    const float e4 = opq(u2f(h0.x) + u2f(h1.x)), e5 = opq(u2f(h0.y) + u2f(h1.y)), e6 = opq(u2f(h0.z) + u2f(h1.z)), e7 = opq(u2f(h0.w) + u2f(h1.w));
    const v4u o0 = {pkh(e0, e1), pkh(e2, e3), pkh(e4, e5), pkh(e6, e7)};
    const v4u o1 = {pkh(e0, e0), pkh(e1, e2), pkh(e2, e2), pkh(e3, e4)};
    sts128(cb + 32 + lane * 16, h ? o1 : o0);
    const float fa = f1(512), fb = f1(513);
    const unsigned aa = pkh(fa, fa), bb = pkh(fb, fb);
    if (lane < 4) sts128(cb + 32 + (128 + lane) * 16, lane == 0 ? (v4u){aa, aa, pkh(fa, fb), bb} : lane == 1 ? (v4u){bb, bb, 0u, 0u} : (v4u){0u, 0u, 0u, 0u});
  } else {
    const float a = f1(384 + 2 * lane), b = f1(385 + 2 * lane), c = f1(386 + 2 * lane);
    sts128(cb + 32 + (64 + lane) * 16, (v4u){pkh(a, a), pkh(a, b), pkh(b, b), pkh(c, c)});
    const float lo = f1(640 - 2 * lane), hi = f1(lane ? 641 - 2 * lane : 640);
    sts32(cb + CH_R3 + lane * 4, pkh(lane ? hi : 0.f, lo));
    if (lane < 16) sts32(cb + CH_R3 + 256 + lane * 4, 0u);
    if (lane < 32) sts32(cb + (lane >> 3) * UCP + (lane & 7) * 4, 0u);
  }
}
DEV void kg_copies(unsigned cb, int g, int lane) {
  auto one = [&](int chunk) {
    const unsigned o = cb + 32 + chunk * 16;
    const v2u pv = lds64(o - 8);
    const v4u e = lds128(o);
    const unsigned a0 = __builtin_amdgcn_alignbit(pv.y, pv.x, 16), a1 = __builtin_amdgcn_alignbit(e.x, pv.y, 16), a2 = __builtin_amdgcn_alignbit(e.y, e.x, 16),
                   a3 = __builtin_amdgcn_alignbit(e.z, e.y, 16), a4 = __builtin_amdgcn_alignbit(e.w, e.z, 16);
    sts128(o + UCP, (v4u){a1, a2, a3, a4});
    sts128(o + 2 * UCP, (v4u){pv.y, e.x, e.y, e.z});
    sts128(o + 3 * UCP, (v4u){a0, a1, a2, a3});
  };
  one(64 * g + lane);
  if (lane < 2) one(128 + 2 * g + lane);
}
DEV void conv_load_u(rsrc_t ws, unsigned grp, int d0, int w, int lane) {
  v4u v[2];
#pragma unroll
  for (int i = 0; i < 2; ++i)
    v[i] = __builtin_amdgcn_raw_buffer_load_b128(ws, (lane >> 1) * 10240 + (lane & 1) * 16, WS_XNT + (64 * w + 32 * i) * 10240 + d0 * 16, SC1);
  if (lane < 16) sts128(grp + (w >> 1) * CH_BYTES + CH_U + (w & 1) * 256 + lane * 16, (v4u){0u, 0u, 0u, 0u});
#pragma unroll
  for (int i = 0; i < 2; ++i) sts128(grp + (lane & 1) * CH_BYTES + CH_U + 512 + (64 * w + 32 * i + (lane >> 1)) * 16, v[i]);
}
template <class Hook, class F1>
DEV float ssm_prepare(rsrc_t ws, const KgIn& k, unsigned grp, unsigned fp, unsigned cnt, int d0, int w, int lane, Hook hook, F1 f1) {
  const int ci = w >> 1, g = w & 1;
  const float isc = kg_wave(k, ci, g, grp + w * KSCR, fp + w * FPOS, lane, hook);
  if (grp == GRP) sts32(W_ISC + ci * 4, f2u(isc));
  group_sync(cnt, 4, lane);
  kg_table0(grp + ci * CH_BYTES, fp + 2 * ci * FPOS, g, lane);
  group_sync(cnt + 4 + ci * 4, 2, lane);
  kg_copies(grp + ci * CH_BYTES, g, lane);
  f1();
  conv_load_u(ws, grp, d0, w, lane);
  group_sync(cnt + 12, 4, lane);
  return isc;
}

template <int J0, int NJ, class Go, class Done>
DEV void conv_tiles(rsrc_t ws, unsigned cb, int d, float isc, int lane, Go go, Done done) {
  constexpr int B0 = J0 > 3 ? J0 - 3 : 0, NB = J0 + NJ - B0;
  const int g = lane >> 4, c16 = lane & 15;
  const unsigned ga = cb + (c16 >> 2) * UCP + 64 - 16 * g + 8 * (lane & 3);
  const unsigned ba = cb + CH_U + 480 + 32 * c16 + 16 * g;
  f4v acc[NJ] = {};
  {
    unsigned w7[4], w9[4];
#pragma unroll
    for (int i = 0; i < 4; ++i) {
      const int k0 = 8 * g + 2 * i - c16;
      w7[i] = ((k0 >= 5 && k0 <= 11) ? 0x3c00u : 0u) | ((k0 >= 4 && k0 <= 10) ? 0x3c000000u : 0u);
      w9[i] = ((k0 >= 4 && k0 <= 12) ? 0x3c00u : 0u) | ((k0 >= 3 && k0 <= 11) ? 0x3c000000u : 0u);
    }
    const v4u W7 = {w7[0], w7[1], w7[2], w7[3]}, W9 = {w9[0], w9[1], w9[2], w9[3]};
    const unsigned r3 = cb + CH_R3 + (127 - c16 + 4 * g) * 4;
    const v4u g4 = ldstr2(ga + 2048, ga + 2048 - 8);
#pragma unroll
    for (int b = 0; b + 4 < J0 + NJ; ++b) {
      const f4v b7 = mfma16h(lds128(ba - 32 + 512 * b), W7, (f4v){}), b9 = mfma16h(lds128(ba - 16 + 512 * b), W9, (f4v){});
      const v4u bx = {pkh(b7[0], b9[0]), pkh(b7[1], b9[1]), pkh(b7[2], b9[2]), pkh(b7[3], b9[3])};
#pragma unroll
      for (int j = (b + 4 > J0 ? b + 4 : J0); j < J0 + NJ; ++j) {
        const unsigned a = r3 - 64 * (j - b);
        acc[j - J0] = mfma16h(bx, (v4u){lds32(a), lds32(a + 4), lds32(a + 8), lds32(a + 12)}, acc[j - J0]);
      }
      if (b + 4 >= J0) acc[b + 4 - J0] = mfma16h(g4, lds128(ba + 512 * b), acc[b + 4 - J0]);
    }
  }
  v4u G[2][4], B[2][NB];
  auto rd = [&](int e, int i) {
    constexpr int top = NJ;
    if (i == 0) G[e & 1][0] = ldstr2(ga + 64 * e, ga + 64 * e - 8);
    else if (i <= top) B[e & 1][NB - top + i - 1] = lds128(ba + 512 * (B0 + NB - top + i - 1) - 64 * e);
    else {
      const int k = i - top - 1;
      if (NB == top || (k & 1) == 0) { const int c = NB == top ? k + 1 : k / 2 + 1; G[e & 1][c] = ldstr2(ga + 512 * c + 64 * e, ga + 512 * c + 64 * e - 8); }
      else { const int bi = NB - top - 1 - k / 2; B[e & 1][bi] = lds128(ba + 512 * (B0 + bi) - 64 * e); }
    }
  };
#pragma unroll
  for (int i = 0; i < 4 + NB; ++i) rd(0, i);
  SCHED();
  go();
#pragma unroll
  for (int e = 0; e < 8; ++e) {
    int m = 0;
#pragma unroll
    for (int c = 0; c < 4; ++c)
#pragma unroll
      for (int j = (c > J0 ? c : J0); j < J0 + NJ; ++j) {
        acc[j - J0] = mfma16h(G[e & 1][c], B[e & 1][j - c - B0], acc[j - J0]);
        if (e < 7 && m < 4 + NB) rd(e + 1, m);
        if (e == 6 && m == 3 + NB) done();
        ++m;
        SCHED();
      }
  }
  unsigned pk[NJ][2];
#pragma unroll
  for (int j = 0; j < NJ; ++j) { pk[j][0] = pkbf(opq(acc[j][0] * isc), opq(acc[j][1] * isc)); pk[j][1] = pkbf(opq(acc[j][2] * isc), opq(acc[j][3] * isc)); }
  const unsigned so = WS_ST + d * 4096 + J0 * 512;
#pragma unroll
  for (int j = 0; j + 1 < NJ; j += 2) {
    const auto x = __builtin_amdgcn_permlane16_swap(pk[j][0], pk[j + 1][0], false, false);
    const auto y = __builtin_amdgcn_permlane16_swap(pk[j][1], pk[j + 1][1], false, false);
    __builtin_amdgcn_raw_buffer_store_b128((v4u){x[0], y[0], x[1], y[1]}, ws, so + (j + (g & 1)) * 512 + 32 * c16 + 16 * (g >> 1), 0, SC1);
  }
  __builtin_amdgcn_raw_buffer_store_b64((v2u){pk[NJ - 1][0], pk[NJ - 1][1]}, ws, so + (NJ - 1) * 512 + 32 * c16 + 8 * g, 0, SC1);
}
template <class Go, class Done>
DEV void conv_channel(rsrc_t ws, unsigned cb, int d, int half, float isc, int lane, Go go, Done done) {
  if (half) conv_tiles<5, 3>(ws, cb, d, isc, lane, go, done);
  else conv_tiles<0, 5>(ws, cb, d, isc, lane, go, done);
}

DEV void retile(v4u (&rows)[4], unsigned stg, int lane) {
#pragma unroll
  for (int j = 0; j < 4; ++j) sts128(stg + j * 1024 + (lane >> 3) * 128 + 16 * ((lane & 7) ^ (((lane >> 4) & 3) << 1)), rows[j]);
  asm volatile("" ::: "memory");
  const int g = lane >> 4, pp = lane & 3, row = 16 * (g >> 1) + 4 * (g & 1) + ((lane & 15) >> 2);
#pragma unroll
  for (int j = 0; j < 4; ++j) {
    const unsigned a = stg + row * 128 + 16 * ((2 * j + (pp >> 1)) ^ (((row >> 1) & 3) << 1)) + 8 * (pp & 1);
    rows[j] = ldstr2(a, a + 1024);
  }
}
DEV void retile_store(rsrc_t ws, int blk, const v4u (&f)[4], int lane) {
#pragma unroll
  for (int j = 0; j < 4; ++j) {
    const int ng = 4 * (blk % 10) + j;
    __builtin_amdgcn_raw_buffer_store_b128(f[j], ws, lane * 16 + WS_W2T + (((ng / 5) * 54 + blk / 10) * 5 + ng % 5) * 1024, 0, SC1);
  }
}

__global__ __launch_bounds__(768, 3) void fused_kernel(Params p) {
  extern __shared__ __attribute__((aligned(16))) unsigned char lds_base[];
  unsigned long long pc;
  unsigned code_bytes;
  asm volatile("s_getpc_b64 %0\n.Lfused_begin:\n\ts_mov_b32 %1, .Lfused_end_a-.Lfused_begin\n\ts_max_i32 %1, %1, .Lfused_end_b-.Lfused_begin"
               : "=s"(pc), "=s"(code_bytes) : : "scc");
  const int lane = threadIdx.x & 63, wave = __builtin_amdgcn_readfirstlane(threadIdx.x >> 6), role = wave >> 2, w = wave & 3;
  const int xcd = blockIdx.x & 7, slot = blockIdx.x >> 3, tb = 32 * xcd + slot;
  const bool gemm_wg = slot < 28;
  const int rp = 4 * (xcd >> 1) + slot / 7, cp = 7 * (xcd & 1) + slot % 7, t0 = 128 * rp, f0 = 128 * cp;
  if (wave >= 10) dma1(make_srd((const void*)pc, code_bytes & ~15u), lane * 16, (2 * slot + wave - 10) * 1024, JUNK);
  STAMP_INIT();
  STAMP(0);
  asm volatile("" ::"v"((LAS unsigned char*)lds_base));

  auto lane_x = [&] { const int l = fresh_lane(); return (unsigned)((l >> 3) * 1280 + 16 * ((l & 7) ^ (l >> 4))); };
  auto lane_w = [&] {
    const int l = fresh_lane(), c = (l & 15) ^ ((l >> 4) << 2);
    return (cp == 13 && c >= 8) ? 0x40000000u : (unsigned)((l >> 4) * 3456 + 16 * c);
  };
  const unsigned sx0 = (t0 + 32 * w) * 1280, sw0 = 16 * w * 3456 + 2 * f0;
  const int rot = ROT7;
  auto slice = [&](int i) { return i < 3 ? i : 3 + (i - 3 + rot) % 7; };
  auto stage_x = [&](int i) {
    const unsigned v0 = lane_x(), v[4] = {v0, (v0 ^ 64) + 9216, v0 + 2 * 9216, (v0 ^ 64) + 3 * 9216};
    dma4(make_srd(p.x, X_BYTES), v, sx0 + 128 * slice(i), ((2 * i) % 7) * UNIT + w * 4096);
  };
  auto stage_w = [&](int i) {
    const unsigned v0 = lane_w(), v[4] = {v0, v0 + 12800, v0 + 2 * 12800, v0 + 3 * 12800};
    dma4(make_srd(p.w1, W_BYTES), v, sw0 + 64 * 3456 * slice(i), ((2 * i + 1) % 7) * UNIT + w * 4096);
  };
  const rsrc_t ws = make_rsrc(p.ws, WS_BYTES), xr = make_rsrc(p.x, X_BYTES);
  const v4u wss = make_srd(p.ws, WS_BYTES);
  if (role == 0 && gemm_wg) { PRIO(3); stage_x(0); stage_w(0); }
  const int tt = 4 * xcd + (slot >> 3), ct = slot & 7;
  const unsigned wq = WS_W2T + ct * 54 * 5120, wr = wq + (14 * w - (w == 3)) * 5120;
  if (threadIdx.x < N_ZERO) sts32(CNT + threadIdx.x * 4, 0u);
  const int dg = 80 * xcd + (gemm_wg ? 2 * slot : 56 + 6 * (slot - 28) + 2 * role);
  KgIn kin = {};
  if (role != 1) {
    if (!gemm_wg || role == 2) kin = kg_load(p, dg + (w >> 1), lane);
    BAR();
    if (gemm_wg && role == 2) {
      stage_x(1); stage_w(1); stage_x(2); stage_w(2);
      if (w == 0) touch(make_srd(p.b1, F_ * 2), lane < 2 ? 2 * f0 + lane * 128 : 0x40000000u);
      if (w == 1) touch(make_srd(p.b2, D_ * 2), lane < 2 ? 160 * ct + lane * 128 : 0x40000000u);
    }
  }
  auto w2_regs = [&](v4u (&R)[35], int lo, int hi) {
#pragma unroll
    for (int j = lo; j < hi; ++j) { R[j] = __builtin_amdgcn_raw_buffer_load_b128(ws, lane * 16 + (j & 3) * 1024, wr + 7 * 5120 + (j >> 2) * 4096, SC1); SCHED(); if (RPACE) { __builtin_amdgcn_s_sleep(RPACE); SCHED(); } }
  };
  auto feed_gemm2 = [&](const v4u (&R)[35]) {
    STAMP(6);
#pragma unroll 1
    for (int i = 0; i < 5; ++i) {
      const int sl = 4 * i + w, qq = sl / 5;
      if (gemm_wg) await<2>(E_DONE + (sl < 6 ? 7 : sl < 12 ? 8 : 9) * 4, 4);
      dma5_lin_sc1(wss, lane * 16, wq + (14 * qq - (qq == 3) + sl % 5) * 5120, sl * 5120);
    }
    WAITV(0);
    signal(E_W0, lane);
    STAMP(7);
    await<2>(E_CONV, 4);
    dma5_lin_sc1(wss, lane * 16, wr + 5 * 5120, SLOT_HI + w * 10240);
    dma5_lin_sc1(wss, lane * 16, wr + 6 * 5120, SLOT_HI + w * 10240 + 5120);
    WAITV(0);
    signal(E_W1, lane);
    STAMP(8);
    PRIO(3);
    const unsigned lo = w * 25600 + lane * 16, hi = SLOT_HI + w * 10240 + lane * 16;
    await<1>(E_QD + w * 4, 2);
#pragma unroll
    for (int i = 0; i < 10; ++i) sts128(lo + i * 1024, R[i]);
    signal(E_QF + w * 4, lane);
    await<1>(E_QD + 16 + w * 4, 2);
#pragma unroll
    for (int i = 10; i < 20; ++i) sts128(lo + i * 1024, R[i]);
    signal(E_QF + 16 + w * 4, lane);
    await<1>(E_QD + 32 + w * 4, 2);
#pragma unroll
    for (int i = 20; i < 25; ++i) sts128(lo + i * 1024, R[i]);
#pragma unroll
    for (int i = 25; i < 30; ++i) sts128(hi + (i - 25) * 1024, R[i]);
    await<1>(E_QD + 48 + w * 4, 2);
#pragma unroll
    for (int i = 30; i < 35; ++i) sts128(hi + (i - 25) * 1024, R[i]);
    signal(E_QF + 32 + w * 4, lane);
    STAMP(9);
    if (w == 0) { poll_words(ws, WS_F3 + (ct * 32 + (lane & 31)) * 128, lds_tag()); signal(E_F3OK, lane); } else { await<1>(E_F3OK, 1); }
    v4u sv[3];
#pragma unroll
    for (int i = 0; i < 3; ++i)
      sv[i] = __builtin_amdgcn_raw_buffer_load_b128(ws, (lane >> 3) * 4096 + (lane & 7) * 16, WS_ST + (80 * ct + 8 * min(w + 4 * i, 9)) * 4096 + tt * 128, SC1);
#pragma unroll
    for (int i = 0; i < 3; ++i) sts128(STILE + (8 * min(w + 4 * i, 9) + (lane >> 3)) * 160 + (lane & 7) * 16, sv[i]);
    signal(E_SRDY, lane);
    STAMP(10);
    STAMP_FLUSH();
  };

  if (role == 1) {
    const int token = 8 * tb + 2 * w + (lane >> 5);
    const int fi = 4 * xcd + slot - 28, blk = w < 2 ? 2 * tb + w : (!gemm_wg && w == 2 && fi < 28) ? 512 + fi : -1;
    PRIO(3);
    LnIn ln = ln_load(make_srd(p.x, X_BYTES), wss, make_srd(p.w2, blk < 0 ? 0u : W_BYTES), token, max(blk, 0), lane);
    BAR();
    ln_wait<0>(ln);
    const unsigned tag = epoch_tag(ln.e, w == 0, lane);
    retile(ln.b, GRP + w * KSCR, lane);
    signal(E_RT, lane);
    STAMP(1);
    const f2v stat = ln_compute(ln, 2 * w + (lane >> 5), lane);
    group_sync<0>(E_LN, 4, lane);
    if ((lane & 31) == 0) __builtin_amdgcn_raw_buffer_store_b128((v4u){f2u(stat.x), tag, f2u(stat.y), tag}, ws, WS_STAT + token * 16, 0, SC1);
    ln_publish(ws, tb, w, lane);
    if (blk >= 0) retile_store(ws, blk, ln.b, lane);
    STAMP(11);
    PRIO(0);
    publish(ws, C_F1, 4, WS_F1 + tb * 128, tag, lane);
    STAMP(12);
    if (gemm_wg) {
      await<SLL>(E_DONE + FALL_AT * 4, 4);
      STAMP(2);
      if (w == 0) {
        poll_words(ws, WS_FALL + xcd * 128, tag);
        signal(E_F1OK, lane);
        if (blockIdx.x == 0 && lane == 0) __builtin_amdgcn_raw_buffer_store_b32(lds32(W_TAG + 4) + 1u, ws, WS_EPOCH, 0, SC1);
      } else {
        await<1>(E_F1OK, 1);
      }
      STAMP(5);
      v4u R[35];
      w2_regs(R, 0, 35);
      feed_gemm2(R);
      asm volatile(".Lfused_end_a:");
      return;
    }
  }
  if (!gemm_wg || role == 2) {
    const bool last_pair = role == (gemm_wg ? 2 : 1);
    const unsigned grp = last_pair ? GRP : (role >> 1) * (4 * KSCR + 4 * FPOS), cnt = C_GRP + role * 48;
    if (role == 1) kin = kg_load(p, dg + (w >> 1), lane);
    if (gemm_wg) {
      if (w < 2) WAITV(9); else WAITV(8);
      signal(E_FULL + 4, lane);
      WAITV(0); signal(E_FULL + 8, lane);
    }
    float isc = ssm_prepare(ws, kin, grp, last_pair ? LNST : grp + 4 * KSCR, cnt, dg, w, lane,
      [&](int i) { if (last_pair) await(i ? C_F1 : E_RT, 4); },
      [&] {
        STAMP(2);
        if (role == 0) {
          await(E_LN, 4);
          const unsigned tag = lds_tag();
          poll_words(ws, WS_F1 + (64 * w + lane) * 128, tag);
          if (lds_arrive(C_AX, lane) == 3) {
            if (slot == 28 && lane == 0) __builtin_amdgcn_raw_buffer_store_b32(tag, ws, WS_FALL + xcd * 128, 0, SC1);
            signal(E_F1OK, lane);
          }
          await<1>(E_F1OK, 1);
        } else {
          await<SLA>(E_F1OK, 1);
          if (gemm_wg) await<SLA>(U_AT, 4);
        }
        STAMP(3);
      });
    STAMP(role == 2 ? 6 : 16);
    if (role != 1) {
      unsigned cb = grp + (w >> 1) * CH_BYTES;
      int d = dg + (w >> 1);
#pragma unroll 1
      for (int last = gemm_wg; last < 2; ++last) {
        conv_channel(ws, cb, d, w & 1, isc, lane, [&] { if (gemm_wg) await(E_GELU, 4); if (role) STAMP(7); }, [&] { signal(last ? E_CONV : E_R1, lane); });
        if (role == 0) break;
        if (!last) {
          await(C_GRP + 48 + 12, 4);
          cb = GRP + (w >> 1) * CH_BYTES;
          d -= 2;
          isc = u2f(peek(W_ISC + (w >> 1) * 4));
        }
      }
      if (role == 0) { WAITV(0); signal(E_SDR, lane); STAMP(5); } else { STAMP(8); }
    } else {
      STAMP(5);
      v4u R[35];
      w2_regs(R, 0, 35);
      await<2>(E_R1, 8);
      feed_gemm2(R);
      return;
    }
  } else if (cp == 13 && (w & 1)) {
    WAITV(0); signal(E_FULL, lane);
#pragma unroll 1
    for (int i = 0; i < 10; ++i) signal(E_DONE + i * 4, lane);
    stage_x(3);
#pragma unroll 1
    for (int i = 3; i < 10; ++i) {
      await<1>(E_DONE + (i - 3) * 4, 4);
      stage_w(i);
      if (i < 9) { stage_x(i + 1); WAITV(4); } else { WAITV(0); }
      signal(E_FULL + i * 4, lane);
    }
    signal(E_VMQ, lane);
    await(E_LN, 4);
    const unsigned tag = lds_tag();
    signal(E_GELU, lane);
    publish(ws, C_H, 4, WS_F2 + (rp * 16 + cp) * 128, tag, lane);
  } else {
    const int wf = w & 1, wt = w >> 1, h = lane >> 5;
    PRIO(2);
    unsigned aa[2][2], ba[2][4];
    {
      const int q = (lane & 15) >> 2, pp = lane & 3, fb = (lane >> 4) & 1;
#pragma unroll
      for (int mi = 0; mi < 2; ++mi) {
        aa[0][mi] = (8 * h + q) * 256 + 16 * ((8 * wf + 4 * (mi ^ wt) + 2 * fb + (pp >> 1)) ^ (q << 2)) + 8 * (pp & 1);
        aa[1][mi] = aa[0][mi] + 4 * UNIT;
        asm volatile("" : "+v"(aa[0][mi]), "+v"(aa[1][mi]));
      }
#pragma unroll
      for (int s = 0; s < 4; ++s) {
        ba[0][s] = (64 * wt + (lane & 31)) * 128 + 16 * ((2 * s + h) ^ ((lane >> 1) & 7));
        ba[1][s] = ba[0][s] + 4 * UNIT;
        asm volatile("" : "+v"(ba[0][s]), "+v"(ba[1][s]));
      }
    }
    f16v acc[2][2] = {};
    v4u A[3][2], B[3][2], st[2];
    unsigned b1v[2], fl = 0, dn = 0, sbase = sig_base(lane), one = 1u;
    asm volatile("" : "+v"(sbase), "+v"(one));
    f4v cs4 = {};
    const unsigned sel1 = (((lane >> 2) ^ (lane >> 4)) & 1) ? 0u : 0x3f803f80u;
    const v4u sel = {sel1, sel1, sel1, sel1};
    auto rdA = [&](int n, int mi) {
      const int u = (2 * (n >> 2) + 1) % 7;
      const unsigned a = aa[u >> 2][mi] + (u & 3) * UNIT + (n & 3) * 4096;
      A[n % 3][mi] = ldstr2(a, a + 1024);
    };
    auto rdB = [&](int n, int ni) {
      const int u = (2 * (n >> 2)) % 7;
      B[n % 3][ni] = lds128(ba[u >> 2][n & 3] + (u & 3) * UNIT + ni * 4096);
    };
    const v4u xs = make_srd(p.x, X_BYTES), w1s = make_srd(p.w1, W_BYTES);
    const unsigned vx0 = lane_x(), vxo = vx0 ^ 64, vw0 = lane_w();
    unsigned sx[4], sw[4], mb = w * 4096;
#pragma unroll
    for (int j = 0; j < 4; ++j) {
      sx[j] = sx0 + (3 + rot) * 128 + j * 9216;
      sw[j] = sw0 + (3 + rot) * 64 * 3456 + j * 12800;
      asm volatile("" : "+s"(sx[j]), "+s"(sw[j]));
    }
    auto piece = [&](auto qc) {
      constexpr int q = qc, i = (q + 8) / 8 + 2 + ((q >> 2) & 1), j = q & 3;
      if constexpr (q >= -4 && q < 52) {
        if constexpr (q & 4) {
          if constexpr (j == 0) dma1(xs, vx0, sx[0], mb + ((2 * i) % 7) * UNIT);
          else dma_next<j * 1024>(xs, (j & 1) ? vxo : vx0, sx[j]);
          sx[j] += i + rot == 9 ? -6 * 128 : 128;
          asm volatile("" : "+s"(sx[j]));
        } else {
          if constexpr (j == 0) dma1(w1s, vw0, sw[0], mb + ((2 * i + 1) % 7) * UNIT);
          else dma_next<j * 1024>(w1s, vw0, sw[j]);
          sw[j] += i + rot == 9 ? -6 * 64 * 3456 : 64 * 3456;
          asm volatile("" : "+s"(sw[j]));
        }
      }
    };
    auto load_stats = [&] {
#pragma unroll
      for (int i = 0; i < 2; ++i) st[i] = __builtin_amdgcn_raw_buffer_load_b128(ws, (lane & 31) * 16, WS_STAT + (t0 + 64 * wt + 32 * i) * 16, SC1);
    };
    WAITV(0);
    group_sync(E_FULL, 4, lane);
    STAMP(2); STAMPC(11);
    rdA(0, 0); rdB(0, 0); rdB(0, 1); rdA(0, 1); rdA(1, 0); rdB(1, 0); rdB(1, 1); rdA(1, 1);
    SCHED();
    unsigned tag = 0;
    v4u eb[2];
    static_for<40>([&](auto nc) {
      constexpr int n = nc, sg = n >> 2, r = n & 3;
      acc[0][0] = mfma32b(A[n % 3][0], B[n % 3][0], acc[0][0]);
      SCHED();
      if (n < 38) { rdA(n + 2, 0); rdB(n + 2, 0); }
      SCHED();
      acc[0][1] = mfma32b(A[n % 3][0], B[n % 3][1], acc[0][1]);
      SCHED();
      piece(IC<2 * (n - 3)>{});
      if (r == 2 && sg < 7) dn = *(volatile LAS unsigned*)lp(sbase + (E_DONE - CNT) + sg * 4);
      if (r == 1 && sg < 9) fl = *(volatile LAS unsigned*)lp(sbase + (E_FULL - CNT) + (sg + 1) * 4);
      if (n == 34) { dn = *(volatile LAS unsigned*)lp(sbase + (E_LN - CNT)); tag = *(volatile LAS unsigned*)lp(sbase + (W_TAG - CNT)); }
      SCHED();
      cs4 = mfma16b(sel, A[n % 3][0], cs4);
      SCHED();
      acc[1][0] = mfma32b(A[n % 3][1], B[n % 3][0], acc[1][0]);
      SCHED();
      if (n < 38) { rdB(n + 2, 1); rdA(n + 2, 1); }
      if (r == 1) signal_at<E_DONE - CNT + sg * 4>(sbase, one);
      if (n == 39) { sts32(SEXC + w * 128 + (lane & 31) * 4, f2u(cs4[0])); signal(E_SX + w * 4, lane); }
      SCHED();
      acc[1][1] = mfma32b(A[n % 3][1], B[n % 3][1], acc[1][1]);
      SCHED();
      piece(IC<2 * (n - 3) + 1>{});
      if (r == 1 && sg < 9) { check(fl, E_FULL + (sg + 1) * 4, 4); if (sg & 1) STAMPL(16 + (sg >> 1)); }
      if (r == 2 && sg < 7) check(dn, E_DONE + sg * 4, 4);
      if (r == 0 && sg >= 2 && sg < 9) {
        if (sg < 8) WAITV(8); else WAITV(4);
        signal_at<E_FULL - CNT + (sg + 1) * 4>(sbase, one);
      }
      if (n == 29) load_stats();
      if (n == 30) {
#pragma unroll
        for (int i = 0; i < 2; ++i)
          b1v[i] = __builtin_amdgcn_raw_buffer_load_b16(make_rsrc(p.b1, F_ * 2), (lane & 31) * 2, (f0 + 64 * wf + 32 * (i ^ wt)) * 2, 0);
        signal_at<E_VMQ - CNT>(sbase, one);
      }
      if (n == 35) {
        if (__builtin_expect(__builtin_amdgcn_readfirstlane(dn) < 4, 0)) { await(E_LN, 4); tag = peek(W_TAG); }
        tag = __builtin_amdgcn_readfirstlane(tag);
      }
      if (n == 37) {
        if (__builtin_expect(!__all(st[0].y == tag && st[0].w == tag && st[1].y == tag && st[1].w == tag), 0)) {
#pragma unroll 1
          for (unsigned s = 0; s < SPIN_CAP; ++s) {
            asm volatile("" ::: "memory");
            load_stats();
            if (__all(st[0].y == tag && st[0].w == tag && st[1].y == tag && st[1].w == tag)) break;
          }
        }
      }
      if (n == 37 || n == 38) {
        constexpr int i = n == 38;
        const float mu = -u2f(st[i].x), ir = u2f(st[i].z), mh = bfr(mu), ih = bfr(ir);
        eb[i] = h ? (v4u){0u, 0u, 0u, 0u} : (v4u){pkbf(mh, opq(mu - mh)), pkbf(mh, 0.f), pkbf(ih, opq(ir - ih)), 0u};
        asm volatile("" : "+v"(eb[i]));
      }
      SCHED();
    });
    STAMP(3); STAMPC(12);
    auto ext = [&](int mi, float S) {
      const float sh = bfr(S);
      const v4u ea = h ? (v4u){0u, 0u, 0u, 0u} : (v4u){pkbf(sh, sh), pkbf(S - sh, 0.f), b1v[mi] | (b1v[mi] << 16), 0u};
      acc[mi][0] = mfma32b(ea, eb[0], acc[mi][0]);
      acc[mi][1] = mfma32b(ea, eb[1], acc[mi][1]);
    };
    fl = peek(E_SX + (w ^ 2) * 4);
    float so = u2f(peek(SEXC + (w ^ 2) * 128 + (lane & 31) * 4));
    ext(0, cs4[0]);
    SCHED();
    if (__builtin_expect(__builtin_amdgcn_readfirstlane(fl) < 1, 0)) {
      await(E_SX + (w ^ 2) * 4, 1);
      so = u2f(peek(SEXC + (w ^ 2) * 128 + (lane & 31) * 4));
    }
    ext(1, so);
    STAMP(1);
    {
      const unsigned hv = ((lane >> 4) & 1) * 55296 + h * 256 + (lane & 15) * 16;
#pragma unroll
      for (int mi = 0; mi < 2; ++mi) {
#pragma unroll
        for (int ni = 0; ni < 2; ++ni) {
          const float ir = u2f(st[ni].z), rho = __builtin_amdgcn_rcpf(ir);
          const float k1 = -2.302208198f * rho, k2 = k1 * 0.044715f * rho * rho;
          const unsigned hs = WS_H + ((t0 + 64 * wt + 32 * ni) / 16) * 55296 + ((f0 + 64 * wf) / 32 + (mi ^ wt)) * 1024;
#pragma unroll
          for (int hf = 0; hf < 2; ++hf) {
            unsigned o[4];
#pragma unroll
            for (int r = 0; r < 4; ++r) {
              const f2v a = {acc[mi][ni][8 * hf + 2 * r], acc[mi][ni][8 * hf + 2 * r + 1]};
              const f2v x = a * (a * a * k2 + k1);
              const f2v e = {__builtin_amdgcn_exp2f(x.x), __builtin_amdgcn_exp2f(x.y)};
              const f2v dd = e * ir + ir;
              const f2v rr = {__builtin_amdgcn_rcpf(dd.x), __builtin_amdgcn_rcpf(dd.y)};
              const f2v gl = a * rr;
              o[r] = pkbf(gl.x, gl.y);
            }
            __builtin_amdgcn_raw_buffer_store_b128((v4u){o[0], o[1], o[2], o[3]}, ws, hv + hs + hf * 512, 0, SC1);
            SCHED();
          }
        }
      }
    }
    signal(E_GELU, lane);
    STAMP(4); STAMPC(13);
    publish(ws, C_H, 4, WS_F2 + (rp * 16 + cp) * 128, tag, lane);
    STAMP(5);
  }

  {
    const int mh = w & 1, q = role + (w >> 1);
    if (wave == 0) {
      PRIO(0);
      poll_words(ws, WS_F2 + ((tt >> 1) * 16 + min(lane, 13)) * 128, lds_tag());
      signal(E_F2OK, lane);
    } else {
      await<1>(E_F2OK, 1);
    }
    if (role == 0) STAMP(6);
    PRIO(3);
    v4u H[7][2], A[2][5];
    f4v acc[2][5] = {};
    const unsigned hs = WS_H + (4 * tt + 2 * mh) * 55296 + (14 * q - (q == 3)) * 1024;
    const unsigned hv = lane * 16;
    auto ldh = [&](int k, int ti) { H[k % 7][ti] = __builtin_amdgcn_raw_buffer_load_b128(ws, hv, hs + ti * 55296 + k * 1024, SC1); SCHED(); };
#pragma unroll
    for (int k = 0; k < 3; ++k) { ldh(k, 0); ldh(k, 1); }
    if (role == 2) { if (!gemm_wg) await(E_SDR, 4); publish(ws, C_GRP + 96 + 36, 4, WS_F3 + tb * 128, lds_tag(), lane); STAMP(9); }
    const int fo = q < 2 ? 3 * q : 2 + 2 * q;
    v2u xv[3], bv[3];
    unsigned alo = q * 25600 + lane * 16, ahi = SLOT_HI + q * 10240 + lane * 16, fl = 0, sq = sig_base(lane) + q * 4, one = 1u;
    asm volatile("" : "+v"(alo), "+v"(ahi), "+v"(sq), "+v"(one));
    auto rd = [&](int k, int n) { A[k & 1][n] = lds128((k % 7 < 5 ? alo + (k % 7) * 5120 : ahi + (k % 7 - 5) * 5120) + n * 1024); };
    await<1>(E_W0, 4);
#pragma unroll
    for (int n = 0; n < 5; ++n) rd(0, n);
    if (role == 0) { STAMP(7); STAMPC(14); }
    SCHED();
    auto step = [&](auto kc) {
      constexpr int k = kc;
      if (k == 3 || k == 7 || k == 10) PRIO(k == 3 ? 2 : k == 7 ? 1 : 0);
#pragma unroll
      for (int ti = 0; ti < 2; ++ti) {
#pragma unroll
        for (int n = 0; n < 5; ++n) {
          acc[ti][n] = mfma16b(A[k & 1][n], H[k % 7][ti], acc[ti][n]);
          SCHED();
          if (ti == 0 && k < 13) {
            rd(k + 1, n);
            if (n == 4 && k == 0) signal_at<E_QD - CNT>(sq, one);
            if (n == 4 && k == 2) signal_at<E_QD - CNT + 16>(sq, one);
            if (n == 4 && k == 4) signal_at<E_QD - CNT + 32>(sq, one);
            if (n == 4 && k == 5) signal_at<E_QD - CNT + 48>(sq, one);
            if (n == 4 && k == 9) signal_at<E_QD - CNT + 64>(sq, one);
            SCHED();
          }
          if (ti == 1 && n == 0) {
            if (k == 3) fl = peek(E_W1);
            if (k == 5) fl = *(volatile LAS unsigned*)lp(sq + (E_QF - CNT));
            if (k == 7) fl = *(volatile LAS unsigned*)lp(sq + (E_QF - CNT) + 16);
            if (k == 9) fl = *(volatile LAS unsigned*)lp(sq + (E_QF - CNT) + 32);
            if (k == 12) fl = *(volatile LAS unsigned*)lp(sq + (E_QD - CNT) + 64);
            SCHED();
          }
        }
        if (k < 2) { ldh(3 + 2 * k + ti, 0); ldh(3 + 2 * k + ti, 1); }
        if (k >= 1 && k < 8) ldh(k + 6, ti);
        if constexpr (k >= 8 && k < 11) {
          const int f = min(fo + k - 8, 9), tl = f >= 5, n = f - 5 * tl;
          const int l1 = fresh_lane();
          if (ti == 0) xv[k - 8] = __builtin_amdgcn_raw_buffer_load_b64(xr, (l1 & 15) * 1280 + 8 * (l1 >> 4), (64 * tt + 32 * mh + 16 * tl) * 1280 + 160 * ct + 32 * n, 0);
          else bv[k - 8] = __builtin_amdgcn_raw_buffer_load_b64(make_rsrc(p.b2, D_ * 2), 8 * (l1 >> 4), 160 * ct + 32 * n, 0);
        }
        SCHED();
      }
      if (k == 3) check(fl, E_W1, 4);
      if (k == 5) check(fl, E_QF + q * 4, 1);
      if (k == 7) check(fl, E_QF + 16 + q * 4, 1);
      if (k == 9) check(fl, E_QF + 32 + q * 4, 1);
      if (role == 0 && (k & 1)) STAMPL(22 + k / 2);
      SCHED();
    };
    static_for<13>(step);
    if (role == 0) step(IC<13>{});
    if (role == 0) { STAMP(8); STAMPC(15); } else { STAMP(4); }
    check(fl, E_QD + 64 + q * 4, 2);
#pragma unroll
    for (int f = 0; f < 10; ++f) sts128(alo + mh * 10240 + f * 1024, __builtin_bit_cast(v4u, acc[f / 5][f % 5]));
    fl = peek(E_SRDY);
    const int l2 = fresh_lane(), g2 = l2 >> 4;
    group_sync<0>(E_RED + mh * 4, 4, l2);
    check(fl, E_SRDY, 4);
    if (role == 0) STAMP(9);
    auto finish = [&](int i) {
      const int f = fo + i, tl = f >= 5, n = f - 5 * tl;
      const unsigned ra = mh * 10240 + f * 1024 + l2 * 16;
      const f4v p0 = __builtin_bit_cast(f4v, lds128(ra)), p1 = __builtin_bit_cast(f4v, lds128(ra + 25600));
      const f4v p2 = __builtin_bit_cast(f4v, lds128(ra + 51200)), p3 = __builtin_bit_cast(f4v, lds128(ra + 76800));
      const v2u sv = ldstr(STILE + (16 * n + (l2 >> 2)) * 160 + (32 * mh + 16 * tl + 4 * (l2 & 3)) * 2);
      const float bz[4] = {blo(bv[i].x), bhi(bv[i].x), blo(bv[i].y), bhi(bv[i].y)};
      const float sz[4] = {blo(sv.x), bhi(sv.x), blo(sv.y), bhi(sv.y)};
      const float xz[4] = {blo(xv[i].x), bhi(xv[i].x), blo(xv[i].y), bhi(xv[i].y)};
      float r[4];
#pragma unroll
      for (int j = 0; j < 4; ++j) {
        const float z = (p0[j] + p1[j]) + (p2[j] + p3[j]) + bz[j];
        const float gt = z * __builtin_amdgcn_rcpf(1.f + __builtin_amdgcn_exp2f(z * -1.44269504088896f));
        r[j] = fmaf(sz[j], gt, xz[j]);
      }
      __builtin_amdgcn_raw_buffer_store_b64((v2u){pkbf(r[0], r[1]), pkbf(r[2], r[3])}, make_rsrc(p.out, X_BYTES), (l2 & 15) * 1280 + 8 * g2,
                                            (64 * tt + 32 * mh + 16 * tl) * 1280 + 160 * ct + 32 * n, OUT_AUX);
    };
    finish(0); finish(1);
    if (role == 0) finish(2);
    STAMP(10);
    asm volatile(".Lfused_end_b:");
  }
  STAMP_FLUSH();
}

}

extern "C" void kernel_launch(void* const* d_in, const int* in_sizes, int n_in, void* d_out, int out_size, void* d_ws, size_t ws_size,
                              hipStream_t stream) {
  Params p;
  p.x = (const unsigned short*)d_in[0];
  p.lA = (const unsigned short*)d_in[1];
  p.Ai = (const unsigned short*)d_in[2];
  p.Cr = (const unsigned short*)d_in[3];
  p.Ci = (const unsigned short*)d_in[4];
  p.ldt = (const unsigned short*)d_in[5];
  p.Dsk = (const unsigned short*)d_in[6];
  p.w1 = (const unsigned short*)d_in[8];
  p.b1 = (const unsigned short*)d_in[9];
  p.w2 = (const unsigned short*)d_in[10];
  p.b2 = (const unsigned short*)d_in[11];
  p.out = (unsigned short*)d_out;
  p.ws = (unsigned char*)d_ws;
  (void)hipFuncSetAttribute((const void*)fused_kernel, hipFuncAttributeMaxDynamicSharedMemorySize, LDS_BYTES);
  fused_kernel<<<256, 768, LDS_BYTES, stream>>>(p);
}
```

```cpp
#include <hip/hip_runtime.h>
#include <hip/hip_bf16.h>

namespace {

typedef unsigned v4u __attribute__((ext_vector_type(4)));
typedef unsigned v2u __attribute__((ext_vector_type(2)));
typedef short s4v __attribute__((ext_vector_type(4)));
typedef float f2v __attribute__((ext_vector_type(2)));
typedef float f4v __attribute__((ext_vector_type(4)));
typedef float f16v __attribute__((ext_vector_type(16)));
typedef _Float16 h2v __attribute__((ext_vector_type(2)));
typedef _Float16 h8v __attribute__((ext_vector_type(8)));
typedef __bf16 b2v __attribute__((ext_vector_type(2)));
typedef __bf16 b8v __attribute__((ext_vector_type(8)));
#define GAS __attribute__((address_space(1)))
#define LAS __attribute__((address_space(3)))
#define DEV __device__ __forceinline__
typedef __amdgpu_buffer_rsrc_t rsrc_t;

constexpr int L_ = 2048, D_ = 640, F_ = 1728;
constexpr unsigned X_BYTES = L_ * D_ * 2, W_BYTES = D_ * F_ * 2;
constexpr int SC1 = 16;
constexpr unsigned SPIN_CAP = 1u << 16;

constexpr unsigned WS_EPOCH = 0;
constexpr unsigned WS_FALL = 4096;
constexpr unsigned WS_STAT = 32768;
constexpr unsigned WS_F2 = 65536;
constexpr unsigned WS_F1 = 5013504;
constexpr unsigned WS_F3 = WS_F1 + 32768;
constexpr unsigned WS_XNT = 131072;
constexpr unsigned WS_W2T = 2752512;
constexpr unsigned WS_H = 5242880;
constexpr unsigned WS_ST = 12582912;
constexpr unsigned WS_STAMP = 15204352;
constexpr unsigned WS_BYTES = WS_STAMP + 256 * 12 * 64 * 8;

constexpr unsigned LDS_BYTES = 163840;
constexpr unsigned UNIT = 16384;
constexpr unsigned KSCR = 8704;
constexpr unsigned FPOS = 2576;
constexpr unsigned GRP = 7 * UNIT;
constexpr unsigned LNST = GRP + 4 * KSCR;
constexpr unsigned CNT = LNST + 10752;
constexpr unsigned SEXC = CNT + 512;
constexpr unsigned JUNK = CNT + 1024;
constexpr unsigned UCP = 2368;
constexpr unsigned CH_R3 = 4 * UCP, CH_U = 9856, CH_BYTES = 2 * KSCR;
constexpr unsigned SLOT_HI = 102400, STILE = 143360;
constexpr unsigned E_FULL = CNT, E_DONE = CNT + 40;
constexpr unsigned E_W0 = CNT + 80, E_W1 = CNT + 84, E_F2OK = CNT + 88, E_F3OK = CNT + 92, E_RED = CNT + 96;
constexpr unsigned E_QD = CNT + 384, E_QF = CNT + 104;
constexpr unsigned E_GELU = CNT + 184, E_LN = CNT + 188, E_LNPUB = CNT + 192, E_SRDY = CNT + 196, E_VMQ = CNT + 200, E_FWOK = CNT + 204, C_AX = CNT + 156, E_R1 = CNT + 160, E_SDR = CNT + 164, W_ISC = CNT + 472;
constexpr unsigned C_H = CNT + 208, C_F1 = CNT + 212, E_F1OK = CNT + 216, E_CONV = CNT + 220, E_SX = CNT + 368, E_RT = CNT + 360;
constexpr unsigned C_GRP = CNT + 224;
constexpr unsigned W_TAG = CNT + 464;
constexpr int N_ZERO = 116;

struct Params {
  const unsigned short *x, *lA, *Ai, *Cr, *Ci, *ldt, *Dsk, *w1, *b1, *w2, *b2;
  unsigned short* out;
  unsigned char* ws;
};

#define STAMP(i) do { } while (0)
#define STAMPC(i) do { } while (0)
#define STAMPV(i, v) do { } while (0)
#define STAMP_INIT() do { } while (0)
#define STAMP_FLUSH() do { } while (0)

#define PRIO(x) __builtin_amdgcn_s_setprio(x)
#define STAMPL(i) do { } while (0)
#define F2_SLEEP 2
#define OUT_AUX 0
#define SLA 1
#define SLL 2
#define RPACE 1
#define FALL_AT 4
#define U_AT E_VMQ
#define ROT7 ((slot % 7 + (slot / 7 * 7 + 2) / 4) % 7)
#define BAR() asm volatile("s_waitcnt lgkmcnt(0)\n\ts_barrier" ::: "memory")
#define WAITV(n) asm volatile("s_waitcnt vmcnt(" #n ")" ::: "memory")
#define SCHED() __builtin_amdgcn_sched_barrier(0)

template <int I> struct IC { constexpr operator int() const { return I; } };
template <int N, int I = 0, class F> DEV void static_for(F&& f) {
  if constexpr (I < N) { f(IC<I>{}); static_for<N, I + 1>(f); }
}
DEV int fresh_lane() {
  int l;
  asm volatile("v_mbcnt_lo_u32_b32 %0, -1, 0\n\tv_mbcnt_hi_u32_b32 %0, -1, %0" : "=v"(l));
  return l;
}
DEV float u2f(unsigned u) { return __builtin_bit_cast(float, u); }
DEV unsigned f2u(float f) { return __builtin_bit_cast(unsigned, f); }
DEV h2v u2h(unsigned u) { return __builtin_bit_cast(h2v, u); }
DEV float blo(unsigned u) { return u2f(u << 16); }
DEV float bhi(unsigned u) { return u2f(u & 0xffff0000u); }
DEV unsigned pkbf(float a, float b) { return __builtin_bit_cast(unsigned, __builtin_convertvector((f2v){a, b}, b2v)); }
DEV unsigned pkh(float a, float b) { return __builtin_bit_cast(unsigned, __builtin_convertvector((f2v){a, b}, h2v)); }
DEV float opq(float x) { asm("" : "+v"(x)); return x; }
DEV float bfr(float a) { return blo(pkbf(a, a)); }
DEV f16v mfma32b(v4u a, v4u b, f16v c) { return __builtin_amdgcn_mfma_f32_32x32x16_bf16(__builtin_bit_cast(b8v, a), __builtin_bit_cast(b8v, b), c, 0, 0, 0); }
DEV f16v mfma32h(v4u a, v4u b, f16v c) { return __builtin_amdgcn_mfma_f32_32x32x16_f16(__builtin_bit_cast(h8v, a), __builtin_bit_cast(h8v, b), c, 0, 0, 0); }
DEV f4v mfma16b(v4u a, v4u b, f4v c) { return __builtin_amdgcn_mfma_f32_16x16x32_bf16(__builtin_bit_cast(b8v, a), __builtin_bit_cast(b8v, b), c, 0, 0, 0); }
DEV f4v mfma16h(v4u a, v4u b, f4v c) { return __builtin_amdgcn_mfma_f32_16x16x32_f16(__builtin_bit_cast(h8v, a), __builtin_bit_cast(h8v, b), c, 0, 0, 0); }

DEV LAS unsigned char* lp(unsigned a) { return (LAS unsigned char*)(size_t)a; }
DEV v4u lds128(unsigned a) { return *(LAS v4u*)lp(a); }
DEV v2u lds64(unsigned a) { return *(LAS v2u*)lp(a); }
DEV unsigned lds32(unsigned a) { return *(LAS unsigned*)lp(a); }
DEV void sts128(unsigned a, v4u v) { *(LAS v4u*)lp(a) = v; }
DEV void sts64(unsigned a, v2u v) { *(LAS v2u*)lp(a) = v; }
DEV void sts32(unsigned a, unsigned v) { *(LAS unsigned*)lp(a) = v; }
DEV v2u ldstr(unsigned a) { return __builtin_bit_cast(v2u, __builtin_amdgcn_ds_read_tr16_b64_v4i16((LAS s4v*)lp(a))); }
DEV v4u ldstr2(unsigned a0, unsigned a1) { const v2u x = ldstr(a0), y = ldstr(a1); return (v4u){x.x, x.y, y.x, y.y}; }

DEV v4u make_srd(const void* ptr, unsigned bytes) {
  return (v4u){(unsigned)(size_t)ptr, (unsigned)((size_t)ptr >> 32) & 0xffffu, bytes, 0x00020000u};
}
DEV rsrc_t make_rsrc(const void* ptr, unsigned bytes) { return __builtin_amdgcn_make_buffer_rsrc((void*)ptr, 0, bytes, 0x00020000); }
#define DMA_HEAD "s_mov_b32 m0, %0\n\ts_nop 0\n\t"
DEV void dma1(v4u srd, unsigned v, unsigned soff, unsigned lds) {
  asm volatile(DMA_HEAD "buffer_load_dwordx4 %1, %2, %3 offen lds" ::"s"(lds), "v"(v), "s"(srd), "s"(soff) : "memory");
}
template <int OFF> DEV void dma_next(v4u srd, unsigned v, unsigned soff) {
  asm volatile("buffer_load_dwordx4 %0, %1, %2 offen offset:%3 lds" ::"v"(v), "s"(srd), "s"(soff), "i"(OFF) : "memory");
}
DEV void touch(v4u srd, unsigned v) {
  asm volatile(DMA_HEAD "buffer_load_dword %1, %2, 0 offen lds" ::"s"(JUNK + 768), "v"(v), "s"(srd) : "memory");
}
DEV void dma4(v4u srd, const unsigned (&v)[4], unsigned soff, unsigned lds) {
  asm volatile(DMA_HEAD "buffer_load_dwordx4 %1, %5, %6 offen lds\n\tbuffer_load_dwordx4 %2, %5, %6 offen offset:1024 lds\n\t"
               "buffer_load_dwordx4 %3, %5, %6 offen offset:2048 lds\n\tbuffer_load_dwordx4 %4, %5, %6 offen offset:3072 lds"
               ::"s"(lds), "v"(v[0]), "v"(v[1]), "v"(v[2]), "v"(v[3]), "s"(srd), "s"(soff) : "memory");
}
DEV void dma5_lin_sc1(v4u srd, unsigned v, unsigned soff, unsigned lds) {
  asm volatile(DMA_HEAD "buffer_load_dwordx4 %1, %2, %3 offen sc1 lds\n\tbuffer_load_dwordx4 %1, %2, %3 offen offset:1024 sc1 lds\n\t"
               "buffer_load_dwordx4 %1, %2, %3 offen offset:2048 sc1 lds\n\tbuffer_load_dwordx4 %1, %2, %3 offen offset:3072 sc1 lds"
               ::"s"(lds), "v"(v), "s"(srd), "s"(soff) : "memory");
  asm volatile(DMA_HEAD "buffer_load_dwordx4 %1, %2, %3 offen sc1 lds" ::"s"(lds + 4096), "v"(v), "s"(srd), "s"(soff + 4096) : "memory");
}

DEV void poll_words(rsrc_t ws, unsigned off, unsigned tag) {
#pragma unroll 1
  for (unsigned s = 0; s < SPIN_CAP; ++s) {
    asm volatile("" ::: "memory");
    if (__all(__builtin_amdgcn_raw_buffer_load_b32(ws, off, 0, SC1) == tag)) break;
    __builtin_amdgcn_s_sleep(F2_SLEEP);
  }
  asm volatile("" ::: "memory");
}
DEV unsigned sig_base(int lane) { return lane == 0 ? CNT : JUNK + lane * 4; }
template <int OFF> DEV void signal_at(unsigned base, unsigned one) {
  asm volatile("ds_add_u32 %0, %1 offset:%2" ::"v"(base), "v"(one), "i"(OFF) : "memory");
}
DEV void signal(unsigned a, int lane) {
  asm volatile("ds_add_u32 %0, %1" ::"v"(sig_base(lane) + (a - CNT)), "v"(1u) : "memory");
}
DEV unsigned peek(unsigned a) { return *(volatile LAS unsigned*)lp(a); }
template <int SLEEP = 1> DEV void await(unsigned a, unsigned need) {
#pragma unroll 1
  for (unsigned s = 0; s < (SPIN_CAP << 2); ++s) {
    asm volatile("" : "+s"(s));
    if (__builtin_amdgcn_readfirstlane(peek(a)) >= need) break;
    if (SLEEP) __builtin_amdgcn_s_sleep(SLEEP);
  }
  asm volatile("" ::: "memory");
}
DEV void check(unsigned early, unsigned a, unsigned need) {
  if (__builtin_expect(__builtin_amdgcn_readfirstlane(early) < need, 0)) await<1>(a, need);
  asm volatile("" ::: "memory");
}
DEV unsigned lds_arrive(unsigned cnt, int lane) {
  unsigned old;
  asm volatile("ds_add_rtn_u32 %0, %1, %2\n\ts_waitcnt lgkmcnt(0)" : "=&v"(old) : "v"(sig_base(lane) + (cnt - CNT)), "v"(1u) : "memory");
  return __builtin_amdgcn_readfirstlane(old);
}
template <int SLEEP = 1> DEV void group_sync(unsigned cnt, unsigned target, int lane) {
  if (lds_arrive(cnt, lane) + 1 < target) await<SLEEP>(cnt, target);
}
DEV void publish(rsrc_t ws, unsigned cnt, unsigned nwaves, unsigned flag_off, unsigned tag, int lane) {
  WAITV(0);
  if (lds_arrive(cnt, lane) + 1 == nwaves && lane == 0) __builtin_amdgcn_raw_buffer_store_b32(tag, ws, flag_off, 0, SC1);
}
DEV unsigned epoch_tag(unsigned ev, bool share, int lane) {
  const unsigned e = __builtin_amdgcn_readfirstlane(ev);
  const unsigned tag = e * 0x9E3779B1u + 0x7F4A7C15u;
  if (share && lane == 0) sts64(W_TAG, (v2u){tag, e});
  return tag;
}
DEV unsigned lds_tag() { return __builtin_amdgcn_readfirstlane(lds32(W_TAG)); }

template <int CTRL> DEV float dpp_add(float v) {
  return v + u2f(__builtin_amdgcn_update_dpp(0u, f2u(v), CTRL, 0xf, 0xf, false));
}
template <int CTRL> DEV float dpp_max(float v) {
  return fmaxf(v, u2f(__builtin_amdgcn_update_dpp(0u, f2u(v), CTRL, 0xf, 0xf, false)));
}
DEV float half_sum(float v) {
  v = dpp_add<0x128>(v); v = dpp_add<0x124>(v); v = dpp_add<0x122>(v); v = dpp_add<0x121>(v);
  const auto r = __builtin_amdgcn_permlane16_swap(f2u(v), f2u(v), false, false);
  return u2f(r[0]) + u2f(r[1]);
}
DEV float wave_max(float v) {
  v = dpp_max<0x128>(v); v = dpp_max<0x124>(v); v = dpp_max<0x122>(v); v = dpp_max<0x121>(v);
  const auto r = __builtin_amdgcn_permlane16_swap(f2u(v), f2u(v), false, false);
  v = fmaxf(u2f(r[0]), u2f(r[1]));
  const auto q = __builtin_amdgcn_permlane32_swap(f2u(v), f2u(v), false, false);
  return fmaxf(u2f(q[0]), u2f(q[1]));
}

struct LnIn { v2u v[5]; };
DEV LnIn ln_load(rsrc_t xr, int token, int lane) {
  LnIn r;
#pragma unroll
  for (int m = 0; m < 5; ++m) r.v[m] = __builtin_amdgcn_raw_buffer_load_b64(xr, token * 1280 + (lane & 31) * 8 + m * 256, 0, 0);
  return r;
}
DEV f2v ln_compute(const LnIn& in, int row, int lane) {
  float xv[20], s = 0.f, q = 0.f;
#pragma unroll
  for (int m = 0; m < 5; ++m) { xv[4 * m] = blo(in.v[m].x); xv[4 * m + 1] = bhi(in.v[m].x); xv[4 * m + 2] = blo(in.v[m].y); xv[4 * m + 3] = bhi(in.v[m].y); }
#pragma unroll
  for (int m = 0; m < 20; ++m) { s = opq(s + xv[m]); q = opq(fmaf(xv[m], xv[m], q)); }
  s = half_sum(s); q = half_sum(q);
  const float mean = s * (1.f / 640.f);
  const float var = fmaxf(fmaf(-mean, mean, q * (1.f / 640.f)), 0.f);
  const float m1 = bfr(mean), s1 = bfr(__builtin_amdgcn_sqrtf(bfr(bfr(var) + 1e-5f)));
  const float r1 = __builtin_amdgcn_rcpf(s1), c = -m1 * r1;
#pragma unroll
  for (int m = 0; m < 5; ++m) {
    const v2u o = {pkh(opq(fmaf(xv[4 * m], r1, c)), opq(fmaf(xv[4 * m + 1], r1, c))), pkh(opq(fmaf(xv[4 * m + 2], r1, c)), opq(fmaf(xv[4 * m + 3], r1, c)))};
    sts64(LNST + row * 1344 + (lane & 31) * 8 + m * 256, o);
  }
  const float m2 = bfr((mean - m1) * r1), s2 = bfr(__builtin_amdgcn_sqrtf(bfr(bfr(var * r1 * r1) + 1e-5f)));
  return (f2v){opq(fmaf(m2, s1, m1)), opq(s1 * s2)};
}
DEV void ln_transpose(v4u (&v)[3], int w, int lane) {
  const unsigned a = LNST + ((lane & 15) >> 2) * 1344 + (lane >> 4) * 32 + (lane & 3) * 8 + w * 128;
  v[0] = ldstr2(a, a + 4 * 1344);
  v[1] = ldstr2(a + 512, a + 512 + 4 * 1344);
  v[2] = ldstr2(a + (w < 2 ? 1024 : 0), a + (w < 2 ? 1024 : 0) + 4 * 1344);
}
DEV void ln_publish(rsrc_t ws, const v4u (&v)[3], int tb, int w, int lane) {
  const unsigned o = WS_XNT + tb * 10240 + (w * 64 + lane) * 16;
  __builtin_amdgcn_raw_buffer_store_b128(v[0], ws, o, 0, SC1);
  __builtin_amdgcn_raw_buffer_store_b128(v[1], ws, o + 4096, 0, SC1);
  if (w < 2) __builtin_amdgcn_raw_buffer_store_b128(v[2], ws, o + 8192, 0, SC1);
}

struct Cx { float r, i; };
DEV Cx cmul(Cx a, Cx b) { return {opq(fmaf(a.r, b.r, -a.i * b.i)), opq(fmaf(a.r, b.i, a.i * b.r))}; }
DEV Cx csqr(Cx a) { return {opq(fmaf(a.r, a.r, -a.i * a.i)), opq((a.r + a.r) * a.i)}; }
DEV unsigned ch(Cx a) { return pkh(a.r, a.i); }
DEV unsigned hmul(unsigned a, unsigned b) {
  unsigned t, r;
  asm("v_pk_mul_f16 %0, %1, %2 op_sel:[0,0] op_sel_hi:[0,1]" : "=v"(t) : "v"(a), "v"(b));
  asm("v_pk_fma_f16 %0, %1, %2, %3 op_sel:[1,1,0] op_sel_hi:[1,0,1] neg_lo:[0,1,0]" : "=v"(r) : "v"(a), "v"(b), "v"(t));
  return r;
}
DEV float exp_bf(float x) {
  const float t = x * 1.44268798828125f, lo = x * 7.0526077138765e-06f;
  const float n = __builtin_rintf(t);
  return bfr(__builtin_amdgcn_ldexpf(__builtin_amdgcn_exp2f((t - n) + lo), (int)fminf(fmaxf(n, -200.f), 200.f)));
}
DEV Cx cexp_at(float lg, float rh, float rl, float P) {
  const float m = __builtin_amdgcn_exp2f(lg * P), ph = fmaf(rl, P, __builtin_amdgcn_fractf(rh * P));
  return {opq(m * __builtin_amdgcn_cosf(ph)), opq(m * __builtin_amdgcn_sinf(ph))};
}
struct KgIn { unsigned lA, Ai, Cr, Ci, ldt, dsk; };
DEV KgIn kg_load(const Params& p, int d, int lane) {
  KgIn k;
  k.lA = ((const GAS unsigned*)p.lA)[d * 64 + lane]; k.Ai = ((const GAS unsigned*)p.Ai)[d * 64 + lane];
  k.Cr = ((const GAS unsigned*)p.Cr)[d * 64 + lane]; k.Ci = ((const GAS unsigned*)p.Ci)[d * 64 + lane];
  k.ldt = __builtin_amdgcn_raw_buffer_load_b32(make_rsrc(p.ldt, D_ * 2), 0, (d >> 1) * 4, 0);
  k.dsk = __builtin_amdgcn_raw_buffer_load_b32(make_rsrc(p.Dsk, D_ * 2), 0, (d >> 1) * 4, 0);
  return k;
}
template <class Hook>
DEV float kg_wave(const KgIn& k, int ci, int g, unsigned scr, unsigned fp, int lane, Hook hook) {
  const float dsk = ci ? bhi(k.dsk) : blo(k.dsk), dt = exp_bf(ci ? bhi(k.ldt) : blo(k.ldt)), c2 = dt + dt;
  const float mx = wave_max(fmaxf(fmaxf(fabsf(blo(k.Cr)), fabsf(bhi(k.Cr))), fmaxf(fabsf(blo(k.Ci)), fabsf(bhi(k.Ci))))) * c2;
  int ex = __builtin_amdgcn_frexp_expf(fmaxf(mx, fabsf(dsk) * 0x1p-10f));
  ex = ex < -120 ? -120 : (ex > 120 ? 120 : ex);
  const float cs = c2 * __builtin_amdgcn_ldexpf(1.f, -ex);
  const int sh = g ? 0 : 16;
  const float lg = -(exp_bf(bhi(k.lA << sh)) * dt) * 1.44269504088896f, ai = bhi(k.Ai << sh) * dt;
  const Cx c0 = {opq(bhi(k.Cr << sh) * cs), opq(-bhi(k.Ci << sh) * cs)};
  const float rh = ai * 0.15915494f, rl = fmaf(ai, 0.15915494f, -rh) + ai * 6.4206382e-09f;
  const Cx v1 = cexp_at(lg, -rh, -rl, 1.f), w1 = cexp_at(lg, rh, rl, 8.f), G = cexp_at(lg, -rh, -rl, 256.f);
  const unsigned wa = scr + lane * 4, ra = scr + (lane & 31) * 272 + (lane >> 5) * 16;
  {
    const Cx w2 = csqr(w1), w3 = cmul(w2, w1), w4 = csqr(w2), w8 = csqr(w4), w12 = cmul(w8, w4), w16 = csqr(w8);
    const Cx w20 = cmul(w16, w4), w24 = csqr(w12), w28 = cmul(w24, w4);
    const unsigned o[3] = {ch(w1), ch(w2), ch(w3)};
    const unsigned bs[7] = {ch(w4), ch(w8), ch(w12), ch(w16), ch(w20), ch(w24), ch(w28)};
    sts32(wa, 0x00003c00u);
#pragma unroll
    for (int j = 0; j < 3; ++j) sts32(wa + (1 + j) * 272, o[j]);
#pragma unroll
    for (int b = 0; b < 7; ++b) {
      sts32(wa + (4 + 4 * b) * 272, bs[b]);
#pragma unroll
      for (int j = 0; j < 3; ++j) sts32(wa + (5 + 4 * b + j) * 272, hmul(bs[b], o[j]));
    }
  }
  asm volatile("" ::: "memory");
  v4u y[8];
#pragma unroll
  for (int s = 0; s < 8; ++s) y[s] = lds128(ra + s * 32);
  asm volatile("" ::: "memory");
  {
    const Cx v2 = csqr(v1), v3 = cmul(v2, v1), v4 = csqr(v2), v6 = csqr(v3), x4 = cmul(c0, v4);
    const unsigned h1 = ch(v1), h2 = ch(v2), h3 = ch(v3), h4 = ch(v4), h6 = ch(v6), hx0 = ch(c0), hx4 = ch(x4);
    sts32(wa + 0 * 272, hx0); sts32(wa + 1 * 272, hmul(hx0, h1)); sts32(wa + 2 * 272, hmul(hx0, h2)); sts32(wa + 3 * 272, hmul(hx0, h3));
    sts32(wa + 4 * 272, hx4); sts32(wa + 5 * 272, hmul(hx4, h1)); sts32(wa + 6 * 272, hmul(hx4, h2)); sts32(wa + 7 * 272, hmul(hx4, h3));
    const Cx d1 = cmul(c0, G), d2 = cmul(d1, G), d3 = cmul(d2, G), d4 = cmul(d3, G);
    const Cx G4 = csqr(csqr(G));
    const Cx d5 = cmul(d1, G4), d6 = cmul(d2, G4), d7 = cmul(d3, G4), d8 = cmul(d4, G4);
    const unsigned e1 = ch(d1), e2 = ch(d2), e3 = ch(d3);
    sts32(wa + 8 * 272, e1); sts32(wa + 9 * 272, hmul(e1, h2)); sts32(wa + 10 * 272, hmul(e1, h4)); sts32(wa + 11 * 272, hmul(e1, h6));
    sts32(wa + 12 * 272, e2); sts32(wa + 13 * 272, hmul(e2, h4)); sts32(wa + 14 * 272, e3); sts32(wa + 15 * 272, hmul(e3, h4));
    sts32(wa + 16 * 272, ch(d4)); sts32(wa + 17 * 272, ch(d5)); sts32(wa + 18 * 272, ch(d6)); sts32(wa + 19 * 272, ch(d7));
    sts32(wa + 20 * 272, ch(d8));
  }
  asm volatile("" ::: "memory");
  v4u xx[8];
#pragma unroll
  for (int s = 0; s < 8; ++s) xx[s] = lds128(ra + s * 32);
  SCHED();
  f16v acc = {};
#pragma unroll
  for (int s = 0; s < 8; ++s) acc = mfma32h(xx[s], y[s], acc);
  const int j = lane & 31, h = lane >> 5;
  hook();
  if (g == 0 && lane == 0) acc[0] += __builtin_amdgcn_ldexpf(dsk, -ex);
  sts128(fp + (8 * j + 4 * h) * 4, (v4u){f2u(acc[0]), f2u(acc[1]), f2u(acc[2]), f2u(acc[3])});
  sts64(fp + (h ? 384 + 2 * j : 256 + 4 * j) * 4, (v2u){f2u(acc[4]), f2u(acc[5])});
  sts64(fp + (h ? 448 + 2 * j : 258 + 4 * j) * 4, (v2u){f2u(acc[6]), f2u(acc[7])});
  if (lane <= 32) sts32(fp + (512 + 128 * h + j) * 4, f2u(acc[8]));
  if (h == 0) {
#pragma unroll
    for (int i = 1; i < 4; ++i) sts32(fp + (512 + j + 32 * i) * 4, f2u(acc[8 + i]));
  }
  return __builtin_amdgcn_ldexpf(1.f, ex);
}
DEV void kg_table0(unsigned cb, unsigned fp, int g, int lane) {
  auto f1 = [&](int i) { return opq(u2f(lds32(fp + i * 4)) + u2f(lds32(fp + FPOS + i * 4))); };
  if (g == 0) {
    const int j = lane & 31, h = lane >> 5;
    const unsigned a = fp + (h ? 256 + 4 * j : 8 * j) * 4;
    const v4u l0 = lds128(a), h0 = lds128(a + 16), l1 = lds128(a + FPOS), h1 = lds128(a + FPOS + 16);
    const float e0 = opq(u2f(l0.x) + u2f(l1.x)), e1 = opq(u2f(l0.y) + u2f(l1.y)), e2 = opq(u2f(l0.z) + u2f(l1.z)), e3 = opq(u2f(l0.w) + u2f(l1.w));
    const float e4 = opq(u2f(h0.x) + u2f(h1.x)), e5 = opq(u2f(h0.y) + u2f(h1.y)), e6 = opq(u2f(h0.z) + u2f(h1.z)), e7 = opq(u2f(h0.w) + u2f(h1.w));
    const v4u o0 = {pkh(e0, e1), pkh(e2, e3), pkh(e4, e5), pkh(e6, e7)};
    const v4u o1 = {pkh(e0, e0), pkh(e1, e2), pkh(e2, e2), pkh(e3, e4)};
    sts128(cb + 32 + lane * 16, h ? o1 : o0);
    const float fa = f1(512), fb = f1(513);
    const unsigned aa = pkh(fa, fa), bb = pkh(fb, fb);
    if (lane < 4) sts128(cb + 32 + (128 + lane) * 16, lane == 0 ? (v4u){aa, aa, pkh(fa, fb), bb} : lane == 1 ? (v4u){bb, bb, 0u, 0u} : (v4u){0u, 0u, 0u, 0u});
  } else {
    const float a = f1(384 + 2 * lane), b = f1(385 + 2 * lane), c = f1(386 + 2 * lane);
    sts128(cb + 32 + (64 + lane) * 16, (v4u){pkh(a, a), pkh(a, b), pkh(b, b), pkh(c, c)});
    const float lo = f1(640 - 2 * lane), hi = f1(lane ? 641 - 2 * lane : 640);
    sts32(cb + CH_R3 + lane * 4, pkh(lane ? hi : 0.f, lo));
    if (lane < 16) sts32(cb + CH_R3 + 256 + lane * 4, 0u);
    if (lane < 32) sts32(cb + (lane >> 3) * UCP + (lane & 7) * 4, 0u);
  }
}
DEV void kg_copies(unsigned cb, int g, int lane) {
  auto one = [&](int chunk) {
    const unsigned o = cb + 32 + chunk * 16;
    const v2u pv = lds64(o - 8);
    const v4u e = lds128(o);
    const unsigned a0 = __builtin_amdgcn_alignbit(pv.y, pv.x, 16), a1 = __builtin_amdgcn_alignbit(e.x, pv.y, 16), a2 = __builtin_amdgcn_alignbit(e.y, e.x, 16),
                   a3 = __builtin_amdgcn_alignbit(e.z, e.y, 16), a4 = __builtin_amdgcn_alignbit(e.w, e.z, 16);
    sts128(o + UCP, (v4u){a1, a2, a3, a4});
    sts128(o + 2 * UCP, (v4u){pv.y, e.x, e.y, e.z});
    sts128(o + 3 * UCP, (v4u){a0, a1, a2, a3});
  };
  one(64 * g + lane);
  if (lane < 2) one(128 + 2 * g + lane);
}
DEV void conv_load_u(rsrc_t ws, unsigned grp, int d0, int w, int lane) {
  v4u v[2];
#pragma unroll
  for (int i = 0; i < 2; ++i)
    v[i] = __builtin_amdgcn_raw_buffer_load_b128(ws, (lane >> 1) * 10240 + (lane & 1) * 16, WS_XNT + (64 * w + 32 * i) * 10240 + d0 * 16, SC1);
  if (lane < 16) sts128(grp + (w >> 1) * CH_BYTES + CH_U + (w & 1) * 256 + lane * 16, (v4u){0u, 0u, 0u, 0u});
#pragma unroll
  for (int i = 0; i < 2; ++i) sts128(grp + (lane & 1) * CH_BYTES + CH_U + 512 + (64 * w + 32 * i + (lane >> 1)) * 16, v[i]);
}
template <class Hook, class F1>
DEV float ssm_prepare(rsrc_t ws, const KgIn& k, unsigned grp, unsigned fp, unsigned cnt, int d0, int w, int lane, Hook hook, F1 f1) {
  const int ci = w >> 1, g = w & 1;
  const float isc = kg_wave(k, ci, g, grp + w * KSCR, fp + w * FPOS, lane, hook);
  if (grp == GRP) sts32(W_ISC + ci * 4, f2u(isc));
  group_sync(cnt, 4, lane);
  kg_table0(grp + ci * CH_BYTES, fp + 2 * ci * FPOS, g, lane);
  group_sync(cnt + 4 + ci * 4, 2, lane);
  kg_copies(grp + ci * CH_BYTES, g, lane);
  f1();
  conv_load_u(ws, grp, d0, w, lane);
  group_sync(cnt + 12, 4, lane);
  return isc;
}

template <int J0, int NJ, class Go, class Done>
DEV void conv_tiles(rsrc_t ws, unsigned cb, int d, float isc, int lane, Go go, Done done) {
  constexpr int B0 = J0 > 3 ? J0 - 3 : 0, NB = J0 + NJ - B0;
  const int g = lane >> 4, c16 = lane & 15;
  const unsigned ga = cb + (c16 >> 2) * UCP + 64 - 16 * g + 8 * (lane & 3);
  const unsigned ba = cb + CH_U + 480 + 32 * c16 + 16 * g;
  f4v acc[NJ] = {};
  {
    unsigned w7[4], w9[4];
#pragma unroll
    for (int i = 0; i < 4; ++i) {
      const int k0 = 8 * g + 2 * i - c16;
      w7[i] = ((k0 >= 5 && k0 <= 11) ? 0x3c00u : 0u) | ((k0 >= 4 && k0 <= 10) ? 0x3c000000u : 0u);
      w9[i] = ((k0 >= 4 && k0 <= 12) ? 0x3c00u : 0u) | ((k0 >= 3 && k0 <= 11) ? 0x3c000000u : 0u);
    }
    const v4u W7 = {w7[0], w7[1], w7[2], w7[3]}, W9 = {w9[0], w9[1], w9[2], w9[3]};
    const unsigned r3 = cb + CH_R3 + (127 - c16 + 4 * g) * 4;
    const v4u g4 = ldstr2(ga + 2048, ga + 2048 - 8);
#pragma unroll
    for (int b = 0; b + 4 < J0 + NJ; ++b) {
      const f4v b7 = mfma16h(lds128(ba - 32 + 512 * b), W7, (f4v){}), b9 = mfma16h(lds128(ba - 16 + 512 * b), W9, (f4v){});
      const v4u bx = {pkh(b7[0], b9[0]), pkh(b7[1], b9[1]), pkh(b7[2], b9[2]), pkh(b7[3], b9[3])};
#pragma unroll
      for (int j = (b + 4 > J0 ? b + 4 : J0); j < J0 + NJ; ++j) {
        const unsigned a = r3 - 64 * (j - b);
        acc[j - J0] = mfma16h(bx, (v4u){lds32(a), lds32(a + 4), lds32(a + 8), lds32(a + 12)}, acc[j - J0]);
      }
      if (b + 4 >= J0) acc[b + 4 - J0] = mfma16h(g4, lds128(ba + 512 * b), acc[b + 4 - J0]);
    }
  }
  v4u G[2][4], B[2][NB];
  auto rd = [&](int e, int i) {
    constexpr int top = NJ;
    if (i == 0) G[e & 1][0] = ldstr2(ga + 64 * e, ga + 64 * e - 8);
    else if (i <= top) B[e & 1][NB - top + i - 1] = lds128(ba + 512 * (B0 + NB - top + i - 1) - 64 * e);
    else {
      const int k = i - top - 1;
      if (NB == top || (k & 1) == 0) { const int c = NB == top ? k + 1 : k / 2 + 1; G[e & 1][c] = ldstr2(ga + 512 * c + 64 * e, ga + 512 * c + 64 * e - 8); }
      else { const int bi = NB - top - 1 - k / 2; B[e & 1][bi] = lds128(ba + 512 * (B0 + bi) - 64 * e); }
    }
  };
#pragma unroll
  for (int i = 0; i < 4 + NB; ++i) rd(0, i);
  SCHED();
  go();
#pragma unroll
  for (int e = 0; e < 8; ++e) {
    int m = 0;
#pragma unroll
    for (int c = 0; c < 4; ++c)
#pragma unroll
      for (int j = (c > J0 ? c : J0); j < J0 + NJ; ++j) {
        acc[j - J0] = mfma16h(G[e & 1][c], B[e & 1][j - c - B0], acc[j - J0]);
        if (e < 7 && m < 4 + NB) rd(e + 1, m);
        if (e == 6 && m == 3 + NB) done();
        ++m;
        SCHED();
      }
  }
  unsigned pk[NJ][2];
#pragma unroll
  for (int j = 0; j < NJ; ++j) { pk[j][0] = pkbf(opq(acc[j][0] * isc), opq(acc[j][1] * isc)); pk[j][1] = pkbf(opq(acc[j][2] * isc), opq(acc[j][3] * isc)); }
  const unsigned so = WS_ST + d * 4096 + J0 * 512;
#pragma unroll
  for (int j = 0; j + 1 < NJ; j += 2) {
    const auto x = __builtin_amdgcn_permlane16_swap(pk[j][0], pk[j + 1][0], false, false);
    const auto y = __builtin_amdgcn_permlane16_swap(pk[j][1], pk[j + 1][1], false, false);
    __builtin_amdgcn_raw_buffer_store_b128((v4u){x[0], y[0], x[1], y[1]}, ws, so + (j + (g & 1)) * 512 + 32 * c16 + 16 * (g >> 1), 0, SC1);
  }
  __builtin_amdgcn_raw_buffer_store_b64((v2u){pk[NJ - 1][0], pk[NJ - 1][1]}, ws, so + (NJ - 1) * 512 + 32 * c16 + 8 * g, 0, SC1);
}
template <class Go, class Done>
DEV void conv_channel(rsrc_t ws, unsigned cb, int d, int half, float isc, int lane, Go go, Done done) {
  if (half) conv_tiles<5, 3>(ws, cb, d, isc, lane, go, done);
  else conv_tiles<0, 5>(ws, cb, d, isc, lane, go, done);
}

DEV void retile(v4u (&rows)[4], unsigned stg, int lane) {
#pragma unroll
  for (int j = 0; j < 4; ++j) sts128(stg + j * 1024 + (lane >> 3) * 128 + 16 * ((lane & 7) ^ (((lane >> 4) & 3) << 1)), rows[j]);
  asm volatile("" ::: "memory");
  const int g = lane >> 4, pp = lane & 3, row = 16 * (g >> 1) + 4 * (g & 1) + ((lane & 15) >> 2);
#pragma unroll
  for (int j = 0; j < 4; ++j) {
    const unsigned a = stg + row * 128 + 16 * ((2 * j + (pp >> 1)) ^ (((row >> 1) & 3) << 1)) + 8 * (pp & 1);
    rows[j] = ldstr2(a, a + 1024);
  }
}
DEV void retile_store(rsrc_t ws, int blk, const v4u (&f)[4], int lane) {
#pragma unroll
  for (int j = 0; j < 4; ++j) {
    const int ng = 4 * (blk % 10) + j;
    __builtin_amdgcn_raw_buffer_store_b128(f[j], ws, lane * 16 + WS_W2T + (((ng / 5) * 54 + blk / 10) * 5 + ng % 5) * 1024, 0, SC1);
  }
}

__global__ __launch_bounds__(768, 3) void fused_kernel(Params p) {
  extern __shared__ __attribute__((aligned(16))) unsigned char lds_base[];
  unsigned long long pc;
  unsigned code_bytes;
  asm volatile("s_getpc_b64 %0\n.Lfused_begin:\n\ts_mov_b32 %1, .Lfused_end_a-.Lfused_begin\n\ts_max_i32 %1, %1, .Lfused_end_b-.Lfused_begin"
               : "=s"(pc), "=s"(code_bytes) : : "scc");
  const int lane = threadIdx.x & 63, wave = __builtin_amdgcn_readfirstlane(threadIdx.x >> 6), role = wave >> 2, w = wave & 3;
  const int xcd = blockIdx.x & 7, slot = blockIdx.x >> 3, tb = 32 * xcd + slot;
  const bool gemm_wg = slot < 28;
  const int rp = 4 * (xcd >> 1) + slot / 7, cp = 7 * (xcd & 1) + slot % 7, t0 = 128 * rp, f0 = 128 * cp;
  if (wave >= 10) dma1(make_srd((const void*)pc, code_bytes & ~15u), lane * 16, (2 * slot + wave - 10) * 1024, JUNK);
  STAMP_INIT();
  STAMP(0);
  asm volatile("" ::"v"((LAS unsigned char*)lds_base));

  auto lane_x = [&] { const int l = fresh_lane(); return (unsigned)((l >> 3) * 1280 + 16 * ((l & 7) ^ (l >> 4))); };
  auto lane_w = [&] {
    const int l = fresh_lane(), c = (l & 15) ^ ((l >> 4) << 2);
    return (cp == 13 && c >= 8) ? 0x40000000u : (unsigned)((l >> 4) * 3456 + 16 * c);
  };
  const unsigned sx0 = (t0 + 32 * w) * 1280, sw0 = 16 * w * 3456 + 2 * f0;
  const int rot = ROT7;
  auto slice = [&](int i) { return i < 3 ? i : 3 + (i - 3 + rot) % 7; };
  auto stage_x = [&](int i) {
    const unsigned v0 = lane_x(), v[4] = {v0, (v0 ^ 64) + 9216, v0 + 2 * 9216, (v0 ^ 64) + 3 * 9216};
    dma4(make_srd(p.x, X_BYTES), v, sx0 + 128 * slice(i), ((2 * i) % 7) * UNIT + w * 4096);
  };
  auto stage_w = [&](int i) {
    const unsigned v0 = lane_w(), v[4] = {v0, v0 + 12800, v0 + 2 * 12800, v0 + 3 * 12800};
    dma4(make_srd(p.w1, W_BYTES), v, sw0 + 64 * 3456 * slice(i), ((2 * i + 1) % 7) * UNIT + w * 4096);
  };
  const rsrc_t ws = make_rsrc(p.ws, WS_BYTES), xr = make_rsrc(p.x, X_BYTES);
  const v4u wss = make_srd(p.ws, WS_BYTES);
  if (role == 0 && gemm_wg) { PRIO(3); stage_x(0); stage_w(0); }
  const int tt = 4 * xcd + (slot >> 3), ct = slot & 7;
  const unsigned wq = WS_W2T + ct * 54 * 5120, wr = wq + (14 * w - (w == 3)) * 5120;
  if (threadIdx.x < N_ZERO) sts32(CNT + threadIdx.x * 4, 0u);
  const int dg = 80 * xcd + (gemm_wg ? 2 * slot : 56 + 6 * (slot - 28) + 2 * role);
  KgIn kin = {};
  if (role != 1) {
    if (!gemm_wg || role == 2) kin = kg_load(p, dg + (w >> 1), lane);
    BAR();
    if (role == 2) {
      if (gemm_wg) { stage_x(1); stage_w(1); stage_x(2); stage_w(2); }
      if (w == 0) touch(make_srd(p.b1, F_ * 2), lane < 2 ? 2 * f0 + lane * 128 : 0x40000000u);
      if (w == 1) touch(make_srd(p.b2, D_ * 2), lane < 2 ? 160 * ct + lane * 128 : 0x40000000u);
    }
  }
  auto w2_regs = [&](v4u (&R)[35], int lo, int hi) {
#pragma unroll
    for (int j = lo; j < hi; ++j) { R[j] = __builtin_amdgcn_raw_buffer_load_b128(ws, lane * 16 + (j & 3) * 1024, wr + 7 * 5120 + (j >> 2) * 4096, SC1); SCHED(); if (RPACE) { __builtin_amdgcn_s_sleep(RPACE); SCHED(); } }
  };
  auto feed_gemm2 = [&](const v4u (&R)[35]) {
    STAMP(6);
#pragma unroll 1
    for (int i = 0; i < 5; ++i) {
      const int sl = 4 * i + w, qq = sl / 5;
      if (gemm_wg) await<2>(E_DONE + (sl < 6 ? 7 : sl < 12 ? 8 : 9) * 4, 4);
      dma5_lin_sc1(wss, lane * 16, wq + (14 * qq - (qq == 3) + sl % 5) * 5120, sl * 5120);
    }
    WAITV(0);
    signal(E_W0, lane);
    STAMP(7);
    await<2>(E_CONV, 4);
    dma5_lin_sc1(wss, lane * 16, wr + 5 * 5120, SLOT_HI + w * 10240);
    dma5_lin_sc1(wss, lane * 16, wr + 6 * 5120, SLOT_HI + w * 10240 + 5120);
    WAITV(0);
    signal(E_W1, lane);
    STAMP(8);
    PRIO(3);
    const unsigned lo = w * 25600 + lane * 16, hi = SLOT_HI + w * 10240 + lane * 16;
    await<1>(E_QD + w * 4, 2);
#pragma unroll
    for (int i = 0; i < 10; ++i) sts128(lo + i * 1024, R[i]);
    signal(E_QF + w * 4, lane);
    await<1>(E_QD + 16 + w * 4, 2);
#pragma unroll
    for (int i = 10; i < 20; ++i) sts128(lo + i * 1024, R[i]);
    signal(E_QF + 16 + w * 4, lane);
    await<1>(E_QD + 32 + w * 4, 2);
#pragma unroll
    for (int i = 20; i < 25; ++i) sts128(lo + i * 1024, R[i]);
#pragma unroll
    for (int i = 25; i < 30; ++i) sts128(hi + (i - 25) * 1024, R[i]);
    await<1>(E_QD + 48 + w * 4, 2);
#pragma unroll
    for (int i = 30; i < 35; ++i) sts128(hi + (i - 25) * 1024, R[i]);
    signal(E_QF + 32 + w * 4, lane);
    STAMP(9);
    if (w == 0) { poll_words(ws, WS_F3 + (ct * 32 + (lane & 31)) * 128, lds_tag()); signal(E_F3OK, lane); } else { await<1>(E_F3OK, 1); }
    v4u sv[3];
#pragma unroll
    for (int i = 0; i < 3; ++i)
      sv[i] = __builtin_amdgcn_raw_buffer_load_b128(ws, (lane >> 3) * 4096 + (lane & 7) * 16, WS_ST + (80 * ct + 8 * min(w + 4 * i, 9)) * 4096 + tt * 128, SC1);
#pragma unroll
    for (int i = 0; i < 3; ++i) sts128(STILE + (8 * min(w + 4 * i, 9) + (lane >> 3)) * 160 + (lane & 7) * 16, sv[i]);
    signal(E_SRDY, lane);
    STAMP(10);
    STAMP_FLUSH();
  };

  if (role == 1) {
    const int token = 8 * tb + 2 * w + (lane >> 5);
    const int fi = 4 * xcd + slot - 28, blk = w < 2 ? 2 * tb + w : (!gemm_wg && w == 2 && fi < 28) ? 512 + fi : -1;
    PRIO(3);
    LnIn ln = ln_load(xr, token, lane);
    const unsigned ev = __builtin_amdgcn_raw_buffer_load_b32(ws, WS_EPOCH, 0, SC1);
    BAR();
    const unsigned tag = epoch_tag(ev, w == 0, lane);
    v4u blkv[4];
    const rsrc_t w2r = make_rsrc(p.w2, blk < 0 ? 0u : W_BYTES);
    asm volatile("" ::"v"(ln.v[0]), "v"(ln.v[1]), "v"(ln.v[2]), "v"(ln.v[3]), "v"(ln.v[4]) : "memory");
#pragma unroll
    for (int j = 0; j < 4; ++j)
      blkv[j] = __builtin_amdgcn_raw_buffer_load_b128(w2r, (lane >> 3) * 1280 + (lane & 7) * 16, (max(blk, 0) / 10) * 40960 + (max(blk, 0) % 10) * 128 + j * 10240, 0);
    SCHED();
    STAMP(1);
    const f2v stat = ln_compute(ln, 2 * w + (lane >> 5), lane);
    group_sync<0>(E_LN, 4, lane);
    v4u xt[3];
    ln_transpose(xt, w, lane);
    group_sync<0>(E_RT, 4, lane);
    if (blk >= 0) retile(blkv, w < 2 ? LNST + w * 4096 : 98304u, lane);
    if ((lane & 31) == 0) __builtin_amdgcn_raw_buffer_store_b128((v4u){f2u(stat.x), tag, f2u(stat.y), tag}, ws, WS_STAT + token * 16, 0, SC1);
    ln_publish(ws, xt, tb, w, lane);
    if (blk >= 0) retile_store(ws, blk, blkv, lane);
    STAMP(11);
    PRIO(0);
    publish(ws, C_F1, 4, WS_F1 + tb * 128, tag, lane);
    STAMP(12);
    if (gemm_wg) {
      await<SLL>(E_DONE + FALL_AT * 4, 4);
      STAMP(2);
      if (w == 0) {
        poll_words(ws, WS_FALL + xcd * 128, tag);
        signal(E_F1OK, lane);
        if (blockIdx.x == 0 && lane == 0) __builtin_amdgcn_raw_buffer_store_b32(lds32(W_TAG + 4) + 1u, ws, WS_EPOCH, 0, SC1);
      } else {
        await<1>(E_F1OK, 1);
      }
      STAMP(5);
      v4u R[35];
      w2_regs(R, 0, 35);
      feed_gemm2(R);
      asm volatile(".Lfused_end_a:");
      return;
    }
  }
  if (!gemm_wg || role == 2) {
    const bool last_pair = role == (gemm_wg ? 2 : 1);
    const unsigned grp = last_pair ? GRP : (role >> 1) * (4 * KSCR + 4 * FPOS), cnt = C_GRP + role * 48;
    if (role == 1) kin = kg_load(p, dg + (w >> 1), lane);
    if (gemm_wg) {
      if (w < 2) WAITV(9); else WAITV(8);
      signal(E_FULL + 4, lane);
      WAITV(0); signal(E_FULL + 8, lane);
    }
    if (wave == 11) { asm volatile("" ::"v"(kin.lA), "v"(kin.Ai), "v"(kin.Cr), "v"(kin.Ci), "v"(kin.ldt), "v"(kin.dsk)); touch(make_srd(p.w1, W_BYTES), 192 * 3456 + (blockIdx.x * 64 + lane) * 128); }
    float isc = ssm_prepare(ws, kin, grp, last_pair ? LNST : grp + 4 * KSCR, cnt, dg, w, lane,
      [&] { if (last_pair) await(C_F1, 4); },
      [&] {
        STAMP(2);
        if (role == 0) {
          await(E_LN, 4);
          const unsigned tag = lds_tag();
          poll_words(ws, WS_F1 + (64 * w + lane) * 128, tag);
          if (lds_arrive(C_AX, lane) == 3) {
            if (slot == 28 && lane == 0) __builtin_amdgcn_raw_buffer_store_b32(tag, ws, WS_FALL + xcd * 128, 0, SC1);
            signal(E_F1OK, lane);
          }
          await<1>(E_F1OK, 1);
        } else {
          await<SLA>(E_F1OK, 1);
          if (gemm_wg) await<SLA>(U_AT, 4);
        }
        STAMP(3);
      });
    STAMP(role == 2 ? 6 : 16);
    if (role != 1) {
      unsigned cb = grp + (w >> 1) * CH_BYTES;
      int d = dg + (w >> 1);
#pragma unroll 1
      for (int last = gemm_wg; last < 2; ++last) {
        conv_channel(ws, cb, d, w & 1, isc, lane, [&] { if (gemm_wg) await(E_GELU, 4); if (role) STAMP(7); }, [&] { signal(last ? E_CONV : E_R1, lane); });
        if (role == 0) break;
        if (!last) {
          await(C_GRP + 48 + 12, 4);
          cb = GRP + (w >> 1) * CH_BYTES;
          d -= 2;
          isc = u2f(peek(W_ISC + (w >> 1) * 4));
        }
      }
      if (role == 0) { WAITV(0); signal(E_SDR, lane); STAMP(5); } else { STAMP(8); }
    } else {
      STAMP(5);
      v4u R[35];
      w2_regs(R, 0, 35);
      await<2>(E_R1, 8);
      feed_gemm2(R);
      return;
    }
  } else if (cp == 13 && (w & 1)) {
    WAITV(0); signal(E_FULL, lane);
#pragma unroll 1
    for (int i = 0; i < 10; ++i) signal(E_DONE + i * 4, lane);
    stage_x(3);
#pragma unroll 1
    for (int i = 3; i < 10; ++i) {
      await<1>(E_DONE + (i - 3) * 4, 4);
      stage_w(i);
      if (i < 9) { stage_x(i + 1); WAITV(4); } else { WAITV(0); }
      signal(E_FULL + i * 4, lane);
    }
    signal(E_VMQ, lane);
    await(E_LN, 4);
    const unsigned tag = lds_tag();
    signal(E_GELU, lane);
    publish(ws, C_H, 4, WS_F2 + (rp * 16 + cp) * 128, tag, lane);
  } else {
    const int wf = w & 1, wt = w >> 1, h = lane >> 5;
    PRIO(2);
    unsigned aa[2][2], ba[2][4];
    {
      const int q = (lane & 15) >> 2, pp = lane & 3, fb = (lane >> 4) & 1;
#pragma unroll
      for (int mi = 0; mi < 2; ++mi) {
        aa[0][mi] = (8 * h + q) * 256 + 16 * ((8 * wf + 4 * (mi ^ wt) + 2 * fb + (pp >> 1)) ^ (q << 2)) + 8 * (pp & 1);
        aa[1][mi] = aa[0][mi] + 4 * UNIT;
        asm volatile("" : "+v"(aa[0][mi]), "+v"(aa[1][mi]));
      }
#pragma unroll
      for (int s = 0; s < 4; ++s) {
        ba[0][s] = (64 * wt + (lane & 31)) * 128 + 16 * ((2 * s + h) ^ ((lane >> 1) & 7));
        ba[1][s] = ba[0][s] + 4 * UNIT;
        asm volatile("" : "+v"(ba[0][s]), "+v"(ba[1][s]));
      }
    }
    f16v acc[2][2] = {};
    v4u A[3][2], B[3][2], st[2];
    unsigned b1v[2], fl = 0, dn = 0, sbase = sig_base(lane), one = 1u;
    asm volatile("" : "+v"(sbase), "+v"(one));
    f4v cs4 = {};
    const unsigned sel1 = (((lane >> 2) ^ (lane >> 4)) & 1) ? 0u : 0x3f803f80u;
    const v4u sel = {sel1, sel1, sel1, sel1};
    auto rdA = [&](int n, int mi) {
      const int u = (2 * (n >> 2) + 1) % 7;
      const unsigned a = aa[u >> 2][mi] + (u & 3) * UNIT + (n & 3) * 4096;
      A[n % 3][mi] = ldstr2(a, a + 1024);
    };
    auto rdB = [&](int n, int ni) {
      const int u = (2 * (n >> 2)) % 7;
      B[n % 3][ni] = lds128(ba[u >> 2][n & 3] + (u & 3) * UNIT + ni * 4096);
    };
    const v4u xs = make_srd(p.x, X_BYTES), w1s = make_srd(p.w1, W_BYTES);
    const unsigned vx0 = lane_x(), vxo = vx0 ^ 64, vw0 = lane_w();
    unsigned sx[4], sw[4], mb = w * 4096;
#pragma unroll
    for (int j = 0; j < 4; ++j) {
      sx[j] = sx0 + (3 + rot) * 128 + j * 9216;
      sw[j] = sw0 + (3 + rot) * 64 * 3456 + j * 12800;
      asm volatile("" : "+s"(sx[j]), "+s"(sw[j]));
    }
    auto piece = [&](auto qc) {
      constexpr int q = qc, i = (q + 8) / 8 + 2 + ((q >> 2) & 1), j = q & 3;
      if constexpr (q >= -4 && q < 52) {
        if constexpr (q & 4) {
          if constexpr (j == 0) dma1(xs, vx0, sx[0], mb + ((2 * i) % 7) * UNIT);
          else dma_next<j * 1024>(xs, (j & 1) ? vxo : vx0, sx[j]);
          sx[j] += i + rot == 9 ? -6 * 128 : 128;
          asm volatile("" : "+s"(sx[j]));
        } else {
          if constexpr (j == 0) dma1(w1s, vw0, sw[0], mb + ((2 * i + 1) % 7) * UNIT);
          else dma_next<j * 1024>(w1s, vw0, sw[j]);
          sw[j] += i + rot == 9 ? -6 * 64 * 3456 : 64 * 3456;
          asm volatile("" : "+s"(sw[j]));
        }
      }
    };
    auto load_stats = [&] {
#pragma unroll
      for (int i = 0; i < 2; ++i) st[i] = __builtin_amdgcn_raw_buffer_load_b128(ws, (lane & 31) * 16, WS_STAT + (t0 + 64 * wt + 32 * i) * 16, SC1);
    };
    WAITV(0);
    group_sync(E_FULL, 4, lane);
    STAMP(2); STAMPC(11);
    rdA(0, 0); rdB(0, 0); rdB(0, 1); rdA(0, 1); rdA(1, 0); rdB(1, 0); rdB(1, 1); rdA(1, 1);
    SCHED();
    unsigned tag = 0;
    v4u eb[2];
    static_for<40>([&](auto nc) {
      constexpr int n = nc, sg = n >> 2, r = n & 3;
      acc[0][0] = mfma32b(A[n % 3][0], B[n % 3][0], acc[0][0]);
      SCHED();
      if (n < 38) { rdA(n + 2, 0); rdB(n + 2, 0); }
      SCHED();
      acc[0][1] = mfma32b(A[n % 3][0], B[n % 3][1], acc[0][1]);
      SCHED();
      piece(IC<2 * (n - 3)>{});
      if (r == 2 && sg < 7) dn = *(volatile LAS unsigned*)lp(sbase + (E_DONE - CNT) + sg * 4);
      if (r == 1 && sg < 9) fl = *(volatile LAS unsigned*)lp(sbase + (E_FULL - CNT) + (sg + 1) * 4);
      if (n == 34) { dn = *(volatile LAS unsigned*)lp(sbase + (E_LN - CNT)); tag = *(volatile LAS unsigned*)lp(sbase + (W_TAG - CNT)); }
      SCHED();
      cs4 = mfma16b(sel, A[n % 3][0], cs4);
      SCHED();
      acc[1][0] = mfma32b(A[n % 3][1], B[n % 3][0], acc[1][0]);
      SCHED();
      if (n < 38) { rdB(n + 2, 1); rdA(n + 2, 1); }
      if (r == 1) signal_at<E_DONE - CNT + sg * 4>(sbase, one);
      if (n == 39) { sts32(SEXC + w * 128 + (lane & 31) * 4, f2u(cs4[0])); signal(E_SX + w * 4, lane); }
      SCHED();
      acc[1][1] = mfma32b(A[n % 3][1], B[n % 3][1], acc[1][1]);
      SCHED();
      piece(IC<2 * (n - 3) + 1>{});
      if (r == 1 && sg < 9) { check(fl, E_FULL + (sg + 1) * 4, 4); if (sg & 1) STAMPL(16 + (sg >> 1)); }
      if (r == 2 && sg < 7) check(dn, E_DONE + sg * 4, 4);
      if (r == 0 && sg >= 2 && sg < 9) {
        if (sg < 8) WAITV(8); else WAITV(4);
        signal_at<E_FULL - CNT + (sg + 1) * 4>(sbase, one);
      }
      if (n == 29) load_stats();
      if (n == 30) {
#pragma unroll
        for (int i = 0; i < 2; ++i)
          b1v[i] = __builtin_amdgcn_raw_buffer_load_b16(make_rsrc(p.b1, F_ * 2), (lane & 31) * 2, (f0 + 64 * wf + 32 * (i ^ wt)) * 2, 0);
        signal_at<E_VMQ - CNT>(sbase, one);
      }
      if (n == 35) {
        if (__builtin_expect(__builtin_amdgcn_readfirstlane(dn) < 4, 0)) { await(E_LN, 4); tag = peek(W_TAG); }
        tag = __builtin_amdgcn_readfirstlane(tag);
      }
      if (n == 37) {
        if (__builtin_expect(!__all(st[0].y == tag && st[0].w == tag && st[1].y == tag && st[1].w == tag), 0)) {
#pragma unroll 1
          for (unsigned s = 0; s < SPIN_CAP; ++s) {
            asm volatile("" ::: "memory");
            load_stats();
            if (__all(st[0].y == tag && st[0].w == tag && st[1].y == tag && st[1].w == tag)) break;
          }
        }
      }
      if (n == 37 || n == 38) {
        constexpr int i = n == 38;
        const float mu = -u2f(st[i].x), ir = u2f(st[i].z), mh = bfr(mu), ih = bfr(ir);
        eb[i] = h ? (v4u){0u, 0u, 0u, 0u} : (v4u){pkbf(mh, opq(mu - mh)), pkbf(mh, 0.f), pkbf(ih, opq(ir - ih)), 0u};
        asm volatile("" : "+v"(eb[i]));
      }
      SCHED();
    });
    STAMP(3); STAMPC(12);
    auto ext = [&](int mi, float S) {
      const float sh = bfr(S);
      const v4u ea = h ? (v4u){0u, 0u, 0u, 0u} : (v4u){pkbf(sh, sh), pkbf(S - sh, 0.f), b1v[mi] | (b1v[mi] << 16), 0u};
      acc[mi][0] = mfma32b(ea, eb[0], acc[mi][0]);
      acc[mi][1] = mfma32b(ea, eb[1], acc[mi][1]);
    };
    fl = peek(E_SX + (w ^ 2) * 4);
    float so = u2f(peek(SEXC + (w ^ 2) * 128 + (lane & 31) * 4));
    ext(0, cs4[0]);
    SCHED();
    if (__builtin_expect(__builtin_amdgcn_readfirstlane(fl) < 1, 0)) {
      await(E_SX + (w ^ 2) * 4, 1);
      so = u2f(peek(SEXC + (w ^ 2) * 128 + (lane & 31) * 4));
    }
    ext(1, so);
    STAMP(1);
    {
      const unsigned hv = ((lane >> 4) & 1) * 55296 + h * 256 + (lane & 15) * 16;
#pragma unroll
      for (int mi = 0; mi < 2; ++mi) {
#pragma unroll
        for (int ni = 0; ni < 2; ++ni) {
          const float ir = u2f(st[ni].z), rho = __builtin_amdgcn_rcpf(ir);
          const float k1 = -2.302208198f * rho, k2 = k1 * 0.044715f * rho * rho;
          const unsigned hs = WS_H + ((t0 + 64 * wt + 32 * ni) / 16) * 55296 + ((f0 + 64 * wf) / 32 + (mi ^ wt)) * 1024;
#pragma unroll
          for (int hf = 0; hf < 2; ++hf) {
            unsigned o[4];
#pragma unroll
            for (int r = 0; r < 4; ++r) {
              const f2v a = {acc[mi][ni][8 * hf + 2 * r], acc[mi][ni][8 * hf + 2 * r + 1]};
              const f2v x = a * (a * a * k2 + k1);
              const f2v e = {__builtin_amdgcn_exp2f(x.x), __builtin_amdgcn_exp2f(x.y)};
              const f2v dd = e * ir + ir;
              const f2v rr = {__builtin_amdgcn_rcpf(dd.x), __builtin_amdgcn_rcpf(dd.y)};
              const f2v gl = a * rr;
              o[r] = pkbf(gl.x, gl.y);
            }
            __builtin_amdgcn_raw_buffer_store_b128((v4u){o[0], o[1], o[2], o[3]}, ws, hv + hs + hf * 512, 0, SC1);
            SCHED();
          }
        }
      }
    }
    signal(E_GELU, lane);
    STAMP(4); STAMPC(13);
    publish(ws, C_H, 4, WS_F2 + (rp * 16 + cp) * 128, tag, lane);
    STAMP(5);
  }

  {
    const int mh = w & 1, q = role + (w >> 1);
    if (wave == 0) {
      PRIO(0);
      poll_words(ws, WS_F2 + ((tt >> 1) * 16 + min(lane, 13)) * 128, lds_tag());
      signal(E_F2OK, lane);
    } else {
      await<1>(E_F2OK, 1);
    }
    if (role == 0) STAMP(6);
    PRIO(3);
    v4u H[7][2], A[2][5];
    f4v acc[2][5] = {};
    const unsigned hs = WS_H + (4 * tt + 2 * mh) * 55296 + (14 * q - (q == 3)) * 1024;
    const unsigned hv = lane * 16;
    auto ldh = [&](int k, int ti) { H[k % 7][ti] = __builtin_amdgcn_raw_buffer_load_b128(ws, hv, hs + ti * 55296 + k * 1024, SC1); SCHED(); };
#pragma unroll
    for (int k = 0; k < 3; ++k) { ldh(k, 0); ldh(k, 1); }
    if (role == 2) { if (!gemm_wg) await(E_SDR, 4); publish(ws, C_GRP + 96 + 36, 4, WS_F3 + tb * 128, lds_tag(), lane); STAMP(9); }
    const int fo = q < 2 ? 3 * q : 2 + 2 * q;
    v2u xv[3], bv[3];
    unsigned alo = q * 25600 + lane * 16, ahi = SLOT_HI + q * 10240 + lane * 16, fl = 0, sq = sig_base(lane) + q * 4, one = 1u;
    asm volatile("" : "+v"(alo), "+v"(ahi), "+v"(sq), "+v"(one));
    auto rd = [&](int k, int n) { A[k & 1][n] = lds128((k % 7 < 5 ? alo + (k % 7) * 5120 : ahi + (k % 7 - 5) * 5120) + n * 1024); };
    await<1>(E_W0, 4);
#pragma unroll
    for (int n = 0; n < 5; ++n) rd(0, n);
    if (role == 0) { STAMP(7); STAMPC(14); }
    SCHED();
    auto step = [&](auto kc) {
      constexpr int k = kc;
      if (k == 3 || k == 7 || k == 10) PRIO(k == 3 ? 2 : k == 7 ? 1 : 0);
#pragma unroll
      for (int ti = 0; ti < 2; ++ti) {
#pragma unroll
        for (int n = 0; n < 5; ++n) {
          acc[ti][n] = mfma16b(A[k & 1][n], H[k % 7][ti], acc[ti][n]);
          SCHED();
          if (ti == 0 && k < 13) {
            rd(k + 1, n);
            if (n == 4 && k == 0) signal_at<E_QD - CNT>(sq, one);
            if (n == 4 && k == 2) signal_at<E_QD - CNT + 16>(sq, one);
            if (n == 4 && k == 4) signal_at<E_QD - CNT + 32>(sq, one);
            if (n == 4 && k == 5) signal_at<E_QD - CNT + 48>(sq, one);
            if (n == 4 && k == 9) signal_at<E_QD - CNT + 64>(sq, one);
            SCHED();
          }
          if (ti == 1 && n == 0) {
            if (k == 3) fl = peek(E_W1);
            if (k == 5) fl = *(volatile LAS unsigned*)lp(sq + (E_QF - CNT));
            if (k == 7) fl = *(volatile LAS unsigned*)lp(sq + (E_QF - CNT) + 16);
            if (k == 9) fl = *(volatile LAS unsigned*)lp(sq + (E_QF - CNT) + 32);
            if (k == 12) fl = *(volatile LAS unsigned*)lp(sq + (E_QD - CNT) + 64);
            SCHED();
          }
        }
        if (k < 2) { ldh(3 + 2 * k + ti, 0); ldh(3 + 2 * k + ti, 1); }
        if (k >= 1 && k < 8) ldh(k + 6, ti);
        if constexpr (k >= 8 && k < 11) {
          const int f = min(fo + k - 8, 9), tl = f >= 5, n = f - 5 * tl;
          const int l1 = fresh_lane();
          if (ti == 0) xv[k - 8] = __builtin_amdgcn_raw_buffer_load_b64(xr, (l1 & 15) * 1280 + 8 * (l1 >> 4), (64 * tt + 32 * mh + 16 * tl) * 1280 + 160 * ct + 32 * n, 0);
          else bv[k - 8] = __builtin_amdgcn_raw_buffer_load_b64(make_rsrc(p.b2, D_ * 2), 8 * (l1 >> 4), 160 * ct + 32 * n, 0);
        }
        SCHED();
      }
      if (k == 3) check(fl, E_W1, 4);
      if (k == 5) check(fl, E_QF + q * 4, 1);
      if (k == 7) check(fl, E_QF + 16 + q * 4, 1);
      if (k == 9) check(fl, E_QF + 32 + q * 4, 1);
      if (role == 0 && (k & 1)) STAMPL(22 + k / 2);
      SCHED();
    };
    static_for<13>(step);
    if (role == 0) step(IC<13>{});
    if (role == 0) { STAMP(8); STAMPC(15); } else { STAMP(4); }
    check(fl, E_QD + 64 + q * 4, 2);
#pragma unroll
    for (int f = 0; f < 10; ++f) sts128(alo + mh * 10240 + f * 1024, __builtin_bit_cast(v4u, acc[f / 5][f % 5]));
    fl = peek(E_SRDY);
    const int l2 = fresh_lane(), g2 = l2 >> 4;
    group_sync<0>(E_RED + mh * 4, 4, l2);
    check(fl, E_SRDY, 4);
    if (role == 0) STAMP(9);
    auto finish = [&](int i) {
      const int f = fo + i, tl = f >= 5, n = f - 5 * tl;
      const unsigned ra = mh * 10240 + f * 1024 + l2 * 16;
      const f4v p0 = __builtin_bit_cast(f4v, lds128(ra)), p1 = __builtin_bit_cast(f4v, lds128(ra + 25600));
      const f4v p2 = __builtin_bit_cast(f4v, lds128(ra + 51200)), p3 = __builtin_bit_cast(f4v, lds128(ra + 76800));
      const v2u sv = ldstr(STILE + (16 * n + (l2 >> 2)) * 160 + (32 * mh + 16 * tl + 4 * (l2 & 3)) * 2);
      const float bz[4] = {blo(bv[i].x), bhi(bv[i].x), blo(bv[i].y), bhi(bv[i].y)};
      const float sz[4] = {blo(sv.x), bhi(sv.x), blo(sv.y), bhi(sv.y)};
      const float xz[4] = {blo(xv[i].x), bhi(xv[i].x), blo(xv[i].y), bhi(xv[i].y)};
      float r[4];
#pragma unroll
      for (int j = 0; j < 4; ++j) {
        const float z = (p0[j] + p1[j]) + (p2[j] + p3[j]) + bz[j];
        const float gt = z * __builtin_amdgcn_rcpf(1.f + __builtin_amdgcn_exp2f(z * -1.44269504088896f));
        r[j] = fmaf(sz[j], gt, xz[j]);
      }
      __builtin_amdgcn_raw_buffer_store_b64((v2u){pkbf(r[0], r[1]), pkbf(r[2], r[3])}, make_rsrc(p.out, X_BYTES), (l2 & 15) * 1280 + 8 * g2,
                                            (64 * tt + 32 * mh + 16 * tl) * 1280 + 160 * ct + 32 * n, OUT_AUX);
    };
    finish(0); finish(1);
    if (role == 0) finish(2);
    STAMP(10);
    asm volatile(".Lfused_end_b:");
  }
  STAMP_FLUSH();
}

}

extern "C" void kernel_launch(void* const* d_in, const int* in_sizes, int n_in, void* d_out, int out_size, void* d_ws, size_t ws_size,
                              hipStream_t stream) {
  Params p;
  p.x = (const unsigned short*)d_in[0];
  p.lA = (const unsigned short*)d_in[1];
  p.Ai = (const unsigned short*)d_in[2];
  p.Cr = (const unsigned short*)d_in[3];
  p.Ci = (const unsigned short*)d_in[4];
  p.ldt = (const unsigned short*)d_in[5];
  p.Dsk = (const unsigned short*)d_in[6];
  p.w1 = (const unsigned short*)d_in[8];
  p.b1 = (const unsigned short*)d_in[9];
  p.w2 = (const unsigned short*)d_in[10];
  p.b2 = (const unsigned short*)d_in[11];
  p.out = (unsigned short*)d_out;
  p.ws = (unsigned char*)d_ws;
  (void)hipFuncSetAttribute((const void*)fused_kernel, hipFuncAttributeMaxDynamicSharedMemorySize, LDS_BYTES);
  fused_kernel<<<256, 768, LDS_BYTES, stream>>>(p);
}
```
